```python
import torch

def _softmax(x, axis=-1):
    return torch.softmax(x, dim=axis)
def _gelu(x):
    return torch.nn.functional.gelu(x, approximate="tanh")
def _silu(x):
    return torch.nn.functional.silu(x)
def _sigmoid(x):
    return torch.sigmoid(x)
def _layernorm(x, axis=-1, eps=1e-5):
    m = x.mean(dim=axis, keepdim=True)
    v = x.var(dim=axis, keepdim=True, unbiased=False)
    return (x - m) / torch.sqrt(v + eps)
def _scatter_add(tgt, idx, src, axis=0):
    return tgt.clone().index_add_(axis, idx, src)
def _segment_sum(src, seg, n):
    out = torch.zeros((n,) + src.shape[1:], dtype=src.dtype)
    return out.index_add_(0, seg, src)
def _pad(x, pw):
    flat = tuple(v for pair in reversed(pw) for v in pair)
    return torch.nn.functional.pad(x, flat)

kernel_name = 'sequential_preLN__softmax_full_mha__dense_activation_mish__prompt'

def setup_inputs():

    _w = (lambda a: (lambda t: t.to(torch.bfloat16) if t.is_floating_point() else t)(torch.as_tensor(a)))
    def _A_setup():
        B, S, D = 1, 2048, 512
        return {
            "x": _w((lambda *s: torch.randn(*s, dtype=torch.bfloat16) * 0.05)(B, S, D)),
            "wqkv": _w((lambda *s: torch.randn(*s, dtype=torch.bfloat16) * 0.05)(D, 3 * D)),
            "wo": _w((lambda *s: torch.randn(*s, dtype=torch.bfloat16) * 0.05)(D, D)),
        }
    _A_in = _A_setup()

    def _B_setup():
        B, S, D, F = 1, 2048, 512, 1792
        return {
            "x": _w((lambda *s: torch.randn(*s, dtype=torch.bfloat16) * 0.05)(B, S, D)),
            "w1": _w((lambda *s: torch.randn(*s, dtype=torch.bfloat16) * 0.8)(D, F) * (D ** -0.5)),
            "b1": _w((lambda *s: torch.randn(*s, dtype=torch.bfloat16) * 0.4)(F)),
            "w2": _w((lambda *s: torch.randn(*s, dtype=torch.bfloat16) * 0.2)(F, D) * (F ** -0.5)),
        }
    _B_in = _B_setup()

    return {
        "A_x": _A_in["x"],
        "A_wqkv": _A_in["wqkv"],
        "A_wo": _A_in["wo"],
        "B_x": _B_in["x"],
        "B_w1": _B_in["w1"],
        "B_b1": _B_in["b1"],
        "B_w2": _B_in["w2"],
    }

def reference(A_x, A_wqkv, A_wo, B_x, B_w1, B_b1, B_w2):
    def _A_ref(x, wqkv, wo):
        B, S, D = x.shape
        H = 32; Dh = D // H
        qkv = (lambda x, *d: x.permute(*d))((x @ wqkv).reshape(B, S, 3, H, Dh), 2, 0, 3, 1, 4)
        q, k, v = qkv[0], qkv[1], qkv[2]
        scale = Dh ** -0.5
        scores = torch.einsum("bhqd,bhkd->bhqk", q, k) * scale
        p = _softmax(scores, axis=-1)
        o = torch.einsum("bhqk,bhkd->bhqd", p, v)
        return (lambda x, *d: x.permute(*d))(o, 0, 2, 1, 3).reshape(B, S, D) @ wo

    def _B_ref(x, w1, b1, w2):
        h = torch.einsum("bsd,df->bsf", x, w1) + b1
        sp = torch.log1p(torch.exp(-torch.abs(h))) + torch.where(h > 0, h, torch.zeros_like(h))
        act = h * torch.tanh(sp)
        return torch.einsum("bsf,fd->bsd", act, w2)

    x = A_x
    h = x + _A_ref(_layernorm(x, axis=-1), A_wqkv, A_wo)
    h2 = h + _B_ref(_layernorm(h, axis=-1), B_w1, B_b1, B_w2)
    return h2
```

```cpp
#include <hip/hip_runtime.h>
#include <hip/hip_bf16.h>
#include <type_traits>

typedef unsigned short u16;
using bf16x8 = __attribute__((ext_vector_type(8))) short;
using s16x4 = __attribute__((ext_vector_type(4))) short;
using f32x16 = __attribute__((ext_vector_type(16))) float;
using f32x4 = __attribute__((ext_vector_type(4))) float;
using f32x2 = __attribute__((ext_vector_type(2))) float;
using u32x4 = __attribute__((ext_vector_type(4))) unsigned;
using u32x2 = __attribute__((ext_vector_type(2))) unsigned;
using bf16x2_t = __attribute__((ext_vector_type(2))) __bf16;
#define DI __device__ __forceinline__
#define LAS __attribute__((address_space(3)))
#define MFMA(a, b, c) __builtin_amdgcn_mfma_f32_32x32x16_bf16((a), (b), (c), 0, 0, 0)

constexpr int S = 2048, D = 512, NH = 32, F = 1792;
constexpr float LN_EPS = 1e-5f;
constexpr float QSCALE = 0.25f * 1.4426950408889634f;
constexpr float BOUND_SAFE = 40.f;

constexpr size_t WS_Q = 0, WS_K = 2u << 20, WS_V = 4u << 20, WS_O = 6u << 20, WS_HF = 8u << 20, WS_HR = 10u << 20, WS_ACT = 12u << 20,
                 WS_STAT = 20u << 20, WS_KMAX = (20u << 20) + (256u << 10);

template <int N> using IC = std::integral_constant<int, N>;
template <int B, int E, class Fn> DI void sfor(Fn&& f) { if constexpr (B < E) { f(IC<B>{}); sfor<B + 1, E>(f); } }

DI int rfl(int v) { return __builtin_amdgcn_readfirstlane(v); }
DI unsigned cvtpk(float lo, float hi) { const f32x2 v = {lo, hi}; return __builtin_bit_cast(unsigned, __builtin_convertvector(v, bf16x2_t)); }
DI float bf_lo(unsigned u) { return __uint_as_float(u << 16); }
DI float bf_hi(unsigned u) { return __uint_as_float(u & 0xffff0000u); }
DI float dot2(unsigned a, unsigned b, float c) {
  return __builtin_amdgcn_fdot2_f32_bf16(__builtin_bit_cast(bf16x2_t, a), __builtin_bit_cast(bf16x2_t, b), c, false);
}
DI void dma16(const void* sbase, unsigned voff, unsigned lds) {
  asm volatile("s_mov_b32 m0, %2\n\ts_nop 0\n\tglobal_load_lds_dwordx4 %0, %1" ::"v"(voff), "s"(sbase), "s"(lds) : "memory");
}
template <int IMM> DI void gload16(bf16x8& dst, const void* sbase, unsigned voff) {
  asm volatile("global_load_dwordx4 %0, %1, %2 offset:%3" : "=v"(dst) : "v"(voff), "s"(sbase), "i"(IMM) : "memory");
}
template <int N> DI void wait_bar() { asm volatile("s_waitcnt vmcnt(%0) lgkmcnt(0)\n\ts_barrier" ::"i"(N) : "memory"); }
DI void lds_bar() { asm volatile("s_waitcnt lgkmcnt(0)\n\ts_barrier" ::: "memory"); }
DI s16x4 trread(const LAS char* p) {
  typedef short v4i16_t __attribute__((ext_vector_type(4)));
  return __builtin_bit_cast(s16x4, __builtin_amdgcn_ds_read_tr16_b64_v4i16((LAS v4i16_t*)p));
}
DI bf16x8 cat8(s16x4 a, s16x4 b) { return __builtin_shufflevector(a, b, 0, 1, 2, 3, 4, 5, 6, 7); }
DI bf16x8 pack8(float a0, float a1, float a2, float a3, float a4, float a5, float a6, float a7) {
  u32x4 w = {cvtpk(a0, a1), cvtpk(a2, a3), cvtpk(a4, a5), cvtpk(a6, a7)};
  return __builtin_bit_cast(bf16x8, w);
}
DI float xor32(float v) { return __shfl_xor(v, 32); }

constexpr int K1_SLOT = 32768, K1_NSLOT = 4, K1_TAB = K1_SLOT * K1_NSLOT;
constexpr int K1_LDS = K1_TAB + 64 * 8 + 4 * 192 * 4;
__global__ __launch_bounds__(512, 2) void k1_qkv(const u16* __restrict__ x, const u16* __restrict__ wqkv, char* __restrict__ ws) {
  extern __shared__ __attribute__((aligned(16))) char lds_g[];
  LAS char* lds = (LAS char*)lds_g;
  const unsigned lds0 = (unsigned)(uintptr_t)lds_g;
  const int tid = threadIdx.x, l = tid & 63, w = rfl(tid >> 6);
  const int b = blockIdx.x, hg = b & 7, rb = b >> 3;
  const int m = w & 1, ng = (w >> 1) & 1, ks = w >> 2, hh = l >> 5;
  const unsigned xoff = (unsigned)(64 * rb + 8 * w + (l >> 3)) * 1024u + (unsigned)(((l & 7) ^ ((4 * w + (l >> 4)) & 7)) * 16);
  const unsigned woff = (unsigned)(8 * w + (l >> 3)) * 3072u + (unsigned)hg * 128u + (unsigned)(((l & 7) ^ (((l >> 4) & 1) << 2)) * 16);
  auto issue = [&](int st) {
    const unsigned dst = lds0 + (st & 3) * K1_SLOT + w * 1024;
    dma16((const char*)x + st * 128, xoff, dst);
    dma16((const char*)wqkv + (size_t)st * 64 * 3072, woff, dst + 8192);
    dma16((const char*)wqkv + (size_t)st * 64 * 3072 + 1024, woff, dst + 16384);
    dma16((const char*)wqkv + (size_t)st * 64 * 3072 + 2048, woff, dst + 24576);
  };
  issue(0); issue(1); issue(2); issue(3);
  f32x16 acc[3] = {};
  float rs = 0.f, rq = 0.f, cs[3] = {0.f, 0.f, 0.f};
  const unsigned ONES = 0x3F803F80u;
  const int swz = (l >> 1) & 7;
  const int xa0 = (32 * m + (l & 31)) * 128 + (((4 * ks + hh) ^ swz) * 16), xa1 = (32 * m + (l & 31)) * 128 + (((4 * ks + 2 + hh) ^ swz) * 16);
  const int q4 = (l & 15) >> 2, p4 = l & 3, blk = (l >> 4) & 1;
  const int wa = 8192 + (32 * ks + 8 * hh + q4) * 128 + (((4 * ng + 2 * blk + (p4 >> 1)) ^ (((q4 >> 1) & 1) << 2)) * 16) + (p4 & 1) * 8;
  sfor<0, 8>([&](auto ST) {
    constexpr int st = ST.value;
    constexpr int N = st == 0 ? 12 : st <= 5 ? 8 : st == 6 ? 4 : 0;
    wait_bar<N>();
    if constexpr (st >= 1 && st + 3 < 8) issue(st + 3);
    const LAS char* sl = lds + (st & 3) * K1_SLOT;
    { const u32x4 v = *(const LAS u32x4*)(sl + w * 1024 + l * 16);
      for (int i = 0; i < 4; ++i) { rs = dot2(v[i], ONES, rs); rq = dot2(v[i], v[i], rq); } }
#pragma unroll
    for (int i = 0; i < 2; ++i) {
      const bf16x8 xf = *(const LAS bf16x8*)(sl + (i ? xa1 : xa0));
      bf16x8 wf[3];
#pragma unroll
      for (int s = 0; s < 3; ++s) wf[s] = cat8(trread(sl + wa + s * 8192 + i * 2048), trread(sl + wa + s * 8192 + i * 2048 + 512));
      acc[0] = MFMA(wf[0], xf, acc[0]);
      acc[1] = MFMA(wf[1], xf, acc[1]);
      acc[2] = MFMA(xf, wf[2], acc[2]);
      if (i == m) {
#pragma unroll
        for (int s = 0; s < 3; ++s) { const u32x4 u = __builtin_bit_cast(u32x4, wf[s]); for (int j = 0; j < 4; ++j) cs[s] = dot2(u[j], ONES, cs[s]); }
      }
    }
  });
  LAS f32x2* tstat = (LAS f32x2*)(lds + K1_TAB);
  LAS float* tcs = (LAS float*)(lds + K1_TAB + 512);
  rs += __shfl_xor(rs, 1); rq += __shfl_xor(rq, 1); rs += __shfl_xor(rs, 2); rq += __shfl_xor(rq, 2); rs += __shfl_xor(rs, 4); rq += __shfl_xor(rq, 4);
  if ((l & 7) == 0) { const float mean = rs * (1.f / D); const float var = fmaxf(rq * (1.f / D) - mean * mean, 0.f); tstat[8 * w + (l >> 3)] = f32x2{mean, rsqrtf(var + LN_EPS)}; }
#pragma unroll
  for (int s = 0; s < 3; ++s) { cs[s] += xor32(cs[s]); if (l < 32) tcs[((m + 2 * ks) * 2 + ng) * 96 + s * 32 + l] = cs[s]; }
  lds_bar();
  LAS float* xch = (LAS float*)lds;
  if (ks == 0) { for (int s = 0; s < 3; ++s) for (int r = 0; r < 8; ++r) xch[((w * 3 + s) * 8 + r) * 64 + l] = acc[s][8 + r]; }
  else         { for (int s = 0; s < 3; ++s) for (int r = 0; r < 8; ++r) xch[((w * 3 + s) * 8 + r) * 64 + l] = acc[s][r]; }
  lds_bar();
  float val[3][8];
  { const int pw = w ^ 4;
    if (ks == 0) { for (int s = 0; s < 3; ++s) for (int r = 0; r < 8; ++r) val[s][r] = acc[s][r] + xch[((pw * 3 + s) * 8 + r) * 64 + l]; }
    else         { for (int s = 0; s < 3; ++s) for (int r = 0; r < 8; ++r) val[s][r] = acc[s][8 + r] + xch[((pw * 3 + s) * 8 + r) * 64 + l]; } }
  auto colsum = [&](int s, int n) { return tcs[(0 * 2 + ng) * 96 + s * 32 + n] + tcs[(1 * 2 + ng) * 96 + s * 32 + n] + tcs[(2 * 2 + ng) * 96 + s * 32 + n] + tcs[(3 * 2 + ng) * 96 + s * 32 + n]; };
  const int tile = 2 * rb + m;
  {
    const f32x2 st = tstat[32 * m + (l & 31)];
    const int head = 4 * hg + 2 * ng + ks;
    float q[8], k[8], n2 = 0.f;
#pragma unroll
    for (int j = 0; j < 8; ++j) { const int n = 16 * ks + 8 * (j >> 2) + 4 * hh + (j & 3);
      q[j] = (val[0][j] - st[0] * colsum(0, n)) * (st[1] * QSCALE);
      k[j] = (val[1][j] - st[0] * colsum(1, n)) * st[1]; n2 += k[j] * k[j]; }
    *(bf16x8*)(ws + WS_Q + ((size_t)(head * 64 + tile) * 64 + l) * 16) = pack8(q[0], q[1], q[2], q[3], q[4], q[5], q[6], q[7]);
    *(bf16x8*)(ws + WS_K + ((size_t)(head * 64 + tile) * 64 + l) * 16) = pack8(k[0], k[1], k[2], k[3], k[4], k[5], k[6], k[7]);
    n2 += xor32(n2);
    for (int o = 16; o >= 1; o >>= 1) n2 = fmaxf(n2, __shfl_xor(n2, o));
    if (l == 0) ((float*)(ws + WS_KMAX))[head * 64 + tile] = n2;
  }
  {
    const int n = l & 31, head = 4 * hg + 2 * ng + (n >> 4);
    const float c = colsum(2, n);
    float v[8];
#pragma unroll
    for (int j = 0; j < 8; ++j) { const f32x2 st = tstat[32 * m + 16 * ks + 8 * (j >> 2) + 4 * hh + (j & 3)]; v[j] = (val[2][j] - st[0] * c) * st[1]; }
    *(bf16x8*)(ws + WS_V + (size_t)(head * 64 + tile) * 1024 + ks * 512 + hh * 256 + (n & 15) * 16) = pack8(v[0], v[1], v[2], v[3], v[4], v[5], v[6], v[7]);
  }
}

constexpr int K2_K = 0, K2_V = 65536, K2_ONES = 131072, K2_LDS = K2_ONES + 4096 + 256;
__global__ __launch_bounds__(1024, 4) void k2_attn(char* __restrict__ ws) {
  extern __shared__ __attribute__((aligned(16))) char lds_g[];
  LAS char* lds = (LAS char*)lds_g;
  const unsigned lds0 = (unsigned)(uintptr_t)lds_g;
  const int tid = threadIdx.x, l = tid & 63, w = rfl(tid >> 6), hh = l >> 5;
  const int b = blockIdx.x, head = 4 * (b & 7) + ((b >> 3) & 3), qb = b >> 5;
  const int qt = w & 7, kh = w >> 3;
  const char* Kg = ws + WS_K + (size_t)head * 65536;
  const char* Vg = ws + WS_V + (size_t)head * 65536;
  bf16x8 qf;
  gload16<0>(qf, ws + WS_Q + (size_t)(head * 64 + qb * 8 + qt) * 1024, l * 16);
  float kmx;
  asm volatile("global_load_dword %0, %1, %2" : "=v"(kmx) : "v"(l * 4), "s"(ws + WS_KMAX + head * 256) : "memory");
#pragma unroll
  for (int c = 0; c < 4; ++c) { const int t = kh * 32 + c * 8 + qt;
    dma16(Kg + t * 1024, l * 16, lds0 + K2_K + t * 1024);
    dma16(Vg + t * 1024, l * 16, lds0 + K2_V + t * 1024); }
  for (int i = tid; i < (4096 + 256) / 4; i += 1024) ((LAS unsigned*)(lds + K2_ONES))[i] = 0x3F803F80u;
  asm volatile("s_waitcnt vmcnt(8)" : "+v"(qf), "+v"(kmx)::"memory");
  for (int o = 32; o >= 1; o >>= 1) kmx = fmaxf(kmx, __shfl_xor(kmx, o));
  float qn; { const u32x4 u = __builtin_bit_cast(u32x4, qf); qn = 0.f; for (int i = 0; i < 4; ++i) qn = dot2(u[i], u[i], qn); qn += xor32(qn); }
  const float bound = sqrtf(qn * kmx) * 1.01f + 1e-6f;
  const bool safe = __all(bound <= BOUND_SAFE);
  f32x16 o = {};
  float cref = bound;
  const int ka = K2_K + kh * 32768 + l * 16;
  const int lo5 = l & 31;
  const int va = lo5 < 16 ? K2_V + kh * 32768 + hh * 256 + lo5 * 16 : K2_ONES + ((lo5 >= 20 && lo5 < 28) ? 64 : 0);
  const int vstep = lo5 < 16 ? 4096 : 0;
  if (safe) {
    f32x16 negc; for (int i = 0; i < 16; ++i) negc[i] = -bound;
    int vcur = va;
    sfor<0, 4>([&](auto C) {
      constexpr int c = C.value;
      wait_bar<6 - 2 * c>();
#pragma unroll
      for (int g = 0; g < 2; ++g) {
#pragma unroll
        for (int t = 0; t < 4; ++t) {
          const bf16x8 kf = *(const LAS bf16x8*)(lds + ka + (c * 8 + g * 4 + t) * 1024);
          const bf16x8 v0 = *(const LAS bf16x8*)(lds + vcur + t * 1024), v1 = *(const LAS bf16x8*)(lds + vcur + t * 1024 + 512);
          f32x16 s = MFMA(kf, qf, negc);
#pragma unroll
          for (int i = 0; i < 16; ++i) s[i] = __builtin_amdgcn_exp2f(s[i]);
          o = MFMA(v0, pack8(s[0], s[1], s[2], s[3], s[4], s[5], s[6], s[7]), o);
          o = MFMA(v1, pack8(s[8], s[9], s[10], s[11], s[12], s[13], s[14], s[15]), o);
        }
        vcur += vstep;
      }
    });
  } else {
    float mrun = -3.0e38f;
    const f32x16 zero = {};
    int vcur = va;
    for (int c = 0; c < 4; ++c) {
      if (c == 0) wait_bar<6>(); else if (c == 1) wait_bar<4>(); else if (c == 2) wait_bar<2>(); else wait_bar<0>();
      for (int g = 0; g < 2; ++g) {
        for (int t = 0; t < 4; ++t) {
          const bf16x8 kf = *(const LAS bf16x8*)(lds + ka + (c * 8 + g * 4 + t) * 1024);
          const bf16x8 v0 = *(const LAS bf16x8*)(lds + vcur + t * 1024), v1 = *(const LAS bf16x8*)(lds + vcur + t * 1024 + 512);
          f32x16 s = MFMA(kf, qf, zero);
          float tm = s[0];
#pragma unroll
          for (int i = 1; i < 16; ++i) tm = fmaxf(tm, s[i]);
          tm = fmaxf(tm, xor32(tm));
          const float mn = fmaxf(mrun, tm), f = __builtin_amdgcn_exp2f(mrun - mn);
          mrun = mn;
#pragma unroll
          for (int i = 0; i < 16; ++i) { o[i] *= f; s[i] = __builtin_amdgcn_exp2f(s[i] - mn); }
          o = MFMA(v0, pack8(s[0], s[1], s[2], s[3], s[4], s[5], s[6], s[7]), o);
          o = MFMA(v1, pack8(s[8], s[9], s[10], s[11], s[12], s[13], s[14], s[15]), o);
        }
        vcur += vstep;
      }
    }
    cref = mrun;
  }
  lds_bar();
  LAS float* xch = (LAS float*)lds;
  if (kh == 1) { for (int r = 0; r < 9; ++r) xch[(qt * 10 + r) * 64 + l] = o[r]; xch[(qt * 10 + 9) * 64 + l] = cref; }
  lds_bar();
  if (kh == 0) {
    const float c1 = xch[(qt * 10 + 9) * 64 + l], cm = fmaxf(cref, c1);
    const float f0 = __builtin_amdgcn_exp2f(cref - cm), f1 = __builtin_amdgcn_exp2f(c1 - cm);
    float r[9];
    for (int i = 0; i < 9; ++i) r[i] = o[i] * f0 + xch[(qt * 10 + i) * 64 + l] * f1;
    const float inv = 1.f / r[8];
    *(bf16x8*)(ws + WS_O + ((size_t)((qb * 8 + qt) * 32 + head) * 64 + l) * 16) =
        pack8(r[0] * inv, r[1] * inv, r[2] * inv, r[3] * inv, r[4] * inv, r[5] * inv, r[6] * inv, r[7] * inv);
  }
}

template <int KQ, int NSLOT> struct GR {
  static constexpr int NST = KQ / 2;
  static constexpr int cmin(int a, int b) { return a < b ? a : b; }
  static constexpr int cmax(int a, int b) { return a > b ? a : b; }
  static constexpr int waitN(int st) {
    if (st < NSLOT) return 4 * (NSLOT - 1 - st) + 2 * (NST - NSLOT) + 2 * cmax(0, cmin(st - 1, NST - NSLOT));
    return 2 * (cmin(NST - 1, st + NSLOT - 2) - st);
  }
};
constexpr int GR_PSTRIDE = 272, GR_PSZ = 64 * GR_PSTRIDE;
template <int KQ, int NSLOT, bool FINAL>
__global__ __launch_bounds__(512, 2) void k_gemm_res(const char* __restrict__ actf, const u16* __restrict__ W, const u16* __restrict__ res  ,
                                                     char* __restrict__ ws, u16* __restrict__ out) {
  using G = GR<KQ, NSLOT>;
  constexpr int NST = G::NST;
  extern __shared__ __attribute__((aligned(16))) char lds_g[];
  LAS char* lds = (LAS char*)lds_g;
  const unsigned lds0 = (unsigned)(uintptr_t)lds_g;
  const int tid = threadIdx.x, l = tid & 63, w = rfl(tid >> 6), hh = l >> 5;
  const int b = blockIdx.x, rb = (b & 7) * 4 + ((b >> 3) & 3), cb = b >> 5;
  const int m = w & 1, kq = w >> 1;
  bf16x8 resv;
  gload16<0>(resv, (const char*)res + (size_t)rb * 64 * 1024 + cb * 128, (unsigned)(tid >> 3) * 1024u + (tid & 7) * 16);
  const unsigned woff = (unsigned)(kq * KQ * 16 + 16 * m + (l >> 3)) * 1024u + (unsigned)cb * 128u + (unsigned)(((l & 7) ^ (((l >> 4) & 1) << 2)) * 16);
  const char* abase = actf + ((size_t)(2 * rb + m) * (KQ * 4) + kq * KQ) * 1024;
  bf16x8 af[KQ];
  auto issueW = [&](int st) {
    const unsigned dst = lds0 + (st % NSLOT) * 16384 + kq * 4096 + m * 2048;
    dma16((const char*)W + (size_t)st * 32 * 1024, woff, dst);
    dma16((const char*)W + (size_t)st * 32 * 1024 + 8 * 1024, woff, dst + 1024);
  };
  sfor<0, NST>([&](auto ST) {
    constexpr int st = ST.value;
    if constexpr (st < NSLOT) issueW(st);
    gload16<((2 * st) & 3) * 1024>(af[2 * st], abase + ((2 * st) >> 2) * 4096, l * 16);
    gload16<((2 * st + 1) & 3) * 1024>(af[2 * st + 1], abase + ((2 * st + 1) >> 2) * 4096, l * 16);
  });
  f32x16 acc[2] = {};
  const int q4 = (l & 15) >> 2, p4 = l & 3, blk = (l >> 4) & 1;
  const int wa0 = kq * 4096 + (4 * hh + q4) * 128 + (((2 * blk + (p4 >> 1)) ^ (((q4 >> 1) & 1) << 2)) * 16) + (p4 & 1) * 8;
  const int wa1 = wa0 ^ 64;
  sfor<0, NST>([&](auto ST) {
    constexpr int st = ST.value;
    asm volatile("s_waitcnt vmcnt(%2) lgkmcnt(0)\n\ts_barrier" : "+v"(af[2 * st]), "+v"(af[2 * st + 1]) : "i"(G::waitN(st)) : "memory");
    if constexpr (st >= 1 && st - 1 + NSLOT < NST) issueW(st - 1 + NSLOT);
    const LAS char* sl = lds + (st % NSLOT) * 16384;
#pragma unroll
    for (int i = 0; i < 2; ++i) {
      const bf16x8 w0 = cat8(trread(sl + wa0 + i * 2048), trread(sl + wa0 + i * 2048 + 1024));
      const bf16x8 w1 = cat8(trread(sl + wa1 + i * 2048), trread(sl + wa1 + i * 2048 + 1024));
      acc[0] = MFMA(w0, af[2 * st + i], acc[0]);
      acc[1] = MFMA(w1, af[2 * st + i], acc[1]);
    }
  });
  lds_bar();
#pragma unroll
  for (int ct = 0; ct < 2; ++ct)
#pragma unroll
    for (int g = 0; g < 4; ++g)
      *(LAS f32x4*)(lds + kq * GR_PSZ + (32 * m + (l & 31)) * GR_PSTRIDE + (32 * ct + 8 * g + 4 * hh) * 4) =
          f32x4{acc[ct][4 * g], acc[ct][4 * g + 1], acc[ct][4 * g + 2], acc[ct][4 * g + 3]};
  lds_bar();
  const int row = tid >> 3, c8 = tid & 7;
  f32x4 s0 = {}, s1 = {};
#pragma unroll
  for (int q = 0; q < 4; ++q) { s0 += *(const LAS f32x4*)(lds + q * GR_PSZ + row * GR_PSTRIDE + c8 * 32); s1 += *(const LAS f32x4*)(lds + q * GR_PSZ + row * GR_PSTRIDE + c8 * 32 + 16); }
  asm volatile("" : "+v"(resv));
  const u32x4 ru = __builtin_bit_cast(u32x4, resv);
  const u32x4 o = {cvtpk(s0[0] + bf_lo(ru[0]), s0[1] + bf_hi(ru[0])), cvtpk(s0[2] + bf_lo(ru[1]), s0[3] + bf_hi(ru[1])),
                   cvtpk(s1[0] + bf_lo(ru[2]), s1[1] + bf_hi(ru[2])), cvtpk(s1[2] + bf_lo(ru[3]), s1[3] + bf_hi(ru[3]))};
  const int grow = rb * 64 + row;
  if constexpr (FINAL) {
    *(u32x4*)((char*)out + (size_t)grow * 1024 + cb * 128 + c8 * 16) = o;
  } else {
    *(u32x4*)(ws + WS_HR + (size_t)grow * 1024 + cb * 128 + c8 * 16) = o;
    char* hf = ws + WS_HF + ((size_t)(grow >> 5) * 32 + cb * 4 + (c8 >> 1)) * 1024 + (grow & 31) * 16 + (c8 & 1) * 8;
    *(u32x2*)hf = u32x2{o[0], o[1]};
    *(u32x2*)(hf + 512) = u32x2{o[2], o[3]};
    float sm = 0.f, sq = 0.f;
    for (int i = 0; i < 4; ++i) { sm = dot2(o[i], 0x3F803F80u, sm); sq = dot2(o[i], o[i], sq); }
    sm += __shfl_xor(sm, 1); sq += __shfl_xor(sq, 1); sm += __shfl_xor(sm, 2); sq += __shfl_xor(sq, 2); sm += __shfl_xor(sm, 4); sq += __shfl_xor(sq, 4);
    if (c8 == 0) *(f32x2*)(ws + WS_STAT + ((size_t)grow * 8 + cb) * 8) = f32x2{sm, sq};
  }
}

constexpr int K4_TAB = 131072, K4_LDS = K4_TAB + 2 * 128 * 4;
__global__ __launch_bounds__(512, 2) void k4_ffn1(const u16* __restrict__ w1, const u16* __restrict__ b1, char* __restrict__ ws) {
  extern __shared__ __attribute__((aligned(16))) char lds_g[];
  LAS char* lds = (LAS char*)lds_g;
  const unsigned lds0 = (unsigned)(uintptr_t)lds_g;
  const int tid = threadIdx.x, l = tid & 63, w = rfl(tid >> 6), hh = l >> 5;
  const int b = blockIdx.x, rb = 2 * (b & 7) + ((b >> 3) & 1), cb = b >> 4;
  const int m = w & 3, kh = w >> 2;
  const int rt = 4 * rb + m;
  bf16x8 st4[4];
  gload16<0>(st4[0], ws + WS_STAT + (size_t)rt * 32 * 64, (l & 31) * 64);
  gload16<16>(st4[1], ws + WS_STAT + (size_t)rt * 32 * 64, (l & 31) * 64);
  gload16<32>(st4[2], ws + WS_STAT + (size_t)rt * 32 * 64, (l & 31) * 64);
  gload16<48>(st4[3], ws + WS_STAT + (size_t)rt * 32 * 64, (l & 31) * 64);
  const unsigned woff = (unsigned)(256 * kh + 16 * m + (l >> 3)) * (F * 2) + (unsigned)cb * 256u + (unsigned)(((l & 7) ^ (((l >> 4) & 1) << 2)) * 16);
  const char* abase = ws + WS_HF + ((size_t)rt * 32 + kh * 16) * 1024;
  bf16x8 af[16];
  sfor<0, 4>([&](auto I) {
    constexpr int i = I.value;
    const unsigned dst = lds0 + (256 * kh + 64 * i + 16 * m) * 128;
    const char* src = (const char*)w1 + (size_t)i * 64 * (F * 2);
    dma16(src, woff, dst);
    dma16(src + 8 * (F * 2), woff, dst + 1024);
    dma16(src + 128, woff, dst + 65536);
    dma16(src + 8 * (F * 2) + 128, woff, dst + 65536 + 1024);
    gload16<0>(af[4 * i], abase + i * 4096, l * 16);
    gload16<1024>(af[4 * i + 1], abase + i * 4096, l * 16);
    gload16<2048>(af[4 * i + 2], abase + i * 4096, l * 16);
    gload16<3072>(af[4 * i + 3], abase + i * 4096, l * 16);
  });
  f32x16 acc[4] = {};
  float cs = 0.f;
  const int q4 = (l & 15) >> 2, p4 = l & 3, blk = (l >> 4) & 1;
  const int wa0 = (256 * kh + 4 * hh + q4) * 128 + (((2 * blk + (p4 >> 1)) ^ (((q4 >> 1) & 1) << 2)) * 16) + (p4 & 1) * 8;
  const int wa1 = wa0 ^ 64;
  sfor<0, 4>([&](auto I) {
    constexpr int i = I.value;
    asm volatile("s_waitcnt vmcnt(%4) lgkmcnt(0)\n\ts_barrier" : "+v"(af[4 * i]), "+v"(af[4 * i + 1]), "+v"(af[4 * i + 2]), "+v"(af[4 * i + 3]) : "i"(8 * (3 - i)) : "memory");
#pragma unroll
    for (int s = 0; s < 4; ++s) {
      const int o = (64 * i + 16 * s) * 128;
      bf16x8 wf[4];
      wf[0] = cat8(trread(lds + wa0 + o), trread(lds + wa0 + o + 1024));
      wf[1] = cat8(trread(lds + wa1 + o), trread(lds + wa1 + o + 1024));
      wf[2] = cat8(trread(lds + wa0 + o + 65536), trread(lds + wa0 + o + 65536 + 1024));
      wf[3] = cat8(trread(lds + wa1 + o + 65536), trread(lds + wa1 + o + 65536 + 1024));
#pragma unroll
      for (int ct = 0; ct < 4; ++ct) acc[ct] = MFMA(wf[ct], af[4 * i + s], acc[ct]);
      const bf16x8 mine = m == 0 ? wf[0] : m == 1 ? wf[1] : m == 2 ? wf[2] : wf[3];
      const u32x4 u = __builtin_bit_cast(u32x4, mine);
#pragma unroll
      for (int j = 0; j < 4; ++j) cs = dot2(u[j], 0x3F803F80u, cs);
    }
  });
  LAS float* tcs = (LAS float*)(lds + K4_TAB);
  cs += xor32(cs);
  if (l < 32) tcs[kh * 128 + m * 32 + l] = cs;
  lds_bar();
  LAS float* xch = (LAS float*)lds;
  if (kh == 0) { for (int c = 0; c < 4; ++c) for (int r = 0; r < 8; ++r) xch[((w * 4 + c) * 8 + r) * 64 + l] = acc[c][8 + r]; }
  else         { for (int c = 0; c < 4; ++c) for (int r = 0; r < 8; ++r) xch[((w * 4 + c) * 8 + r) * 64 + l] = acc[c][r]; }
  lds_bar();
  asm volatile("" : "+v"(st4[0]), "+v"(st4[1]), "+v"(st4[2]), "+v"(st4[3]));
  float sm = 0.f, sq = 0.f;
  for (int i = 0; i < 4; ++i) { const f32x4 t = __builtin_bit_cast(f32x4, st4[i]); sm += t[0] + t[2]; sq += t[1] + t[3]; }
  const float mean = sm * (1.f / D), rstd = rsqrtf(fmaxf(sq * (1.f / D) - mean * mean, 0.f) + LN_EPS);
  const int pw = w ^ 4;
#pragma unroll
  for (int c = 0; c < 4; ++c) {
    float a[8];
#pragma unroll
    for (int j = 0; j < 8; ++j) {
      const float v = (kh == 0 ? acc[c][j] : acc[c][8 + j]) + xch[((pw * 4 + c) * 8 + j) * 64 + l];
      const int n = 32 * c + 16 * kh + 8 * (j >> 2) + 4 * hh + (j & 3);
      const float bias = __uint_as_float((unsigned)b1[128 * cb + n] << 16);
      const float y = (v - mean * (tcs[n] + tcs[128 + n])) * rstd + bias;
      const float u = __builtin_amdgcn_exp2f(fminf(y, 30.f) * 1.4426950408889634f);
      const float nn = u * (u + 2.f);
      a[j] = y * nn * __builtin_amdgcn_rcpf(nn + 2.f);
    }
    *(bf16x8*)(ws + WS_ACT + ((size_t)rt * 112 + cb * 8 + c * 2 + kh) * 1024 + l * 16) = pack8(a[0], a[1], a[2], a[3], a[4], a[5], a[6], a[7]);
  }
}

extern "C" void kernel_launch(void* const* d_in, const int* in_sizes, int n_in, void* d_out, int out_size, void* d_ws, size_t ws_size, hipStream_t stream) {
  const u16* x = (const u16*)d_in[0];
  const u16* wqkv = (const u16*)d_in[1];
  const u16* wo = (const u16*)d_in[2];
  const u16* w1 = (const u16*)d_in[4];
  const u16* b1 = (const u16*)d_in[5];
  const u16* w2 = (const u16*)d_in[6];
  char* ws = (char*)d_ws;
  constexpr int L3 = 4 * GR_PSZ > 4 * 16384 ? 4 * GR_PSZ : 4 * 16384;
  constexpr int L5 = 9 * 16384;
  hipFuncSetAttribute((const void*)k1_qkv, hipFuncAttributeMaxDynamicSharedMemorySize, K1_LDS);
  hipFuncSetAttribute((const void*)k2_attn, hipFuncAttributeMaxDynamicSharedMemorySize, K2_LDS);
  hipFuncSetAttribute((const void*)k_gemm_res<8, 4, false>, hipFuncAttributeMaxDynamicSharedMemorySize, L3);
  hipFuncSetAttribute((const void*)k4_ffn1, hipFuncAttributeMaxDynamicSharedMemorySize, K4_LDS);
  hipFuncSetAttribute((const void*)k_gemm_res<28, 9, true>, hipFuncAttributeMaxDynamicSharedMemorySize, L5);
  k1_qkv<<<256, 512, K1_LDS, stream>>>(x, wqkv, ws);
  k2_attn<<<256, 1024, K2_LDS, stream>>>(ws);
  k_gemm_res<8, 4, false><<<256, 512, L3, stream>>>(ws + WS_O, wo, x, ws, nullptr);
  k4_ffn1<<<224, 512, K4_LDS, stream>>>(w1, b1, ws);
  k_gemm_res<28, 9, true><<<256, 512, L5, stream>>>(ws + WS_ACT, w2, (const u16*)(ws + WS_HR), ws, (u16*)d_out);
}
```

```cpp
#include <hip/hip_runtime.h>
#include <hip/hip_bf16.h>
#include <type_traits>
#define STAMP_DECL
#define STAMP_AT(i)
#define STAMP_OUT(k, n)
#define MEGA 1

typedef unsigned short u16;
using bf16x8 = __attribute__((ext_vector_type(8))) short;
using s16x4 = __attribute__((ext_vector_type(4))) short;
using f32x16 = __attribute__((ext_vector_type(16))) float;
using f32x4 = __attribute__((ext_vector_type(4))) float;
using f32x2 = __attribute__((ext_vector_type(2))) float;
using u32x4 = __attribute__((ext_vector_type(4))) unsigned;
using u32x2 = __attribute__((ext_vector_type(2))) unsigned;
using bf16x2_t = __attribute__((ext_vector_type(2))) __bf16;
#define DI __device__ __forceinline__
#define LAS __attribute__((address_space(3)))
#define GAS __attribute__((address_space(1)))
#define MFMA(a, b, c) __builtin_amdgcn_mfma_f32_32x32x16_bf16((a), (b), (c), 0, 0, 0)
#define SBAR() __builtin_amdgcn_sched_barrier(0)

constexpr int S = 2048, D = 512, NH = 32, F = 1792;
constexpr float LN_EPS = 1e-5f;
constexpr float LOG2E = 1.4426950408889634f;
constexpr float QSCALE = 0.25f * LOG2E;
constexpr float BOUND_SAFE = 40.f;
constexpr int LDS_BYTES = 163840;

constexpr unsigned WS_FLAG = 0;
constexpr unsigned FL_A = 4096, FL_B = FL_A + 256 * 64, FL_C = FL_B + 256 * 64, FL_D = FL_C + 256 * 64, FL_TMO = 64;
constexpr unsigned WS_KMAX = 128u << 10, WS_STAT = 256u << 10;
constexpr unsigned WS_Q = 1u << 20, WS_K = 3u << 20, WS_V = 5u << 20, WS_O = 7u << 20, WS_HF = 9u << 20, WS_ACT = 11u << 20;
constexpr unsigned WS_USED = 19u << 20;

struct Params { const u16 *x, *wqkv, *wo, *w1, *b1, *w2; char* ws; u16* out; };

template <int N> using IC = std::integral_constant<int, N>;
template <int B, int E, class Fn> DI void sfor(Fn&& f) { if constexpr (B < E) { f(IC<B>{}); sfor<B + 1, E>(f); } }

DI int rfl(int v) { return __builtin_amdgcn_readfirstlane(v); }
DI unsigned cvtpk(float lo, float hi) { const f32x2 v = {lo, hi}; return __builtin_bit_cast(unsigned, __builtin_convertvector(v, bf16x2_t)); }
DI float bf_lo(unsigned u) { return __uint_as_float(u << 16); }
DI float bf_hi(unsigned u) { return __uint_as_float(u & 0xffff0000u); }
DI float dot2(unsigned a, unsigned b, float c) {
  return __builtin_amdgcn_fdot2_f32_bf16(__builtin_bit_cast(bf16x2_t, a), __builtin_bit_cast(bf16x2_t, b), c, false);
}
constexpr unsigned ONES2 = 0x3F803F80u;
DI void dma16(const void* sbase, unsigned voff, unsigned lds) {
  asm volatile("s_mov_b32 m0, %2\n\ts_nop 0\n\tglobal_load_lds_dwordx4 %0, %1" ::"v"(voff), "s"(sbase), "s"(lds) : "memory");
}
template <int IMM, bool SC1> DI void gload16(bf16x8& dst, const void* sbase, unsigned voff) {
  if constexpr (SC1) asm volatile("global_load_dwordx4 %0, %1, %2 offset:%3 sc1" : "=v"(dst) : "v"(voff), "s"(sbase), "i"(IMM) : "memory");
  else asm volatile("global_load_dwordx4 %0, %1, %2 offset:%3" : "=v"(dst) : "v"(voff), "s"(sbase), "i"(IMM) : "memory");
}
template <int IMM> DI void gload8(u32x2& dst, const void* sbase, unsigned voff) {
  asm volatile("global_load_dwordx2 %0, %1, %2 offset:%3" : "=v"(dst) : "v"(voff), "s"(sbase), "i"(IMM) : "memory");
}
template <bool SC1> DI void gload4(float& dst, const void* sbase, unsigned voff) {
  if constexpr (SC1) asm volatile("global_load_dword %0, %1, %2 sc1" : "=v"(dst) : "v"(voff), "s"(sbase) : "memory");
  else asm volatile("global_load_dword %0, %1, %2" : "=v"(dst) : "v"(voff), "s"(sbase) : "memory");
}
typedef __attribute__((ext_vector_type(4))) unsigned v4u_;
typedef __attribute__((ext_vector_type(2))) unsigned v2u_;
struct WsBuf { __amdgpu_buffer_rsrc_t r; };
DI __amdgpu_buffer_rsrc_t make_rsrc(void* p, unsigned bytes) { return __builtin_amdgcn_make_buffer_rsrc(p, 0, bytes, 0x00020000); }
template <bool SC1> DI void st16(__amdgpu_buffer_rsrc_t r, unsigned off, bf16x8 v) { __builtin_amdgcn_raw_buffer_store_b128(__builtin_bit_cast(v4u_, v), r, off, 0, SC1 ? 16 : 0); }
template <bool SC1> DI void st8(__amdgpu_buffer_rsrc_t r, unsigned off, u32x2 v) { __builtin_amdgcn_raw_buffer_store_b64(v, r, off, 0, SC1 ? 16 : 0); }
template <bool SC1> DI void st4(__amdgpu_buffer_rsrc_t r, unsigned off, unsigned v) { __builtin_amdgcn_raw_buffer_store_b32(v, r, off, 0, SC1 ? 16 : 0); }

template <int N> DI void wait_bar() { asm volatile("s_waitcnt vmcnt(%0) lgkmcnt(0)\n\ts_barrier" ::"i"(N) : "memory"); }
DI void lds_bar() { asm volatile("s_waitcnt lgkmcnt(0)\n\ts_barrier" ::: "memory"); }
DI s16x4 trread(const LAS char* p) {
  typedef short v4i16_t __attribute__((ext_vector_type(4)));
  return __builtin_bit_cast(s16x4, __builtin_amdgcn_ds_read_tr16_b64_v4i16((LAS v4i16_t*)p));
}
DI bf16x8 cat8(s16x4 a, s16x4 b) { return __builtin_shufflevector(a, b, 0, 1, 2, 3, 4, 5, 6, 7); }
DI bf16x8 pack8(float a0, float a1, float a2, float a3, float a4, float a5, float a6, float a7) {
  u32x4 w = {cvtpk(a0, a1), cvtpk(a2, a3), cvtpk(a4, a5), cvtpk(a6, a7)};
  return __builtin_bit_cast(bf16x8, w);
}
template <int CTRL> DI float dpp(float v) { return __uint_as_float(__builtin_amdgcn_update_dpp(0u, __float_as_uint(v), CTRL, 0xf, 0xf, true)); }
DI float sum8(float v) { v += dpp<0xB1>(v); v += dpp<0x4E>(v); v += dpp<0x141>(v); return v; }
DI float sumx32(float v) { auto r = __builtin_amdgcn_permlane32_swap(__float_as_uint(v), __float_as_uint(v), false, false); return __uint_as_float(r[0]) + __uint_as_float(r[1]); }
DI float maxx32(float v) { auto r = __builtin_amdgcn_permlane32_swap(__float_as_uint(v), __float_as_uint(v), false, false); return fmaxf(__uint_as_float(r[0]), __uint_as_float(r[1])); }
DI float maxx16(float v) { auto r = __builtin_amdgcn_permlane16_swap(__float_as_uint(v), __float_as_uint(v), false, false); return fmaxf(__uint_as_float(r[0]), __uint_as_float(r[1])); }
DI float max16(float v) { v = fmaxf(v, dpp<0xB1>(v)); v = fmaxf(v, dpp<0x4E>(v)); v = fmaxf(v, dpp<0x141>(v)); v = fmaxf(v, dpp<0x140>(v)); return v; }

DI void publish(char* ws, unsigned flag_off, unsigned epoch) {
  asm volatile("s_waitcnt vmcnt(0) lgkmcnt(0)\n\ts_barrier" ::: "memory");
  if (threadIdx.x == 0) __hip_atomic_store((GAS unsigned*)(ws + flag_off), epoch, __ATOMIC_RELAXED, __HIP_MEMORY_SCOPE_AGENT);
}
DI void await(char* ws, unsigned flag_off, int n, unsigned epoch) {
  if (threadIdx.x < 64) {
    const int l = threadIdx.x;
    GAS unsigned* f = (GAS unsigned*)(ws + flag_off + (l < n ? l : 0) * 64);
    for (unsigned spins = 0; spins < (1u << 20); ++spins) {
      const unsigned v = __hip_atomic_load(f, __ATOMIC_RELAXED, __HIP_MEMORY_SCOPE_AGENT);
      if (__all(v == epoch)) break;
      __builtin_amdgcn_s_sleep(1);
    }
  }
  asm volatile("s_barrier" ::: "memory");
}

struct Ctx { LAS char* lds; unsigned lds0; int tid, l, w, hh, b; unsigned epoch; };

template <bool MG> DI void phaseA(const Params& p, Ctx& c) {
  STAMP_DECL STAMP_AT(0);
  char* ws = p.ws;
  LAS char* lds = c.lds;
  const int l = c.l, w = c.w, hh = c.hh, b = c.b;
  const int rb = 8 * ((b & 7) >> 1) + ((b >> 3) & 7), hg = 4 * (b & 1) + (b >> 6);
  const int m = w & 1, ng = (w >> 1) & 1, ks = w >> 2;
  const unsigned xoff = (unsigned)(64 * rb + 8 * w + (l >> 3)) * 1024u + (unsigned)(((l & 7) ^ ((4 * w + (l >> 4)) & 7)) * 16);
  const unsigned woff = (unsigned)(8 * w + (l >> 3)) * 3072u + (unsigned)hg * 128u + (unsigned)(((l & 7) ^ (((l >> 4) & 1) << 2)) * 16);
  auto issue = [&](int st) {
    const unsigned dst = c.lds0 + (st % 5) * 32768 + w * 1024;
    dma16((const char*)p.x + st * 128, xoff, dst);
    dma16((const char*)p.wqkv + (size_t)st * 64 * 3072, woff, dst + 8192);
    dma16((const char*)p.wqkv + (size_t)st * 64 * 3072 + 1024, woff, dst + 16384);
    dma16((const char*)p.wqkv + (size_t)st * 64 * 3072 + 2048, woff, dst + 24576);
  };
  issue(0); issue(1); issue(2); issue(3); issue(4);
  STAMP_AT(1);
  f32x16 acc[3] = {};
  float rs = 0.f, rq = 0.f, cs[3] = {0.f, 0.f, 0.f};
  const int swz = (l >> 1) & 7;
  const int xa0 = (32 * m + (l & 31)) * 128 + (((4 * ks + hh) ^ swz) * 16), xa1 = (32 * m + (l & 31)) * 128 + (((4 * ks + 2 + hh) ^ swz) * 16);
  const int q4 = (l & 15) >> 2, p4 = l & 3, blk = (l >> 4) & 1;
  const int wa = 8192 + (32 * ks + 8 * hh + q4) * 128 + (((4 * ng + 2 * blk + (p4 >> 1)) ^ (((q4 >> 1) & 1) << 2)) * 16) + (p4 & 1) * 8;
  sfor<0, 8>([&](auto ST) {
    constexpr int st = ST.value;
    constexpr int last = st == 0 ? 4 : (st + 3 > 7 ? 7 : (st + 3 < 4 ? 4 : st + 3));
    wait_bar<4 * (last - st)>();
    if constexpr (st == 0) STAMP_AT(2);
    if constexpr (st == 4) STAMP_AT(3);
    if constexpr (st >= 1 && st + 4 < 8) issue(st + 4);
    const LAS char* sl = lds + (st % 5) * 32768;
    const u32x4 sv = *(const LAS u32x4*)(sl + w * 1024 + l * 16);
#pragma unroll
    for (int i = 0; i < 2; ++i) {
      const bf16x8 xf = *(const LAS bf16x8*)(sl + (i ? xa1 : xa0));
      bf16x8 wf[3];
#pragma unroll
      for (int s = 0; s < 3; ++s) wf[s] = cat8(trread(sl + wa + s * 8192 + i * 2048), trread(sl + wa + s * 8192 + i * 2048 + 512));
      acc[0] = MFMA(wf[0], xf, acc[0]);
      acc[1] = MFMA(wf[1], xf, acc[1]);
      acc[2] = MFMA(xf, wf[2], acc[2]);
      if (i == m) {
#pragma unroll
        for (int s = 0; s < 3; ++s) { const u32x4 u = __builtin_bit_cast(u32x4, wf[s]); for (int j = 0; j < 4; ++j) cs[s] = dot2(u[j], ONES2, cs[s]); }
      }
    }
#pragma unroll
    for (int i = 0; i < 4; ++i) { rs = dot2(sv[i], ONES2, rs); rq = dot2(sv[i], sv[i], rq); }
  });
  STAMP_AT(4);
  if constexpr (MG) { asm volatile("" : "+v"(c.epoch)); c.epoch += 1u; }
  lds_bar();
  LAS float* tmean = (LAS float*)(lds + 98304);
  LAS float* trstd = tmean + 64;
  LAS float* tcs = tmean + 128;
  rs = sum8(rs); rq = sum8(rq);
  if ((l & 7) == 0) { const float mean = rs * (1.f / D); tmean[8 * w + (l >> 3)] = mean; trstd[8 * w + (l >> 3)] = rsqrtf(fmaxf(rq * (1.f / D) - mean * mean, 0.f) + LN_EPS); }
#pragma unroll
  for (int s = 0; s < 3; ++s) { const float t = sumx32(cs[s]); if (l < 32) tcs[(w * 3 + s) * 32 + l] = t; }
  lds_bar();
  const float mean_l = tmean[32 * m + (l & 31)], rstd_l = trstd[32 * m + (l & 31)];
#pragma unroll
  for (int s = 0; s < 2; ++s)
#pragma unroll
    for (int g = 0; g < 4; ++g) {
      const f32x4 ca = *(const LAS f32x4*)(tcs + (w * 3 + s) * 32 + 8 * g + 4 * hh), cb = *(const LAS f32x4*)(tcs + ((w ^ 1) * 3 + s) * 32 + 8 * g + 4 * hh);
#pragma unroll
      for (int j = 0; j < 4; ++j) acc[s][4 * g + j] -= mean_l * (ca[j] + cb[j]);
    }
  { const float cv = tcs[(w * 3 + 2) * 32 + (l & 31)] + tcs[((w ^ 1) * 3 + 2) * 32 + (l & 31)];
#pragma unroll
    for (int g = 0; g < 4; ++g) { const f32x4 mm = *(const LAS f32x4*)(tmean + 32 * m + 8 * g + 4 * hh);
#pragma unroll
      for (int j = 0; j < 4; ++j) acc[2][4 * g + j] -= mm[j] * cv; } }
  LAS f32x4* xch = (LAS f32x4*)lds;
  float val[3][8];
  if (ks == 0) {
#pragma unroll
    for (int s = 0; s < 3; ++s) for (int g = 0; g < 2; ++g) xch[(w * 6 + s * 2 + g) * 64 + l] = f32x4{acc[s][8 + 4 * g], acc[s][9 + 4 * g], acc[s][10 + 4 * g], acc[s][11 + 4 * g]};
#pragma unroll
    for (int s = 0; s < 3; ++s) for (int r = 0; r < 8; ++r) val[s][r] = acc[s][r];
  } else {
#pragma unroll
    for (int s = 0; s < 3; ++s) for (int g = 0; g < 2; ++g) xch[(w * 6 + s * 2 + g) * 64 + l] = f32x4{acc[s][4 * g], acc[s][1 + 4 * g], acc[s][2 + 4 * g], acc[s][3 + 4 * g]};
#pragma unroll
    for (int s = 0; s < 3; ++s) for (int r = 0; r < 8; ++r) val[s][r] = acc[s][8 + r];
  }
  lds_bar();
#pragma unroll
  for (int s = 0; s < 3; ++s) for (int g = 0; g < 2; ++g) { const f32x4 t = xch[((w ^ 4) * 6 + s * 2 + g) * 64 + l]; for (int j = 0; j < 4; ++j) val[s][4 * g + j] += t[j]; }
  const __amdgpu_buffer_rsrc_t rw = make_rsrc(ws, WS_USED);
  const int tile = 2 * rb + m;
  {
    const int head = 4 * hg + 2 * ng + ks;
    const float sq = rstd_l * QSCALE;
    float k[8], n2 = 0.f;
#pragma unroll
    for (int j = 0; j < 8; ++j) { k[j] = val[1][j] * rstd_l; n2 += k[j] * k[j]; }
    st16<MG>(rw, WS_Q + (unsigned)((head * 64 + tile) * 64 + l) * 16, pack8(val[0][0] * sq, val[0][1] * sq, val[0][2] * sq, val[0][3] * sq, val[0][4] * sq, val[0][5] * sq, val[0][6] * sq, val[0][7] * sq));
    st16<MG>(rw, WS_K + (unsigned)((head * 64 + tile) * 64 + l) * 16, pack8(k[0], k[1], k[2], k[3], k[4], k[5], k[6], k[7]));
    n2 = maxx16(max16(sumx32(n2)));
    if (l == 0) st4<MG>(rw, WS_KMAX + (unsigned)(head * 64 + tile) * 4, __float_as_uint(n2));
  }
  {
    const int n = l & 31, head = 4 * hg + 2 * ng + (n >> 4);
    const f32x4 r0 = *(const LAS f32x4*)(trstd + 32 * m + 16 * ks + 4 * hh), r1 = *(const LAS f32x4*)(trstd + 32 * m + 16 * ks + 8 + 4 * hh);
    st16<MG>(rw, WS_V + (unsigned)(head * 64 + tile) * 1024 + ks * 512 + hh * 256 + (n & 15) * 16,
             pack8(val[2][0] * r0[0], val[2][1] * r0[1], val[2][2] * r0[2], val[2][3] * r0[3], val[2][4] * r1[0], val[2][5] * r1[1], val[2][6] * r1[2], val[2][7] * r1[3]));
  }
  STAMP_AT(5);
  if constexpr (MG) publish(ws, FL_A + (hg * 32 + rb) * 64, c.epoch);
  STAMP_AT(6);
  STAMP_OUT(0, 7);
}

DI void prefetch_wo(const Params& p, const Ctx& c) {
  const int cb = c.b >> 5, l = c.l;
  const unsigned off = (unsigned)(64 * c.w + (l >> 3)) * 1024u + (unsigned)cb * 128u + (unsigned)(((l & 7) ^ (((l >> 4) & 1) << 2)) * 16);
#pragma unroll
  for (int i = 0; i < 8; ++i) dma16((const char*)p.wo + i * 8192, off, c.lds0 + c.w * 8192 + i * 1024);
}
DI unsigned w1_stage_base(int i) { return i < 3 ? 65536 + i * 32768 : 0; }
DI void prefetch_w1(const Params& p, const Ctx& c, int i) {
  const int cb = c.b >> 4, l = c.l, kh = c.w >> 2, m = c.w & 3;
  const unsigned off = (unsigned)(256 * kh + 16 * m + (l >> 3)) * (F * 2) + (unsigned)cb * 256u + (unsigned)(((l & 7) ^ (((l >> 4) & 1) << 2)) * 16);
  const char* src = (const char*)p.w1 + (size_t)i * 64 * (F * 2);
  const unsigned dst = c.lds0 + w1_stage_base(i) + kh * 16384 + m * 2048;
  dma16(src, off, dst);
  dma16(src + 8 * (F * 2), off, dst + 1024);
  dma16(src + 128, off, dst + 8192);
  dma16(src + 8 * (F * 2) + 128, off, dst + 8192 + 1024);
}
DI void prefetch_w2(const Params& p, const Ctx& c, int st, int slot) {
  const int cb = c.b >> 5, l = c.l, kq = c.w >> 1, m = c.w & 1;
  const unsigned off = (unsigned)(448 * kq + 16 * m + (l >> 3)) * 1024u + (unsigned)cb * 128u + (unsigned)(((l & 7) ^ (((l >> 4) & 1) << 2)) * 16);
  const unsigned dst = c.lds0 + slot * 16384 + kq * 4096 + m * 2048;
  dma16((const char*)p.w2 + (size_t)st * 32 * 1024, off, dst);
  dma16((const char*)p.w2 + (size_t)st * 32 * 1024 + 8 * 1024, off, dst + 1024);
}

constexpr int B_ONES = 131072;
template <bool MG> DI void phaseB(const Params& p, const Ctx& c) {
  STAMP_DECL STAMP_AT(0);
  char* ws = p.ws;
  LAS char* lds = c.lds;
  const int l = c.l, w = c.w, hh = c.hh, b = c.b, tid = c.tid;
  const int head = 4 * (b & 7) + ((b >> 3) & 3), qb = b >> 5;
  for (int i = tid; i < (8192 + 256) / 16; i += 512) ((LAS u32x4*)(lds + B_ONES))[i] = u32x4{ONES2, ONES2, ONES2, ONES2};
  if constexpr (MG) await(ws, FL_A + (head >> 2) * 32 * 64, 32, c.epoch);
  STAMP_AT(1);
  const char* Kg = ws + WS_K + (size_t)head * 65536;
  const char* Vg = ws + WS_V + (size_t)head * 65536;
  bf16x8 qf, kst[8], vst[8];
  float kmx;
  gload16<0, MG>(kst[0], Kg + w * 1024, l * 16);
  gload16<0, MG>(vst[0], Vg + w * 1024, l * 16);
  gload16<0, MG>(qf, ws + WS_Q + (size_t)(head * 64 + qb * 8 + w) * 1024, l * 16);
  gload4<MG>(kmx, ws + WS_KMAX + head * 256, l * 4);
  sfor<1, 8>([&](auto J) { constexpr int j = J.value;
    gload16<0, MG>(kst[j], Kg + (8 * j + w) * 1024, l * 16);
    gload16<0, MG>(vst[j], Vg + (8 * j + w) * 1024, l * 16); });
  asm volatile("s_waitcnt vmcnt(14)" : "+v"(kst[0]), "+v"(vst[0]), "+v"(qf), "+v"(kmx)::"memory");
  *(LAS bf16x8*)(lds + w * 2048 + l * 16) = kst[0];
  *(LAS bf16x8*)(lds + w * 2048 + 1024 + l * 16) = vst[0];
  kmx = maxx32(maxx16(max16(kmx)));
  float qn; { const u32x4 u = __builtin_bit_cast(u32x4, qf); qn = 0.f; for (int i = 0; i < 4; ++i) qn = dot2(u[i], u[i], qn); qn = sumx32(qn); }
  const float bound = __builtin_amdgcn_sqrtf(qn * kmx) * 1.01f + 1e-6f;
  const bool safe = __all(bound <= BOUND_SAFE);
  f32x16 o = {};
  const int ka = l * 16;
  const int lo5 = l & 31;
  const bool isv = lo5 < 16;
  int va = isv ? 1024 + hh * 256 + lo5 * 16 : B_ONES + ((lo5 >= 20 && lo5 < 28) ? 64 : 0);
  const int vstep = isv ? 8192 : 0;
  lds_bar();
  STAMP_AT(2);
  auto stage_rest = [&]() {
    asm volatile("s_waitcnt vmcnt(0)" : "+v"(kst[1]), "+v"(vst[1]), "+v"(kst[2]), "+v"(vst[2]), "+v"(kst[3]), "+v"(vst[3]), "+v"(kst[4]), "+v"(vst[4]),
                 "+v"(kst[5]), "+v"(vst[5]), "+v"(kst[6]), "+v"(vst[6]), "+v"(kst[7]), "+v"(vst[7])::"memory");
#pragma unroll
    for (int j = 1; j < 8; ++j) { *(LAS bf16x8*)(lds + (8 * j + w) * 2048 + l * 16) = kst[j]; *(LAS bf16x8*)(lds + (8 * j + w) * 2048 + 1024 + l * 16) = vst[j]; }
    lds_bar();
  };
  if (safe) {
    f32x16 negc; for (int i = 0; i < 16; ++i) negc[i] = -bound;
    f32x16 s0, s1;
    bf16x8 kf, v0, v1;
#define LDK(t) (*(const LAS bf16x8*)(lds + ka + (t) * 2048))
#define LDV(t4, h) (*(const LAS bf16x8*)(lds + va + (t4) * 2048 + (h) * 512))
#define EX4(X, i) do { X[i] = __builtin_amdgcn_exp2f(X[i]); X[i + 1] = __builtin_amdgcn_exp2f(X[i + 1]); X[i + 2] = __builtin_amdgcn_exp2f(X[i + 2]); X[i + 3] = __builtin_amdgcn_exp2f(X[i + 3]); } while (0)
#define STEP(SC, SN, t4, KN, LAST) do { \
      u32x4 pa, pb; \
      v0 = LDV(t4, 0); v1 = LDV(t4, 1); \
      if (!(LAST)) SN = MFMA(kf, qf, negc); \
      EX4(SC, 0); SBAR(); \
      if (!(LAST)) kf = (KN); \
      EX4(SC, 4); pa[0] = cvtpk(SC[0], SC[1]); pa[1] = cvtpk(SC[2], SC[3]); SBAR(); \
      EX4(SC, 8); pa[2] = cvtpk(SC[4], SC[5]); pa[3] = cvtpk(SC[6], SC[7]); SBAR(); \
      o = MFMA(v0, __builtin_bit_cast(bf16x8, pa), o); \
      EX4(SC, 12); pb[0] = cvtpk(SC[8], SC[9]); pb[1] = cvtpk(SC[10], SC[11]); SBAR(); \
      pb[2] = cvtpk(SC[12], SC[13]); pb[3] = cvtpk(SC[14], SC[15]); SBAR(); \
      o = MFMA(v1, __builtin_bit_cast(bf16x8, pb), o); SBAR(); \
    } while (0)
    auto group = [&](int tb, bool last) {
      const int kn = tb * 2048;
      STEP(s0, s1, 0, *(const LAS bf16x8*)(lds + ka + kn + 2 * 2048), false);
      STEP(s1, s0, 1, *(const LAS bf16x8*)(lds + ka + kn + 3 * 2048), false);
      STEP(s0, s1, 2, *(const LAS bf16x8*)(lds + ka + kn + 4 * 2048), false);
      if (last) STEP(s1, s0, 3, kf, true);
      else STEP(s1, s0, 3, *(const LAS bf16x8*)(lds + ka + kn + 5 * 2048), false);
      va += vstep;
    };
    kf = LDK(0); s0 = MFMA(kf, qf, negc); kf = LDK(1);
    group(0, false);
    STEP(s0, s1, 0, LDK(6), false); STEP(s1, s0, 1, LDK(7), false);
    stage_rest();
    STAMP_AT(3);
    STEP(s0, s1, 2, LDK(8), false); STEP(s1, s0, 3, LDK(9), false); va += vstep;
    for (int tb = 8; tb < 32; tb += 4) group(tb, false);
    if constexpr (MG) { lds_bar(); prefetch_wo(p, c); }
    for (int tb = 32; tb < 60; tb += 4) group(tb, false);
    { const int kn = 60 * 2048;
      STEP(s0, s1, 0, *(const LAS bf16x8*)(lds + ka + kn + 2 * 2048), false);
      STEP(s1, s0, 1, *(const LAS bf16x8*)(lds + ka + kn + 3 * 2048), false);
      STEP(s0, s1, 2, kf, false);
      STEP(s1, s0, 3, kf, true); }
  } else {
    float mrun = -3.0e38f;
    const f32x16 zero = {};
    for (int t = 0; t < 64; ++t) {
      if (t == 8) stage_rest();
      if (MG && t == 32) { lds_bar(); prefetch_wo(p, c); }
      const bf16x8 kf = *(const LAS bf16x8*)(lds + ka + t * 2048);
      const int vo = isv ? va + t * 2048 : va;
      const bf16x8 v0 = *(const LAS bf16x8*)(lds + vo), v1 = *(const LAS bf16x8*)(lds + vo + 512);
      f32x16 s = MFMA(kf, qf, zero);
      float tm = s[0];
#pragma unroll
      for (int i = 1; i < 16; ++i) tm = fmaxf(tm, s[i]);
      tm = maxx32(tm);
      const float mn = fmaxf(mrun, tm), f = __builtin_amdgcn_exp2f(mrun - mn);
      mrun = mn;
#pragma unroll
      for (int i = 0; i < 16; ++i) { o[i] *= f; s[i] = __builtin_amdgcn_exp2f(s[i] - mn); }
      o = MFMA(v0, pack8(s[0], s[1], s[2], s[3], s[4], s[5], s[6], s[7]), o);
      o = MFMA(v1, pack8(s[8], s[9], s[10], s[11], s[12], s[13], s[14], s[15]), o);
    }
  }
  STAMP_AT(4);
  const float inv = 1.f / o[8];
  const __amdgpu_buffer_rsrc_t rw = make_rsrc(ws, WS_USED);
  st16<MG>(rw, WS_O + (unsigned)(((qb * 8 + w) * 32 + head) * 64 + l) * 16, pack8(o[0] * inv, o[1] * inv, o[2] * inv, o[3] * inv, o[4] * inv, o[5] * inv, o[6] * inv, o[7] * inv));
  STAMP_AT(5);
  if constexpr (MG) {
    publish(ws, FL_B + (qb * 32 + head) * 64, c.epoch);
    if (b < 224) { prefetch_w1(p, c, 0); prefetch_w1(p, c, 1); prefetch_w1(p, c, 2); }
  }
  STAMP_AT(6);
  STAMP_OUT(1, 7);
}

DI void quarter_reduce(const Ctx& c, LAS f32x4* xch  , const f32x16 (&acc)[2], float (&val)[8]) {
  const int m = c.w & 1, kq = c.w >> 1, l = c.l;
#pragma unroll
  for (int d = 0; d < 4; ++d) {
    if (d == kq) continue;
    xch[(((d * 4 + kq) * 2 + m) * 2 + 0) * 64 + l] = f32x4{acc[d >> 1][8 * (d & 1)], acc[d >> 1][8 * (d & 1) + 1], acc[d >> 1][8 * (d & 1) + 2], acc[d >> 1][8 * (d & 1) + 3]};
    xch[(((d * 4 + kq) * 2 + m) * 2 + 1) * 64 + l] = f32x4{acc[d >> 1][8 * (d & 1) + 4], acc[d >> 1][8 * (d & 1) + 5], acc[d >> 1][8 * (d & 1) + 6], acc[d >> 1][8 * (d & 1) + 7]};
  }
#pragma unroll
  for (int d = 0; d < 4; ++d) if (d == kq) { for (int j = 0; j < 8; ++j) val[j] = acc[d >> 1][8 * (d & 1) + j]; }
  lds_bar();
#pragma unroll
  for (int s = 0; s < 4; ++s) {
    if (s == kq) continue;
    const f32x4 a = xch[(((kq * 4 + s) * 2 + m) * 2 + 0) * 64 + l], bb = xch[(((kq * 4 + s) * 2 + m) * 2 + 1) * 64 + l];
    for (int j = 0; j < 4; ++j) { val[j] += a[j]; val[4 + j] += bb[j]; }
  }
}

template <bool MG> DI void phaseC(const Params& p, const Ctx& c) {
  STAMP_DECL STAMP_AT(0);
  char* ws = p.ws;
  LAS char* lds = c.lds;
  const int l = c.l, w = c.w, hh = c.hh, b = c.b;
  const int rb = (b & 7) * 4 + ((b >> 3) & 3), cb = b >> 5;
  const int m = w & 1, kq = w >> 1;
  const int rt = 2 * rb + m, kstep = 4 * cb + kq;
  u32x2 xr0, xr1;
  gload8<0>(xr0, (const char*)p.x + (size_t)rt * 32 * 1024 + kstep * 32, (l & 31) * 1024 + hh * 8);
  gload8<16>(xr1, (const char*)p.x + (size_t)rt * 32 * 1024 + kstep * 32, (l & 31) * 1024 + hh * 8);
  if constexpr (!MG) prefetch_wo(p, c);
  if constexpr (MG) await(ws, FL_B + (rb >> 2) * 32 * 64, 32, c.epoch);
  STAMP_AT(1);
  const char* abase = ws + WS_O + ((size_t)rt * 32 + kq * 8) * 1024;
  bf16x8 af[8];
  sfor<0, 8>([&](auto I) { constexpr int i = I.value; gload16<(i & 3) * 1024, MG>(af[i], abase + (i >> 2) * 4096, l * 16); });
  f32x16 acc[2] = {};
  const int q4 = (l & 15) >> 2, p4 = l & 3, blk = (l >> 4) & 1;
  const int wa0 = (128 * kq + 4 * hh + q4) * 128 + (((2 * blk + (p4 >> 1)) ^ (((q4 >> 1) & 1) << 2)) * 16) + (p4 & 1) * 8;
  const int wa1 = wa0 ^ 64;
  asm volatile("s_waitcnt vmcnt(7)\n\ts_barrier" : "+v"(af[0]), "+v"(xr0), "+v"(xr1)::"memory");
  STAMP_AT(2);
  sfor<0, 8>([&](auto I) {
    constexpr int i = I.value;
    if constexpr (i > 0) asm volatile("s_waitcnt vmcnt(%1)" : "+v"(af[i]) : "i"(7 - i) : "memory");
    const bf16x8 w0 = cat8(trread(lds + wa0 + i * 2048), trread(lds + wa0 + i * 2048 + 1024));
    const bf16x8 w1 = cat8(trread(lds + wa1 + i * 2048), trread(lds + wa1 + i * 2048 + 1024));
    acc[0] = MFMA(w0, af[i], acc[0]);
    acc[1] = MFMA(w1, af[i], acc[1]);
  });
  STAMP_AT(3);
  lds_bar();
  float val[8];
  quarter_reduce(c, (LAS f32x4*)lds, acc, val);
  const u32x4 o = {cvtpk(val[0] + bf_lo(xr0[0]), val[1] + bf_hi(xr0[0])), cvtpk(val[2] + bf_lo(xr0[1]), val[3] + bf_hi(xr0[1])),
                   cvtpk(val[4] + bf_lo(xr1[0]), val[5] + bf_hi(xr1[0])), cvtpk(val[6] + bf_lo(xr1[1]), val[7] + bf_hi(xr1[1]))};
  const __amdgpu_buffer_rsrc_t rw = make_rsrc(ws, WS_USED);
  st16<MG>(rw, WS_HF + (unsigned)((rt * 32 + kstep) * 64 + l) * 16, __builtin_bit_cast(bf16x8, o));
  float sm = 0.f, sq = 0.f;
  for (int i = 0; i < 4; ++i) { sm = dot2(o[i], ONES2, sm); sq = dot2(o[i], o[i], sq); }
  sm = sumx32(sm); sq = sumx32(sq);
  if (l < 32) st8<MG>(rw, WS_STAT + (unsigned)((rt * 32 + l) * 32 + kstep) * 8, u32x2{__float_as_uint(sm), __float_as_uint(sq)});
  STAMP_AT(4);
  if constexpr (MG) {
    publish(ws, FL_C + ((rb >> 1) * 16 + (rb & 1) * 8 + cb) * 64, c.epoch);
    if (b < 224) prefetch_w1(p, c, 3);
  }
  STAMP_AT(5);
  STAMP_OUT(2, 6);
}

template <bool MG> DI void phaseD(const Params& p, const Ctx& c) {
  STAMP_DECL STAMP_AT(0);
  char* ws = p.ws;
  LAS char* lds = c.lds;
  const int l = c.l, w = c.w, hh = c.hh, b = c.b;
  const int rb = 2 * (b & 7) + ((b >> 3) & 1), cb = b >> 4;
  const int m = w & 3, kh = w >> 2;
  const int rt = 4 * rb + m;
  bf16x8 bv;
  gload16<0, false>(bv, (const char*)p.b1 + cb * 256, (l & 15) * 16);
  if constexpr (!MG) { prefetch_w1(p, c, 0); prefetch_w1(p, c, 1); prefetch_w1(p, c, 2); prefetch_w1(p, c, 3); }
  if constexpr (MG) await(ws, FL_C + rb * 16 * 64, 16, c.epoch);
  STAMP_AT(1);
  const char* abase = ws + WS_HF + ((size_t)rt * 32 + kh * 16) * 1024;
  bf16x8 af[16];
  sfor<0, 16>([&](auto I) { constexpr int i = I.value; gload16<(i & 3) * 1024, MG>(af[i], abase + (i >> 2) * 4096, l * 16); });
  bf16x8 st8v[8];
  sfor<0, 8>([&](auto I) { constexpr int i = I.value; gload16<i * 16, MG>(st8v[i], ws + WS_STAT + (size_t)rt * 32 * 256, (l & 31) * 256 + hh * 128); });
  f32x16 acc[4] = {};
  float cs = 0.f;
  const int q4 = (l & 15) >> 2, p4 = l & 3, blk = (l >> 4) & 1;
  const int wa0 = kh * 16384 + (4 * hh + q4) * 128 + (((2 * blk + (p4 >> 1)) ^ (((q4 >> 1) & 1) << 2)) * 16) + (p4 & 1) * 8;
  const int wa1 = wa0 ^ 64;
  asm volatile("s_waitcnt vmcnt(23)\n\ts_barrier" : "+v"(af[0]), "+v"(bv)::"memory");
  STAMP_AT(2);
  sfor<0, 16>([&](auto I) {
    constexpr int i = I.value;
    if constexpr (i > 0) asm volatile("s_waitcnt vmcnt(%1)" : "+v"(af[i]) : "i"(23 - i) : "memory");
    const int o = (int)w1_stage_base(i >> 2) + (i & 3) * 2048;
    bf16x8 wf[4];
    wf[0] = cat8(trread(lds + wa0 + o), trread(lds + wa0 + o + 1024));
    wf[1] = cat8(trread(lds + wa1 + o), trread(lds + wa1 + o + 1024));
    wf[2] = cat8(trread(lds + wa0 + o + 8192), trread(lds + wa0 + o + 8192 + 1024));
    wf[3] = cat8(trread(lds + wa1 + o + 8192), trread(lds + wa1 + o + 8192 + 1024));
#pragma unroll
    for (int ct = 0; ct < 4; ++ct) acc[ct] = MFMA(wf[ct], af[i], acc[ct]);
    const bf16x8 mine = m == 0 ? wf[0] : m == 1 ? wf[1] : m == 2 ? wf[2] : wf[3];
    const u32x4 u = __builtin_bit_cast(u32x4, mine);
#pragma unroll
    for (int j = 0; j < 4; ++j) cs = dot2(u[j], ONES2, cs);
  });
  STAMP_AT(3);
  LAS float* tcs = (LAS float*)(lds + 32768);
  LAS float* tb = tcs + 256;
  cs = sumx32(cs);
  if (l < 32) tcs[kh * 128 + m * 32 + l] = cs;
  if (w == 0 && l < 16) { const u32x4 u = __builtin_bit_cast(u32x4, bv);
    *(LAS f32x4*)(tb + l * 8) = f32x4{bf_lo(u[0]), bf_hi(u[0]), bf_lo(u[1]), bf_hi(u[1])}; *(LAS f32x4*)(tb + l * 8 + 4) = f32x4{bf_lo(u[2]), bf_hi(u[2]), bf_lo(u[3]), bf_hi(u[3])}; }
  asm volatile("s_waitcnt vmcnt(0)" : "+v"(st8v[0]), "+v"(st8v[1]), "+v"(st8v[2]), "+v"(st8v[3]), "+v"(st8v[4]), "+v"(st8v[5]), "+v"(st8v[6]), "+v"(st8v[7])::"memory");
  float sm = 0.f, sq = 0.f;
#pragma unroll
  for (int i = 0; i < 8; ++i) { const f32x4 t = __builtin_bit_cast(f32x4, st8v[i]); sm += t[0] + t[2]; sq += t[1] + t[3]; }
  sm = sumx32(sm); sq = sumx32(sq);
  const float mean = sm * (1.f / D), rstd = rsqrtf(fmaxf(sq * (1.f / D) - mean * mean, 0.f) + LN_EPS);
  lds_bar();
  LAS f32x4* xch = (LAS f32x4*)(lds + 65536);
#pragma unroll
  for (int ct = 0; ct < 4; ++ct)
#pragma unroll
    for (int g = 0; g < 4; ++g) { const f32x4 cv = *(const LAS f32x4*)(tcs + kh * 128 + 32 * ct + 8 * g + 4 * hh); for (int j = 0; j < 4; ++j) acc[ct][4 * g + j] -= mean * cv[j]; }
  float val[4][8];
  if (kh == 0) {
#pragma unroll
    for (int ct = 0; ct < 4; ++ct) { for (int g = 0; g < 2; ++g) xch[((w * 4 + ct) * 2 + g) * 64 + l] = f32x4{acc[ct][8 + 4 * g], acc[ct][9 + 4 * g], acc[ct][10 + 4 * g], acc[ct][11 + 4 * g]};
      for (int r = 0; r < 8; ++r) val[ct][r] = acc[ct][r]; }
  } else {
#pragma unroll
    for (int ct = 0; ct < 4; ++ct) { for (int g = 0; g < 2; ++g) xch[((w * 4 + ct) * 2 + g) * 64 + l] = f32x4{acc[ct][4 * g], acc[ct][1 + 4 * g], acc[ct][2 + 4 * g], acc[ct][3 + 4 * g]};
      for (int r = 0; r < 8; ++r) val[ct][r] = acc[ct][8 + r]; }
  }
  lds_bar();
  STAMP_AT(4);
  const __amdgpu_buffer_rsrc_t rw = make_rsrc(ws, WS_USED);
#pragma unroll
  for (int ct = 0; ct < 4; ++ct) {
    float a[8];
#pragma unroll
    for (int g = 0; g < 2; ++g) {
      const f32x4 t = xch[(((w ^ 4) * 4 + ct) * 2 + g) * 64 + l];
      const f32x4 bb = *(const LAS f32x4*)(tb + 32 * ct + 16 * kh + 8 * g + 4 * hh);
#pragma unroll
      for (int j = 0; j < 4; ++j) {
        const float y = (val[ct][4 * g + j] + t[j]) * rstd + bb[j];
        const float u = __builtin_amdgcn_exp2f(fminf(y, 30.f) * LOG2E);
        const float nn = u * (u + 2.f);
        a[4 * g + j] = y * nn * __builtin_amdgcn_rcpf(nn + 2.f);
      }
    }
    st16<MG>(rw, WS_ACT + (unsigned)((rt * 112 + cb * 8 + ct * 2 + kh) * 64 + l) * 16, pack8(a[0], a[1], a[2], a[3], a[4], a[5], a[6], a[7]));
  }
  STAMP_AT(5);
  if constexpr (MG) publish(ws, FL_D + (rb * 16 + cb) * 64, c.epoch);
  STAMP_AT(6);
  STAMP_OUT(3, 7);
}

constexpr int E_NSLOT = 10, E_NST = 14;
constexpr int cmin(int a, int b) { return a < b ? a : b; }
constexpr int cmax(int a, int b) { return a > b ? a : b; }
constexpr int e_wait(int st) {
  int n = 2 * (E_NST - 1 - st);
  if (st < E_NSLOT) return n + 2 * cmax(0, cmin(st - 1, E_NST - E_NSLOT));
  return 2 * (cmin(E_NST - 1, st + E_NSLOT - 2) - st);
}
template <bool MG> DI void phaseE(const Params& p, const Ctx& c) {
  STAMP_DECL STAMP_AT(0);
  char* ws = p.ws;
  LAS char* lds = c.lds;
  const int l = c.l, w = c.w, hh = c.hh, b = c.b;
  const int rb = (b & 7) * 4 + ((b >> 3) & 3), cb = b >> 5;
  const int m = w & 1, kq = w >> 1;
  const int rt = 2 * rb + m, kstep = 4 * cb + kq;
  sfor<0, E_NSLOT>([&](auto ST) { prefetch_w2(p, c, ST.value, ST.value); });
  if constexpr (MG) await(ws, FL_D + (rb >> 1) * 16 * 64, 14, c.epoch);
  STAMP_AT(1);
  bf16x8 resv;
  gload16<0, MG>(resv, ws + WS_HF + (size_t)(rt * 32 + kstep) * 1024, l * 16);
  const char* abase = ws + WS_ACT + ((size_t)rt * 112 + kq * 28) * 1024;
  bf16x8 af[28];
  sfor<0, 28>([&](auto I) { constexpr int i = I.value; gload16<(i & 3) * 1024, MG>(af[i], abase + (i >> 2) * 4096, l * 16); });
  f32x16 acc[2] = {};
  const int q4 = (l & 15) >> 2, p4 = l & 3, blk = (l >> 4) & 1;
  const int wa0 = kq * 4096 + (4 * hh + q4) * 128 + (((2 * blk + (p4 >> 1)) ^ (((q4 >> 1) & 1) << 2)) * 16) + (p4 & 1) * 8;
  const int wa1 = wa0 ^ 64;
  sfor<0, E_NST>([&](auto ST) {
    constexpr int st = ST.value;
    asm volatile("s_waitcnt vmcnt(%2) lgkmcnt(0)\n\ts_barrier" : "+v"(af[2 * st]), "+v"(af[2 * st + 1]) : "i"(e_wait(st)) : "memory");
    if constexpr (st == 0) STAMP_AT(2);
    if constexpr (st >= 1 && st - 1 + E_NSLOT < E_NST) prefetch_w2(p, c, st - 1 + E_NSLOT, (st - 1) % E_NSLOT);
    const LAS char* sl = lds + (st % E_NSLOT) * 16384;
#pragma unroll
    for (int i = 0; i < 2; ++i) {
      const bf16x8 w0 = cat8(trread(sl + wa0 + i * 2048), trread(sl + wa0 + i * 2048 + 1024));
      const bf16x8 w1 = cat8(trread(sl + wa1 + i * 2048), trread(sl + wa1 + i * 2048 + 1024));
      acc[0] = MFMA(w0, af[2 * st + i], acc[0]);
      acc[1] = MFMA(w1, af[2 * st + i], acc[1]);
    }
  });
  STAMP_AT(3);
  lds_bar();
  float val[8];
  quarter_reduce(c, (LAS f32x4*)lds, acc, val);
  asm volatile("" : "+v"(resv));
  const u32x4 ru = __builtin_bit_cast(u32x4, resv);
  const u32x2 o0 = {cvtpk(val[0] + bf_lo(ru[0]), val[1] + bf_hi(ru[0])), cvtpk(val[2] + bf_lo(ru[1]), val[3] + bf_hi(ru[1]))};
  const u32x2 o1 = {cvtpk(val[4] + bf_lo(ru[2]), val[5] + bf_hi(ru[2])), cvtpk(val[6] + bf_lo(ru[3]), val[7] + bf_hi(ru[3]))};
  const __amdgpu_buffer_rsrc_t ro = make_rsrc(p.out, S * D * 2);
  const unsigned oo = (unsigned)(rt * 32 + (l & 31)) * 1024u + kstep * 32 + hh * 8;
  st8<false>(ro, oo, o0);
  st8<false>(ro, oo + 16, o1);
  STAMP_AT(4);
  STAMP_OUT(4, 5);
}

DI Ctx make_ctx(char* lds_g) {
  Ctx c;
  c.lds = (LAS char*)lds_g; c.lds0 = (unsigned)(uintptr_t)lds_g;
  c.tid = threadIdx.x; c.l = c.tid & 63; c.w = rfl(c.tid >> 6); c.hh = c.l >> 5; c.b = blockIdx.x; c.epoch = 0;
  return c;
}

__global__ __launch_bounds__(512, 2) void block_mega(Params p) {
  extern __shared__ __attribute__((aligned(16))) char lds_g[];
  Ctx c = make_ctx(lds_g);
  asm volatile("global_load_dword %0, %1, %2 sc1" : "=v"(c.epoch) : "v"(0u), "s"(p.ws + WS_FLAG) : "memory");
  phaseA<true>(p, c);
  phaseB<true>(p, c);
  phaseC<true>(p, c);
  if (c.b < 224) phaseD<true>(p, c);
  phaseE<true>(p, c);
  if (c.b == 0 && c.tid == 0) __hip_atomic_store((GAS unsigned*)(p.ws + WS_FLAG), c.epoch, __ATOMIC_RELAXED, __HIP_MEMORY_SCOPE_AGENT);
}

extern "C" void kernel_launch(void* const* d_in, const int* in_sizes, int n_in, void* d_out, int out_size, void* d_ws, size_t ws_size, hipStream_t stream) {
  Params p;
  p.x = (const u16*)d_in[0]; p.wqkv = (const u16*)d_in[1]; p.wo = (const u16*)d_in[2];
  p.w1 = (const u16*)d_in[4]; p.b1 = (const u16*)d_in[5]; p.w2 = (const u16*)d_in[6];
  p.ws = (char*)d_ws; p.out = (u16*)d_out;
  (void)hipFuncSetAttribute((const void*)block_mega, hipFuncAttributeMaxDynamicSharedMemorySize, LDS_BYTES);
  block_mega<<<256, 512, LDS_BYTES, stream>>>(p);
}
```

```cpp
#include <hip/hip_runtime.h>
#include <hip/hip_bf16.h>
#include <type_traits>
#define STAMP_DECL
#define STAMP_AT(i)
#define STAMP_OUT(k, n)
#define MEGA 1

typedef unsigned short u16;
using bf16x8 = __attribute__((ext_vector_type(8))) short;
using s16x4 = __attribute__((ext_vector_type(4))) short;
using f32x16 = __attribute__((ext_vector_type(16))) float;
using f32x4 = __attribute__((ext_vector_type(4))) float;
using f32x2 = __attribute__((ext_vector_type(2))) float;
using u32x4 = __attribute__((ext_vector_type(4))) unsigned;
using u32x2 = __attribute__((ext_vector_type(2))) unsigned;
using bf16x2_t = __attribute__((ext_vector_type(2))) __bf16;
#define DI __device__ __forceinline__
#define LAS __attribute__((address_space(3)))
#define GAS __attribute__((address_space(1)))
#define MFMA(a, b, c) __builtin_amdgcn_mfma_f32_32x32x16_bf16((a), (b), (c), 0, 0, 0)
#define SBAR() __builtin_amdgcn_sched_barrier(0)

constexpr int S = 2048, D = 512, NH = 32, F = 1792;
constexpr float LN_EPS = 1e-5f;
constexpr float LOG2E = 1.4426950408889634f;
constexpr float QSCALE = 0.25f * LOG2E;
constexpr float BOUND_SAFE = 40.f;
constexpr int LDS_BYTES = 163840;

constexpr unsigned WS_FLAG = 0;
constexpr unsigned FL_A = 4096, FL_B = FL_A + 256 * 64, FL_C = FL_B + 256 * 64, FL_D = FL_C + 256 * 64, FL_TMO = 64;
constexpr unsigned WS_KMAX = 128u << 10, WS_STAT = 256u << 10;
constexpr unsigned WS_Q = 1u << 20, WS_KV = 3u << 20  , WS_O = 7u << 20, WS_HF = 9u << 20, WS_ACT = 11u << 20;
constexpr unsigned WS_USED = 19u << 20;

struct Params { const u16 *x, *wqkv, *wo, *w1, *b1, *w2; char* ws; u16* out; };

template <int N> using IC = std::integral_constant<int, N>;
template <int B, int E, class Fn> DI void sfor(Fn&& f) { if constexpr (B < E) { f(IC<B>{}); sfor<B + 1, E>(f); } }

DI int rfl(int v) { return __builtin_amdgcn_readfirstlane(v); }
DI unsigned cvtpk(float lo, float hi) { const f32x2 v = {lo, hi}; return __builtin_bit_cast(unsigned, __builtin_convertvector(v, bf16x2_t)); }
DI float bf_lo(unsigned u) { return __uint_as_float(u << 16); }
DI float bf_hi(unsigned u) { return __uint_as_float(u & 0xffff0000u); }
DI float dot2(unsigned a, unsigned b, float c) {
  return __builtin_amdgcn_fdot2_f32_bf16(__builtin_bit_cast(bf16x2_t, a), __builtin_bit_cast(bf16x2_t, b), c, false);
}
constexpr unsigned ONES2 = 0x3F803F80u;
#define MFMA16(a, b, c) __builtin_amdgcn_mfma_f32_16x16x32_bf16((a), (b), (c), 0, 0, 0)
DI f32x2 unpk(unsigned u) { return f32x2{__uint_as_float(u << 16), __uint_as_float(u & 0xffff0000u)}; }
DI bf16x8 rowsum_sel(int l) { const unsigned on = ((l & 15) == ((l >> 4) & 1)) ? ONES2 : 0u; return __builtin_bit_cast(bf16x8, u32x4{on, on, on, on}); }
DI void dma16(const void* sbase, unsigned voff, unsigned lds) {
  asm volatile("s_mov_b32 m0, %2\n\ts_nop 0\n\tglobal_load_lds_dwordx4 %0, %1" ::"v"(voff), "s"(sbase), "s"(lds) : "memory");
}
DI void dma16_sc1(const void* sbase, unsigned voff, unsigned lds) {
  asm volatile("s_mov_b32 m0, %2\n\ts_nop 0\n\tglobal_load_lds_dwordx4 %0, %1 sc1" ::"v"(voff), "s"(sbase), "s"(lds) : "memory");
}
template <int IMM, bool SC1> DI void gload16(bf16x8& dst, const void* sbase, unsigned voff) {
  if constexpr (SC1) asm volatile("global_load_dwordx4 %0, %1, %2 offset:%3 sc1" : "=v"(dst) : "v"(voff), "s"(sbase), "i"(IMM) : "memory");
  else asm volatile("global_load_dwordx4 %0, %1, %2 offset:%3" : "=v"(dst) : "v"(voff), "s"(sbase), "i"(IMM) : "memory");
}
template <int IMM, bool SC1 = false> DI void gload8(u32x2& dst, const void* sbase, unsigned voff) {
  if constexpr (SC1) asm volatile("global_load_dwordx2 %0, %1, %2 offset:%3 sc1" : "=v"(dst) : "v"(voff), "s"(sbase), "i"(IMM) : "memory");
  else asm volatile("global_load_dwordx2 %0, %1, %2 offset:%3" : "=v"(dst) : "v"(voff), "s"(sbase), "i"(IMM) : "memory");
}
template <bool SC1> DI void gload4(float& dst, const void* sbase, unsigned voff) {
  if constexpr (SC1) asm volatile("global_load_dword %0, %1, %2 sc1" : "=v"(dst) : "v"(voff), "s"(sbase) : "memory");
  else asm volatile("global_load_dword %0, %1, %2" : "=v"(dst) : "v"(voff), "s"(sbase) : "memory");
}
typedef __attribute__((ext_vector_type(4))) unsigned v4u_;
typedef __attribute__((ext_vector_type(2))) unsigned v2u_;
struct WsBuf { __amdgpu_buffer_rsrc_t r; };
DI __amdgpu_buffer_rsrc_t make_rsrc(void* p, unsigned bytes) { return __builtin_amdgcn_make_buffer_rsrc(p, 0, bytes, 0x00020000); }
template <bool SC1> DI void st16(__amdgpu_buffer_rsrc_t r, unsigned off, bf16x8 v) { __builtin_amdgcn_raw_buffer_store_b128(__builtin_bit_cast(v4u_, v), r, off, 0, SC1 ? 16 : 0); }
template <bool SC1> DI void st8(__amdgpu_buffer_rsrc_t r, unsigned off, u32x2 v) { __builtin_amdgcn_raw_buffer_store_b64(v, r, off, 0, SC1 ? 16 : 0); }
template <bool SC1> DI void st4(__amdgpu_buffer_rsrc_t r, unsigned off, unsigned v) { __builtin_amdgcn_raw_buffer_store_b32(v, r, off, 0, SC1 ? 16 : 0); }

template <int N> DI void wait_bar() { asm volatile("s_waitcnt vmcnt(%0) lgkmcnt(0)\n\ts_barrier" ::"i"(N) : "memory"); }
DI void lds_bar() { asm volatile("s_waitcnt lgkmcnt(0)\n\ts_barrier" ::: "memory"); }
DI s16x4 trread(const LAS char* p) {
  typedef short v4i16_t __attribute__((ext_vector_type(4)));
  return __builtin_bit_cast(s16x4, __builtin_amdgcn_ds_read_tr16_b64_v4i16((LAS v4i16_t*)p));
}
DI bf16x8 cat8(s16x4 a, s16x4 b) { return __builtin_shufflevector(a, b, 0, 1, 2, 3, 4, 5, 6, 7); }
DI bf16x8 pack8(float a0, float a1, float a2, float a3, float a4, float a5, float a6, float a7) {
  u32x4 w = {cvtpk(a0, a1), cvtpk(a2, a3), cvtpk(a4, a5), cvtpk(a6, a7)};
  return __builtin_bit_cast(bf16x8, w);
}
template <int CTRL> DI float dpp(float v) { return __uint_as_float(__builtin_amdgcn_update_dpp(0u, __float_as_uint(v), CTRL, 0xf, 0xf, true)); }
DI float sum8(float v) { v += dpp<0xB1>(v); v += dpp<0x4E>(v); v += dpp<0x141>(v); return v; }
DI float sumx32(float v) { auto r = __builtin_amdgcn_permlane32_swap(__float_as_uint(v), __float_as_uint(v), false, false); return __uint_as_float(r[0]) + __uint_as_float(r[1]); }
DI float maxx32(float v) { auto r = __builtin_amdgcn_permlane32_swap(__float_as_uint(v), __float_as_uint(v), false, false); return fmaxf(__uint_as_float(r[0]), __uint_as_float(r[1])); }
DI float maxx16(float v) { auto r = __builtin_amdgcn_permlane16_swap(__float_as_uint(v), __float_as_uint(v), false, false); return fmaxf(__uint_as_float(r[0]), __uint_as_float(r[1])); }
DI float max16(float v) { v = fmaxf(v, dpp<0xB1>(v)); v = fmaxf(v, dpp<0x4E>(v)); v = fmaxf(v, dpp<0x141>(v)); v = fmaxf(v, dpp<0x140>(v)); return v; }

DI void publish(char* ws, unsigned flag_off, unsigned epoch) {
  asm volatile("s_waitcnt vmcnt(0) lgkmcnt(0)\n\ts_barrier" ::: "memory");
  if (threadIdx.x == 0) __hip_atomic_store((GAS unsigned*)(ws + flag_off), epoch, __ATOMIC_RELAXED, __HIP_MEMORY_SCOPE_AGENT);
}
DI void await(char* ws, unsigned flag_off, int n, unsigned epoch) {
  if (threadIdx.x >= 448) {
    const int l = threadIdx.x & 63;
    GAS unsigned* f = (GAS unsigned*)(ws + flag_off + (l < n ? l : 0) * 64);
    for (unsigned spins = 0; spins < (1u << 20); ++spins) {
      const unsigned v = __hip_atomic_load(f, __ATOMIC_RELAXED, __HIP_MEMORY_SCOPE_AGENT);
      if (__all(v == epoch)) break;
      __builtin_amdgcn_s_sleep(1);
    }
  }
  asm volatile("s_barrier" ::: "memory");
}

struct Ctx { LAS char* lds; unsigned lds0; int tid, l, w, hh, b; unsigned epoch; };

template <bool MG> DI void phaseA(const Params& p, Ctx& c) {
  STAMP_DECL STAMP_AT(0);
  char* ws = p.ws;
  LAS char* lds = c.lds;
  const int l = c.l, w = c.w, hh = c.hh, b = c.b;
  const int rb = 8 * ((b & 7) >> 1) + ((b >> 3) & 7), hg = 4 * (b & 1) + (b >> 6);
  if constexpr (MG) {
    dma16((const char*)p.x + (size_t)b * 8192, w * 1024 + l * 16, c.lds0 + 4 * 32768 + w * 1024);
    dma16((const char*)p.wqkv + (size_t)b * 6144, (w < 6 ? w : 5) * 1024 + l * 16, c.lds0 + 4 * 32768 + 8192 + w * 1024);
  }
  const unsigned xoff = (unsigned)(64 * rb + 8 * w + (l >> 3)) * 1024u + (unsigned)(((l & 7) ^ ((4 * w + (l >> 4)) & 7)) * 16);
  const unsigned woff = (unsigned)(8 * w + (l >> 3)) * 3072u + (unsigned)hg * 128u + (unsigned)(((l & 7) ^ (((l >> 4) & 1) << 2)) * 16);
  auto issue = [&](int st) {
    const unsigned dst = c.lds0 + (st % 5) * 32768 + w * 1024;
    dma16((const char*)p.x + st * 128, xoff, dst);
    dma16((const char*)p.wqkv + (size_t)st * 64 * 3072, woff, dst + 8192);
    dma16((const char*)p.wqkv + (size_t)st * 64 * 3072 + 1024, woff, dst + 16384);
    dma16((const char*)p.wqkv + (size_t)st * 64 * 3072 + 2048, woff, dst + 24576);
  };
  issue(0); issue(1); issue(2); issue(3);
  if constexpr (MG) asm volatile("s_waitcnt vmcnt(16)\n\ts_barrier" ::: "memory");
  issue(4);
  STAMP_AT(1);
  f32x2 rs = {0.f, 0.f}, rq = {0.f, 0.f};
  const int q4 = (l & 15) >> 2, p4 = l & 3, blk = (l >> 4) & 1;
  const int wlane = (8 * hh + q4) * 128 + (((2 * blk + (p4 >> 1)) ^ (((q4 >> 1) & 1) << 2)) * 16) + (p4 & 1) * 8;
  LAS float* tmean = (LAS float*)(lds + 0);
  LAS float* trstd = tmean + 64;
  LAS float* tcs = tmean + 128;
  auto row_stats = [&](u32x4 sv) {
#pragma unroll
    for (int i = 0; i < 4; ++i) { const f32x2 v = unpk(sv[i]); rs += v; rq = __builtin_elementwise_fma(v, v, rq); }
    asm volatile("" : "+v"(rs), "+v"(rq));
  };
  auto stage_sync = [&](auto ST) {
    constexpr int st = ST.value;
    constexpr int last = 4 + st > 7 ? 7 : 4 + st;
    wait_bar<4 * (last - (st + 1))>();
    if constexpr (st + 5 < 8) issue(st + 5);
  };
  f32x16 acc[2] = {};
  const int m = w & 1, ty = w >> 1;
  if (w < 6) {
    const int swz = (l >> 1) & 7;
    const int xrow = (32 * m + (l & 31)) * 128;
    const int wa = 8192 + ty * 8192 + wlane;
    bf16x8 xf[2][4], wf[2][4][2]; u32x4 sv[2];
    auto load_frags = [&](auto SET, int st) {
      constexpr int q = SET.value;
      const LAS char* sl = lds + (st % 5) * 32768;
#pragma unroll
      for (int i = 0; i < 4; ++i) {
        xf[q][i] = *(const LAS bf16x8*)(sl + xrow + (((2 * i + hh) ^ swz) * 16));
        wf[q][i][0] = cat8(trread(sl + wa + i * 2048), trread(sl + wa + i * 2048 + 512));
        wf[q][i][1] = cat8(trread(sl + (wa ^ 64) + i * 2048), trread(sl + (wa ^ 64) + i * 2048 + 512));
      }
      sv[q] = *(const LAS u32x4*)(sl + w * 1024 + l * 16);
    };
    auto run = [&](auto SWAP) {
      constexpr bool vt = SWAP.value;
      wait_bar<16>();
      STAMP_AT(2);
      load_frags(IC<0>{}, 0);
      sfor<0, 8>([&](auto ST) {
        constexpr int st = ST.value, q = st & 1;
        if constexpr (st + 1 < 8) stage_sync(ST);
        SBAR();
#pragma unroll
        for (int i = 0; i < 2; ++i)
#pragma unroll
          for (int t = 0; t < 2; ++t) acc[t] = vt ? MFMA(xf[q][i], wf[q][i][t], acc[t]) : MFMA(wf[q][i][t], xf[q][i], acc[t]);
        SBAR();
        if constexpr (st + 1 < 8) load_frags(IC<(st + 1) & 1>{}, st + 1);
        SBAR();
#pragma unroll
        for (int i = 2; i < 4; ++i)
#pragma unroll
          for (int t = 0; t < 2; ++t) acc[t] = vt ? MFMA(xf[q][i], wf[q][i][t], acc[t]) : MFMA(wf[q][i][t], xf[q][i], acc[t]);
        row_stats(sv[q]);
        SBAR();
      });
    };
    if (ty == 2) run(IC<1>{}); else run(IC<0>{});
  } else {
    const bf16x8 sel = rowsum_sel(l);
    const int h7 = w - 6;
    int wa[3];
#pragma unroll
    for (int t = 0; t < 3; ++t) { const int ti = 3 * h7 + t; wa[t] = 8192 + (ti >> 1) * 8192 + (wlane ^ ((ti & 1) * 64)); }
    f32x4 cs[3] = {};
    wait_bar<16>();
    sfor<0, 8>([&](auto ST) {
      constexpr int st = ST.value;
      const LAS char* sl = lds + (st % 5) * 32768;
      bf16x8 wf[4][3];
#pragma unroll
      for (int i = 0; i < 4; ++i)
#pragma unroll
        for (int t = 0; t < 3; ++t) wf[i][t] = cat8(trread(sl + wa[t] + i * 2048), trread(sl + wa[t] + i * 2048 + 512));
      const u32x4 sv = *(const LAS u32x4*)(sl + w * 1024 + l * 16);
      if constexpr (st + 1 < 8) stage_sync(ST);
#pragma unroll
      for (int i = 0; i < 4; ++i)
#pragma unroll
        for (int t = 0; t < 3; ++t) cs[t] = MFMA16(wf[i][t], sel, cs[t]);
      row_stats(sv);
    });
    asm volatile("s_waitcnt lgkmcnt(0)" ::: "memory");
    if ((l & 15) < 2) {
#pragma unroll
      for (int t = 0; t < 3; ++t) *(LAS f32x4*)(tcs + (3 * h7 + t) * 32 + 16 * (l & 15) + 4 * (l >> 4)) = cs[t];
    }
  }
  STAMP_AT(4);
  if constexpr (MG) { asm volatile("" : "+v"(c.epoch)); c.epoch += 1u; }
  { const float s1 = sum8(rs[0] + rs[1]), s2 = sum8(rq[0] + rq[1]);
    if ((l & 7) == 0) { const float mean = s1 * (1.f / D); tmean[8 * w + (l >> 3)] = mean; trstd[8 * w + (l >> 3)] = __builtin_amdgcn_rsqf(fmaxf(s2 * (1.f / D) - mean * mean, 0.f) + LN_EPS); } }
  lds_bar();
  const __amdgpu_buffer_rsrc_t rw = make_rsrc(ws, WS_USED);
  const int tile = 2 * rb + m;
  if (w < 4) {
    const float mean_l = tmean[32 * m + (l & 31)], rstd_l = trstd[32 * m + (l & 31)];
    const float sc = ty == 0 ? rstd_l * QSCALE : rstd_l, nm = -mean_l * sc;
    const unsigned dst = (ty == 0 ? WS_Q + (unsigned)tile * 1024 : WS_KV + (unsigned)tile * 2048) + l * 16;
    const unsigned hstride = ty == 0 ? 65536u : 131072u;
    f32x4 cv[2][4];
#pragma unroll
    for (int t = 0; t < 2; ++t)
#pragma unroll
      for (int g = 0; g < 4; ++g) cv[t][g] = *(const LAS f32x4*)(tcs + ty * 64 + t * 32 + 8 * g + 4 * hh);
#pragma unroll
    for (int t = 0; t < 2; ++t)
#pragma unroll
      for (int s = 0; s < 2; ++s) {
        f32x2 v[4];
#pragma unroll
        for (int j = 0; j < 4; ++j) { const int r = 8 * s + 2 * j; const f32x2 a = {acc[t][r], acc[t][r + 1]}, cc = {cv[t][r >> 2][r & 3], cv[t][r >> 2][(r & 3) + 1]};
          v[j] = __builtin_elementwise_fma(a, f32x2{sc, sc}, cc * nm); }
        const int head = 4 * hg + 2 * t + s;
        st16<MG>(rw, dst + (unsigned)head * hstride, pack8(v[0][0], v[0][1], v[1][0], v[1][1], v[2][0], v[2][1], v[3][0], v[3][1]));
        if (ty == 1) {
          f32x2 n2 = v[0] * v[0]; n2 = __builtin_elementwise_fma(v[1], v[1], n2); n2 = __builtin_elementwise_fma(v[2], v[2], n2); n2 = __builtin_elementwise_fma(v[3], v[3], n2);
          const float mx = maxx16(max16(sumx32(n2[0] + n2[1])));
          if (l == 0) st4<MG>(rw, WS_KMAX + (unsigned)(head * 64 + tile) * 4, __float_as_uint(mx));
        }
      }
  } else if (w < 6) {
    const int n = l & 31;
    f32x4 mm[4], rr[4];
#pragma unroll
    for (int g = 0; g < 4; ++g) { mm[g] = *(const LAS f32x4*)(tmean + 32 * m + 8 * g + 4 * hh); rr[g] = *(const LAS f32x4*)(trstd + 32 * m + 8 * g + 4 * hh); }
#pragma unroll
    for (int t = 0; t < 2; ++t) {
      const float ncs = -tcs[128 + t * 32 + n];
      const int head = 4 * hg + 2 * t + (n >> 4);
#pragma unroll
      for (int s = 0; s < 2; ++s) {
        f32x2 v[4];
#pragma unroll
        for (int j = 0; j < 4; ++j) { const int r = 8 * s + 2 * j; const f32x2 a = {acc[t][r], acc[t][r + 1]}, m2 = {mm[r >> 2][r & 3], mm[r >> 2][(r & 3) + 1]}, r2 = {rr[r >> 2][r & 3], rr[r >> 2][(r & 3) + 1]};
          v[j] = __builtin_elementwise_fma(m2, f32x2{ncs, ncs}, a) * r2; }
        st16<MG>(rw, WS_KV + (unsigned)(head * 64 + tile) * 2048 + 1024 + s * 512 + hh * 256 + (n & 15) * 16,
                 pack8(v[0][0], v[0][1], v[1][0], v[1][1], v[2][0], v[2][1], v[3][0], v[3][1]));
      }
    }
  }
  STAMP_AT(5);
  if constexpr (MG) publish(ws, FL_A + (hg * 32 + rb) * 64, c.epoch);
  STAMP_AT(6);
  STAMP_OUT(0, 7);
}

DI unsigned wpiece_lane(int l, unsigned row_bytes) { return (unsigned)(l >> 3) * row_bytes + (unsigned)(((l & 7) ^ (((l >> 4) & 1) << 2)) * 16); }
DI void prefetch_wo(const Params& p, const Ctx& c) {
  const unsigned off = wpiece_lane(c.l, 1024) + (unsigned)(c.b >> 5) * 128u;
#pragma unroll
  for (int i = 0; i < 8; ++i) dma16((const char*)p.wo + (size_t)(64 * c.w + 8 * i) * 1024, off, c.lds0 + c.w * 8192 + i * 1024);
}
DI constexpr unsigned w1_stage_base(int j) { return j < 6 ? 65536 + j * 16384 : (j - 6) * 16384; }
template <int NW> DI void prefetch_w1(const Params& p, const Ctx& c, int p0, int p1) {
  if (c.w >= NW) return;
  const unsigned off = wpiece_lane(c.l, F * 2) + (unsigned)(c.b >> 4) * 256u;
  for (int pc = p0 + c.w; pc < p1; pc += NW) {
    const int j = pc >> 4, seg = (pc >> 3) & 1, r8 = pc & 7;
    dma16((const char*)p.w1 + (size_t)(64 * j + 8 * r8) * (F * 2) + seg * 128, off, c.lds0 + (j < 6 ? 65536 + j * 16384 : (j - 6) * 16384) + seg * 8192 + r8 * 1024);
  }
}
template <int NW> DI void prefetch_w2(const Params& p, const Ctx& c, int p0, int p1) {
  if (c.w >= NW) return;
  const unsigned off = wpiece_lane(c.l, 1024) + (unsigned)(c.b >> 5) * 128u;
  for (int pc = p0 + c.w; pc < p1; pc += NW) {
    const int st = pc >> 4, kq = (pc >> 2) & 3, r8 = pc & 3;
    dma16((const char*)p.w2 + (size_t)(448 * kq + 32 * st + 8 * r8) * 1024, off, c.lds0 + (st % 10) * 16384 + kq * 4096 + r8 * 1024);
  }
}

DI void warm_l2(const Params& p, const Ctx& c) {
  const unsigned off = (unsigned)(c.b >> 3) * 57344u + (unsigned)c.w * 7168u + (unsigned)c.l * 16u;
#pragma unroll
  for (int i = 0; i < 7; ++i) dma16((const char*)p.w2 + i * 1024, off, c.lds0 + 139520);
#pragma unroll
  for (int i = 0; i < 7; ++i) dma16((const char*)p.w1 + i * 1024, off, c.lds0 + 139520);
}

constexpr int B_ONES = 131072;
template <bool MG> DI void phaseB(const Params& p, const Ctx& c) {
  STAMP_DECL STAMP_AT(0);
  char* ws = p.ws;
  LAS char* lds = c.lds;
  const int l = c.l, w = c.w, hh = c.hh, b = c.b, tid = c.tid;
  const int head = 4 * (b & 7) + ((b >> 3) & 3), qb = b >> 5;
  for (int i = tid; i < (8192 + 256) / 16; i += 512) ((LAS u32x4*)(lds + B_ONES))[i] = u32x4{ONES2, ONES2, ONES2, ONES2};
  if constexpr (MG) await(ws, FL_A + (head >> 2) * 32 * 64, 32, c.epoch);
  STAMP_AT(1);
  const char* kvg = ws + WS_KV + (size_t)head * 131072 + w * 4096;
  unsigned kvl = c.lds0 + w * 4096;
  const unsigned l16 = l * 16;
#define KV_DMA4(SC) asm volatile("s_mov_b32 m0, %2\n\ts_nop 0\n\tglobal_load_lds_dwordx4 %0, %1" SC "\n\tglobal_load_lds_dwordx4 %0, %1 offset:1024" SC \
                "\n\tglobal_load_lds_dwordx4 %0, %1 offset:2048" SC "\n\tglobal_load_lds_dwordx4 %0, %1 offset:3072" SC ::"v"(l16), "s"(kvg), "s"(kvl) : "memory")
  if constexpr (MG) KV_DMA4(" sc1"); else KV_DMA4("");
  bf16x8 qf;
  float kmx;
  gload16<0, MG>(qf, ws + WS_Q + (size_t)(head * 64 + qb * 8 + w) * 1024, l * 16);
  gload4<MG>(kmx, ws + WS_KMAX + head * 256, l * 4);
  asm volatile("s_waitcnt vmcnt(0)" : "+v"(qf), "+v"(kmx)::"memory");
  kmx = maxx32(maxx16(max16(kmx)));
  float qn; { const u32x4 u = __builtin_bit_cast(u32x4, qf); f32x2 t = {0.f, 0.f}; for (int i = 0; i < 4; ++i) { const f32x2 v = unpk(u[i]); t = __builtin_elementwise_fma(v, v, t); } qn = sumx32(t[0] + t[1]); }
  const float bound = __builtin_amdgcn_sqrtf(qn * kmx) * 1.01f + 1e-6f;
  const bool safe = __all(bound <= BOUND_SAFE);
  f32x16 o = {};
  const int lo5 = l & 31;
  const bool isv = lo5 < 16;
  const int va0 = isv ? 1024 + hh * 256 + lo5 * 16 : B_ONES + ((lo5 >= 20 && lo5 < 28) ? 64 : 0);
  lds_bar();
  STAMP_AT(2);
  if (safe) {
    f32x16 negc; for (int i = 0; i < 16; ++i) negc[i] = -bound;
    f32x16 s0, s1;
    bf16x8 pa, pb = {};
    bf16x8 kf, v0a, v0b, v1 = {};
    unsigned ka = c.lds0 + l * 16, va = c.lds0 + va0;
    const unsigned vstep = isv ? 8192u : 0u;
    asm volatile("ds_read_b128 %1, %3\n\tds_read_b128 %2, %4\n\ts_waitcnt lgkmcnt(1)\n\t"
                 "v_mfma_f32_32x32x16_bf16 %0, %1, %5, %6\n\tds_read_b128 %1, %3 offset:2048\n\ts_waitcnt lgkmcnt(0)\n\ts_nop 7\n\ts_nop 3"
                 : "=&{v[192:207]}"(s0), "=&v"(kf), "=&v"(v0a) : "v"(ka), "v"(va), "v"(qf), "v"(negc) : "memory");
#define E_(b, i) "v_exp_f32 v[" #b "+" #i "], v[" #b "+" #i "]\n\t"
#define C_(d, i, b, j, k) "v_cvt_pk_bf16_f32 v[" #d "+" #i "], v[" #b "+" #j "], v[" #b "+" #k "]\n\t"
#define STEP_(SC, SN, V0C, V0N, KOFF, V1OFF, V0OFF, X) \
      "s_waitcnt lgkmcnt(0)\n\t" \
      "v_mfma_f32_32x32x16_bf16 v[" #SN ":" #SN "+15], %[kf], %[q], %[neg]\n\t" \
      "ds_read_b128 %[kf], %[ka] offset:" #KOFF "\n\t" \
      E_(SC, 0) E_(SC, 1) E_(SC, 2) E_(SC, 3) \
      "v_mfma_f32_32x32x16_bf16 %[o], %[v1], v[228:231], %[o]\n\t" \
      "ds_read_b128 %[v1], %[va] offset:" #V1OFF "\n\t" \
      "ds_read_b128 " V0N ", %[va] offset:" #V0OFF "\n\t" \
      E_(SC, 4) E_(SC, 5) E_(SC, 6) E_(SC, 7) \
      C_(224, 0, SC, 0, 1) C_(224, 1, SC, 2, 3) \
      E_(SC, 8) E_(SC, 9) \
      C_(224, 2, SC, 4, 5) C_(224, 3, SC, 6, 7) \
      E_(SC, 10) E_(SC, 11) \
      "v_mfma_f32_32x32x16_bf16 %[o], " V0C ", v[224:227], %[o]\n\t" \
      X \
      E_(SC, 12) E_(SC, 13) E_(SC, 14) E_(SC, 15) \
      C_(228, 0, SC, 8, 9) C_(228, 1, SC, 10, 11) C_(228, 2, SC, 12, 13) C_(228, 3, SC, 14, 15)
#define GROUP_(X0, X1, X2, X3) \
        STEP_(192, 208, "%[v0a]", "%[v0b]", 4096, 512, 2048, X0) \
        STEP_(208, 192, "%[v0b]", "%[v0a]", 6144, 2560, 4096, X1) \
        STEP_(192, 208, "%[v0a]", "%[v0b]", 8192, 4608, 6144, X2) \
        STEP_(208, 192, "%[v0b]", "%[v0a]", 10240, 6656, 8192, X3) \
        "v_add_u32 %[ka], 0x2000, %[ka]\n\tv_add_u32 %[va], %[va], %[vs]\n\t"
#define LOOP_TAIL_ "s_sub_u32 %[cnt], %[cnt], 1\n\ts_cmp_lg_u32 %[cnt], 0\n\ts_cbranch_scc1 1b\n\ts_waitcnt lgkmcnt(0)\n\ts_nop 7\n\ts_nop 3"
#define ATT_OUT_ "+{v[192:207]}"(s0), "=&{v[208:223]}"(s1), "=&{v[224:227]}"(pa), "+{v[228:231]}"(pb), [o] "+v"(o), [kf] "+v"(kf), [v0a] "+v"(v0a), [v0b] "=&v"(v0b), [v1] "+v"(v1), \
          [ka] "+v"(ka), [va] "+v"(va), [cnt] "+s"(cnt_)
#define RUN_GROUPS(NG) do { int cnt_ = (NG); \
      asm volatile("1:\n\t" GROUP_("", "", "", "") LOOP_TAIL_ : ATT_OUT_ : [q] "v"(qf), [neg] "v"(negc), [vs] "v"(vstep) : "memory", "scc"); } while (0)
    {
      int cnt_ = 3; unsigned ml = kvl;
      unsigned long long gb = (unsigned long long)kvg;
#define STG_(SC) asm volatile("1:\n\ts_add_u32 s60, s60, 0x8000\n\ts_addc_u32 s61, s61, 0\n\ts_add_u32 %[ml], %[ml], 0x8000\n\ts_mov_b32 m0, %[ml]\n\t" \
        GROUP_("global_load_lds_dwordx4 %[l16], s[60:61]" SC "\n\t", "global_load_lds_dwordx4 %[l16], s[60:61] offset:1024" SC "\n\t", \
               "global_load_lds_dwordx4 %[l16], s[60:61] offset:2048" SC "\n\t", "global_load_lds_dwordx4 %[l16], s[60:61] offset:3072" SC "\n\t") LOOP_TAIL_ \
        : ATT_OUT_, [ml] "+s"(ml), "+{s[60:61]}"(gb) : [q] "v"(qf), [neg] "v"(negc), [vs] "v"(vstep), [l16] "v"(l16) : "memory", "scc")
      if constexpr (MG) STG_(" sc1"); else STG_("");
    }
    wait_bar<4>();
    STAMP_AT(3);
    if constexpr (MG) warm_l2(p, c);
    RUN_GROUPS(5);
    wait_bar<0>();
    if constexpr (MG) { prefetch_wo(p, c); if (b < 224) prefetch_w1<8>(p, c, 80, 96); }
    RUN_GROUPS(4);
    if constexpr (MG) { lds_bar(); if (b < 224) prefetch_w1<8>(p, c, 0, 32); }
    RUN_GROUPS(4);
    o = MFMA(v1, pb, o);
  } else {
    for (int j = 1; j < 4; ++j) { kvg += 32768; kvl += 32768; const char* kvg_ = kvg; const unsigned kvl_ = kvl;
      if constexpr (MG) { dma16_sc1(kvg_, l16, kvl_); dma16_sc1(kvg_ + 1024, l16, kvl_ + 1024); dma16_sc1(kvg_ + 2048, l16, kvl_ + 2048); dma16_sc1(kvg_ + 3072, l16, kvl_ + 3072); }
      else { dma16(kvg_, l16, kvl_); dma16(kvg_ + 1024, l16, kvl_ + 1024); dma16(kvg_ + 2048, l16, kvl_ + 2048); dma16(kvg_ + 3072, l16, kvl_ + 3072); } }
    float mrun = -3.0e38f;
    const f32x16 zero = {};
    for (int t = 0; t < 64; ++t) {
      if (t == 12) wait_bar<4>();
      if (t == 32) { wait_bar<0>(); if (MG) { prefetch_wo(p, c); if (b < 224) prefetch_w1<8>(p, c, 80, 96); } }
      if (MG && t == 48) { lds_bar(); if (b < 224) prefetch_w1<8>(p, c, 0, 32); }
      const bf16x8 kf = *(const LAS bf16x8*)(lds + l * 16 + t * 2048);
      const int vo = isv ? va0 + t * 2048 : va0;
      const bf16x8 v0 = *(const LAS bf16x8*)(lds + vo), v1 = *(const LAS bf16x8*)(lds + vo + 512);
      f32x16 s = MFMA(kf, qf, zero);
      float tm = s[0];
#pragma unroll
      for (int i = 1; i < 16; ++i) tm = fmaxf(tm, s[i]);
      tm = maxx32(tm);
      const float mn = fmaxf(mrun, tm), f = __builtin_amdgcn_exp2f(mrun - mn);
      mrun = mn;
#pragma unroll
      for (int i = 0; i < 16; ++i) { o[i] *= f; s[i] = __builtin_amdgcn_exp2f(s[i] - mn); }
      o = MFMA(v0, pack8(s[0], s[1], s[2], s[3], s[4], s[5], s[6], s[7]), o);
      o = MFMA(v1, pack8(s[8], s[9], s[10], s[11], s[12], s[13], s[14], s[15]), o);
    }
  }
  STAMP_AT(4);
  const float inv = __builtin_amdgcn_rcpf(o[8]);
  const __amdgpu_buffer_rsrc_t rw = make_rsrc(ws, WS_USED);
  st16<MG>(rw, WS_O + (unsigned)(((qb * 8 + w) * 32 + head) * 64 + l) * 16, pack8(o[0] * inv, o[1] * inv, o[2] * inv, o[3] * inv, o[4] * inv, o[5] * inv, o[6] * inv, o[7] * inv));
  STAMP_AT(5);
  if constexpr (MG) {
    publish(ws, FL_B + (qb * 32 + head) * 64, c.epoch);
    if (b < 224) prefetch_w1<7>(p, c, 32, 80);
  }
  STAMP_AT(6);
  STAMP_OUT(1, 7);
}

template <int KQ> DI void quarter_reduce_t(int m, int l, LAS f32x4* xch, const f32x16 (&acc)[2], float (&val)[8]) {
#pragma unroll
  for (int d = 0; d < 4; ++d) {
    if (d == KQ) continue;
    xch[(((d * 4 + KQ) * 2 + m) * 2 + 0) * 64 + l] = f32x4{acc[d >> 1][8 * (d & 1)], acc[d >> 1][8 * (d & 1) + 1], acc[d >> 1][8 * (d & 1) + 2], acc[d >> 1][8 * (d & 1) + 3]};
    xch[(((d * 4 + KQ) * 2 + m) * 2 + 1) * 64 + l] = f32x4{acc[d >> 1][8 * (d & 1) + 4], acc[d >> 1][8 * (d & 1) + 5], acc[d >> 1][8 * (d & 1) + 6], acc[d >> 1][8 * (d & 1) + 7]};
  }
  lds_bar();
  f32x4 t[4][2];
#pragma unroll
  for (int s = 0; s < 4; ++s) { if (s == KQ) continue; t[s][0] = xch[(((KQ * 4 + s) * 2 + m) * 2 + 0) * 64 + l]; t[s][1] = xch[(((KQ * 4 + s) * 2 + m) * 2 + 1) * 64 + l]; }
#pragma unroll
  for (int j = 0; j < 8; ++j) val[j] = acc[KQ >> 1][8 * (KQ & 1) + j];
#pragma unroll
  for (int s = 0; s < 4; ++s) { if (s == KQ) continue; for (int j = 0; j < 4; ++j) { val[j] += t[s][0][j]; val[4 + j] += t[s][1][j]; } }
}
DI void quarter_reduce(const Ctx& c, LAS f32x4* xch, const f32x16 (&acc)[2], float (&val)[8]) {
  const int m = c.w & 1, kq = c.w >> 1;
  if (kq == 0) quarter_reduce_t<0>(m, c.l, xch, acc, val);
  else if (kq == 1) quarter_reduce_t<1>(m, c.l, xch, acc, val);
  else if (kq == 2) quarter_reduce_t<2>(m, c.l, xch, acc, val);
  else quarter_reduce_t<3>(m, c.l, xch, acc, val);
}

template <bool MG> DI void phaseC(const Params& p, const Ctx& c, u32x4& hkeep) {
  STAMP_DECL STAMP_AT(0);
  char* ws = p.ws;
  LAS char* lds = c.lds;
  const int l = c.l, w = c.w, hh = c.hh, b = c.b;
  const int rb = (b & 7) * 4 + ((b >> 3) & 3), cb = b >> 5;
  const int m = w & 1, kq = w >> 1;
  const int rt = 2 * rb + m, kstep = 4 * cb + kq;
  u32x2 xr0, xr1;
  gload8<0>(xr0, (const char*)p.x + (size_t)rt * 32 * 1024 + kstep * 32, (l & 31) * 1024 + hh * 8);
  gload8<16>(xr1, (const char*)p.x + (size_t)rt * 32 * 1024 + kstep * 32, (l & 31) * 1024 + hh * 8);
  if constexpr (!MG) prefetch_wo(p, c);
  if constexpr (MG) await(ws, FL_B + (rb >> 2) * 32 * 64, 32, c.epoch);
  STAMP_AT(1);
  const char* abase = ws + WS_O + ((size_t)rt * 32 + kq * 8) * 1024;
  bf16x8 af[8];
  sfor<0, 8>([&](auto I) { constexpr int i = I.value; gload16<(i & 3) * 1024, MG>(af[i], abase + (i >> 2) * 4096, l * 16); });
  f32x16 acc[2] = {};
  const int q4 = (l & 15) >> 2, p4 = l & 3, blk = (l >> 4) & 1;
  const int wa0 = (128 * kq + 4 * hh + q4) * 128 + (((2 * blk + (p4 >> 1)) ^ (((q4 >> 1) & 1) << 2)) * 16) + (p4 & 1) * 8;
  const int wa1 = wa0 ^ 64;
  asm volatile("s_waitcnt vmcnt(8)\n\ts_barrier" : "+v"(xr0), "+v"(xr1)::"memory");
  bf16x8 wf[8][2];
#pragma unroll
  for (int i = 0; i < 8; ++i) {
    wf[i][0] = cat8(trread(lds + wa0 + i * 2048), trread(lds + wa0 + i * 2048 + 1024));
    wf[i][1] = cat8(trread(lds + wa1 + i * 2048), trread(lds + wa1 + i * 2048 + 1024));
  }
  lds_bar();
  STAMP_AT(2);
  sfor<0, 8>([&](auto I) {
    constexpr int i = I.value;
    asm volatile("s_waitcnt vmcnt(%1)" : "+v"(af[i]) : "i"(7 - i) : "memory");
    acc[0] = MFMA(wf[i][0], af[i], acc[0]);
    acc[1] = MFMA(wf[i][1], af[i], acc[1]);
    SBAR();
  });
  STAMP_AT(3);
  float val[8];
  quarter_reduce(c, (LAS f32x4*)lds, acc, val);
  const u32x4 o = {cvtpk(val[0] + bf_lo(xr0[0]), val[1] + bf_hi(xr0[0])), cvtpk(val[2] + bf_lo(xr0[1]), val[3] + bf_hi(xr0[1])),
                   cvtpk(val[4] + bf_lo(xr1[0]), val[5] + bf_hi(xr1[0])), cvtpk(val[6] + bf_lo(xr1[1]), val[7] + bf_hi(xr1[1]))};
  hkeep = o;
  const __amdgpu_buffer_rsrc_t rw = make_rsrc(ws, WS_USED);
  st16<MG>(rw, WS_HF + (unsigned)((rt * 32 + kstep) * 64 + l) * 16, __builtin_bit_cast(bf16x8, o));
  f32x2 sm = {0.f, 0.f}, sq = {0.f, 0.f};
#pragma unroll
  for (int i = 0; i < 4; ++i) { const f32x2 v = unpk(o[i]); sm += v; sq = __builtin_elementwise_fma(v, v, sq); }
  LAS f32x2* tst = (LAS f32x2*)(lds + 65536 - 4096);
  tst[((m * 4 + kq) * 2 + hh) * 32 + (l & 31)] = f32x2{sm[0] + sm[1], sq[0] + sq[1]};
  lds_bar();
  if (w < 2 && l < 32) {
    f32x2 t8[8];
#pragma unroll
    for (int i = 0; i < 8; ++i) t8[i] = tst[(w * 8 + i) * 32 + l];
    const f32x2 t = ((t8[0] + t8[1]) + (t8[2] + t8[3])) + ((t8[4] + t8[5]) + (t8[6] + t8[7]));
    st8<MG>(rw, WS_STAT + (unsigned)(((2 * rb + w) * 8 + cb) * 32 + l) * 8, __builtin_bit_cast(u32x2, t));
  }
  STAMP_AT(4);
  if constexpr (MG) {
    publish(ws, FL_C + ((rb >> 1) * 16 + (rb & 1) * 8 + cb) * 64, c.epoch);
    if (b < 224) prefetch_w1<7>(p, c, 96, 128);
  }
  STAMP_AT(5);
  STAMP_OUT(2, 6);
}

constexpr int D_WIN = 12;
template <bool MG> DI void phaseD(const Params& p, const Ctx& c) {
  STAMP_DECL STAMP_AT(0);
  char* ws = p.ws;
  LAS char* lds = c.lds;
  const int l = c.l, w = c.w, hh = c.hh, b = c.b;
  const int rb = 2 * (b & 7) + ((b >> 3) & 1), cb = b >> 4;
  const int m = w & 3, nh = w >> 2;
  const int rt = 4 * rb + m;
  LAS float* tcs = (LAS float*)(lds + 49152);
  LAS float* tb = tcs + 128;
  if (c.tid < 128) tcs[c.tid] = 0.f;
  bf16x8 bv;
  gload16<0, false>(bv, (const char*)p.b1 + cb * 256, (l & 15) * 16);
  if constexpr (!MG) prefetch_w1<8>(p, c, 0, 128);
  if constexpr (MG) await(ws, FL_C + rb * 16 * 64, 16, c.epoch);
  STAMP_AT(1);
  u32x2 st[4];
  sfor<0, 4>([&](auto I) { constexpr int i = I.value; gload8<i * 256, MG>(st[i], ws + WS_STAT + (size_t)rt * 2048, (l & 31) * 8 + hh * 1024); });
  const int par = m >> 1;
  const char* abase = ws + WS_HF + (size_t)rt * 32 * 1024;
  const unsigned aoff0 = l * 16 + par * 1024, aoff1 = l * 16 + (1 - par) * 1024;
  bf16x8 af[32];
  auto load_a = [&](auto I) { constexpr int i = I.value;
    if constexpr (i & 1) gload16<((i >> 1) & 1) * 2048, MG>(af[i], abase + (i >> 2) * 4096, aoff1);
    else gload16<((i >> 1) & 1) * 2048, MG>(af[i], abase + (i >> 2) * 4096, aoff0); };
  sfor<0, D_WIN>(load_a);
  f32x16 acc[2] = {};
  f32x4 cs = {};
  const bf16x8 sel = rowsum_sel(l);
  const int q4 = (l & 15) >> 2, p4 = l & 3, blk = (l >> 4) & 1;
  const int wl = nh * 8192 + (4 * hh + q4) * 128 + (((4 * (m & 1) + 2 * blk + (p4 >> 1)) ^ (((q4 >> 1) & 1) << 2)) * 16) + (p4 & 1) * 8;
  const int wa_e = wl + par * 2048, wa_o = wl + (1 - par) * 2048;
  asm volatile("s_waitcnt vmcnt(%1) lgkmcnt(0)\n\ts_barrier" : "+v"(bv) : "i"(D_WIN + 4) : "memory");
  bf16x8 wf[2][2];
  auto load_w = [&](auto I) {
    constexpr int i = I.value;
    constexpr int o = (int)w1_stage_base(i >> 2) + (i & 2) * 2048;
    const int wa = (i & 1) ? wa_o : wa_e;
    wf[i & 1][0] = cat8(trread(lds + wa + o), trread(lds + wa + o + 1024));
    wf[i & 1][1] = cat8(trread(lds + (wa ^ 64) + o), trread(lds + (wa ^ 64) + o + 1024));
  };
  load_w(IC<0>{});
  STAMP_AT(2);
  sfor<0, 32>([&](auto I) {
    constexpr int i = I.value;
    if constexpr (i + D_WIN < 32) load_a(IC<i + D_WIN>{});
    constexpr int issued = i + D_WIN < 32 ? i + D_WIN + 1 : 32;
    asm volatile("s_waitcnt vmcnt(%1)" : "+v"(af[i]) : "i"(issued - i - 1) : "memory");
    acc[0] = MFMA(wf[i & 1][0], af[i], acc[0]);
    if constexpr (i + 1 < 32) { SBAR(); load_w(IC<i + 1>{}); SBAR(); }
    acc[1] = MFMA(wf[i & 1][1], af[i], acc[1]);
    if constexpr ((i & 1) == 0) cs = MFMA16(wf[i & 1][0], sel, cs);
    SBAR();
  });
  STAMP_AT(3);
  if ((l & 15) < 2) {
    LAS float* d = tcs + nh * 64 + (m & 1) * 32 + 16 * (l & 15) + 4 * (l >> 4);
#pragma unroll
    for (int j = 0; j < 4; ++j) __hip_atomic_fetch_add(d + j, cs[j], __ATOMIC_RELAXED, __HIP_MEMORY_SCOPE_WORKGROUP);
  }
  if (w == 0 && l < 16) { const u32x4 u = __builtin_bit_cast(u32x4, bv);
    *(LAS f32x4*)(tb + l * 8) = f32x4{bf_lo(u[0]), bf_hi(u[0]), bf_lo(u[1]), bf_hi(u[1])}; *(LAS f32x4*)(tb + l * 8 + 4) = f32x4{bf_lo(u[2]), bf_hi(u[2]), bf_lo(u[3]), bf_hi(u[3])}; }
  asm volatile("" : "+v"(st[0]), "+v"(st[1]), "+v"(st[2]), "+v"(st[3]));
  const f32x2 ss = __builtin_bit_cast(f32x2, st[0]) + __builtin_bit_cast(f32x2, st[1]) + __builtin_bit_cast(f32x2, st[2]) + __builtin_bit_cast(f32x2, st[3]);
  const float sm = sumx32(ss[0]), sq = sumx32(ss[1]);
  const float mean = sm * (1.f / D), rstd = __builtin_amdgcn_rsqf(fmaxf(sq * (1.f / D) - mean * mean, 0.f) + LN_EPS);
  const float nmr = -mean * rstd;
  lds_bar();
  STAMP_AT(4);
  const __amdgpu_buffer_rsrc_t rw = make_rsrc(ws, WS_USED);
#pragma unroll
  for (int t = 0; t < 2; ++t) {
    const int ct = 2 * nh + ((m & 1) ^ t);
    f32x4 cv[4], bb[4];
#pragma unroll
    for (int g = 0; g < 4; ++g) { cv[g] = *(const LAS f32x4*)(tcs + 32 * ct + 8 * g + 4 * hh); bb[g] = *(const LAS f32x4*)(tb + 32 * ct + 8 * g + 4 * hh); }
#pragma unroll
    for (int s = 0; s < 2; ++s) {
      unsigned pk[4];
#pragma unroll
      for (int j = 0; j < 4; ++j) {
        const int r = 8 * s + 2 * j;
        const f32x2 a = {acc[t][r], acc[t][r + 1]}, c2 = {cv[r >> 2][r & 3], cv[r >> 2][(r & 3) + 1]}, b2 = {bb[r >> 2][r & 3], bb[r >> 2][(r & 3) + 1]};
        const f32x2 y = __builtin_elementwise_fma(a, f32x2{rstd, rstd}, __builtin_elementwise_fma(c2, f32x2{nmr, nmr}, b2));
        const f32x2 z = y * LOG2E;
        const f32x2 u = {__builtin_amdgcn_exp2f(z[0]), __builtin_amdgcn_exp2f(z[1])};
        const f32x2 d = __builtin_elementwise_fma(u, u + 2.f, f32x2{2.f, 2.f});
        const f32x2 r2 = {__builtin_amdgcn_rcpf(d[0]), __builtin_amdgcn_rcpf(d[1])};
        const f32x2 th = __builtin_elementwise_fma(r2, f32x2{-2.f, -2.f}, f32x2{1.f, 1.f});
        const f32x2 o = y * th;
        pk[j] = cvtpk(o[0], o[1]);
      }
      st16<MG>(rw, WS_ACT + (unsigned)((rt * 112 + cb * 8 + ct * 2 + s) * 64 + l) * 16, __builtin_bit_cast(bf16x8, u32x4{pk[0], pk[1], pk[2], pk[3]}));
      if constexpr (MG) {
        const int q = 2 * t + s, st0 = q < 2 ? 0 : 4;
        prefetch_w2<8>(p, c, 16 * st0 + 24 * (q & 1), 16 * st0 + 24 * (q & 1) + 24);
      }
    }
  }
  STAMP_AT(5);
  if constexpr (MG) publish(ws, FL_D + (rb * 16 + cb) * 64, c.epoch);
  STAMP_AT(6);
  STAMP_OUT(3, 7);
}

constexpr int E_NSLOT = 10, E_NST = 14, E_WIN = 12, E_PRE = 8;
struct ESched {
  int posA[28], posW[E_NST], n_top[E_NST], n_mid[E_NST];
  constexpr ESched() : posA(), posW(), n_top(), n_mid() {
    int n = 0;
    for (int i = 0; i < E_WIN; ++i) posA[i] = n++;
    for (int st = 0; st < E_NST; ++st) {
      n_top[st] = n;
      if (E_PRE + st < E_NST) { n += 2; posW[E_PRE + st] = n - 1; }
      for (int j = 0; j < 2; ++j) if (E_WIN + 2 * st + j < 28) posA[E_WIN + 2 * st + j] = n++;
      n_mid[st] = n;
    }
  }
};
template <bool MG> DI void phaseE(const Params& p, const Ctx& c, u32x4 hkeep) {
  STAMP_DECL STAMP_AT(0);
  constexpr ESched sch{};
  char* ws = p.ws;
  LAS char* lds = c.lds;
  const int l = c.l, w = c.w, hh = c.hh, b = c.b;
  const int rb = (b & 7) * 4 + ((b >> 3) & 3), cb = b >> 5;
  const int m = w & 1, kq = w >> 1;
  const int rt = 2 * rb + m, kstep = 4 * cb + kq;
  if (!MG || b >= 224) prefetch_w2<7>(p, c, 0, 16 * E_PRE);
  else { prefetch_w2<7>(p, c, 16 * 3, 16 * 4); prefetch_w2<7>(p, c, 16 * 7, 16 * 8); }
  bf16x8 resv = __builtin_bit_cast(bf16x8, hkeep);
  if constexpr (!MG) gload16<0, false>(resv, ws + WS_HF + (size_t)(rt * 32 + kstep) * 1024, l * 16);
  if constexpr (MG) await(ws, FL_D + (rb >> 1) * 16 * 64, 14, c.epoch);
  STAMP_AT(1);
  const char* abase = ws + WS_ACT + ((size_t)rt * 112 + kq * 28) * 1024;
  bf16x8 af[28];
  auto load_a = [&](auto I) { constexpr int i = I.value; gload16<(i & 3) * 1024, MG>(af[i], abase + (i >> 2) * 4096, l * 16); };
  sfor<0, E_WIN>(load_a);
  f32x16 acc[2] = {};
  const int q4 = (l & 15) >> 2, p4 = l & 3, blk = (l >> 4) & 1;
  const int wa0 = kq * 4096 + (4 * hh + q4) * 128 + (((2 * blk + (p4 >> 1)) ^ (((q4 >> 1) & 1) << 2)) * 16) + (p4 & 1) * 8;
  const int wa1 = wa0 ^ 64;
  bf16x8 wf[2][2][2];
  auto load_w = [&](auto ST) {
    constexpr int st = ST.value;
    const LAS char* sl = lds + (st % E_NSLOT) * 16384;
#pragma unroll
    for (int i = 0; i < 2; ++i) {
      wf[st & 1][i][0] = cat8(trread(sl + wa0 + i * 2048), trread(sl + wa0 + i * 2048 + 1024));
      wf[st & 1][i][1] = cat8(trread(sl + wa1 + i * 2048), trread(sl + wa1 + i * 2048 + 1024));
    }
  };
  asm volatile("s_waitcnt vmcnt(%0)\n\ts_barrier" ::"i"(E_WIN) : "memory");
  load_w(IC<0>{});
  STAMP_AT(2);
  sfor<0, E_NST>([&](auto ST) {
    constexpr int st = ST.value;
    if constexpr (st == 2) asm volatile("s_waitcnt lgkmcnt(0)\n\ts_barrier" ::: "memory");
    if constexpr (st == 3) asm volatile("s_waitcnt lgkmcnt(0)\n\ts_barrier" ::: "memory");
    if constexpr (st == 7 || st == 9 || st == 11) asm volatile("s_waitcnt vmcnt(%0) lgkmcnt(0)\n\ts_barrier" ::"i"(sch.n_top[st] - sch.posW[st + 2] - 1) : "memory");
    if constexpr (E_PRE + st < E_NST) prefetch_w2<8>(p, c, 16 * (st + E_PRE), 16 * (st + E_PRE + 1));
    if constexpr (E_WIN + 2 * st < 28) load_a(IC<E_WIN + 2 * st>{});
    if constexpr (E_WIN + 2 * st + 1 < 28) load_a(IC<E_WIN + 2 * st + 1>{});
    asm volatile("s_waitcnt vmcnt(%2)" : "+v"(af[2 * st]), "+v"(af[2 * st + 1]) : "i"(sch.n_mid[st] - sch.posA[2 * st + 1] - 1) : "memory");
    acc[0] = MFMA(wf[st & 1][0][0], af[2 * st], acc[0]);
    acc[1] = MFMA(wf[st & 1][0][1], af[2 * st], acc[1]);
    if constexpr (st + 1 < E_NST) { SBAR(); load_w(IC<st + 1>{}); SBAR(); }
    acc[0] = MFMA(wf[st & 1][1][0], af[2 * st + 1], acc[0]);
    acc[1] = MFMA(wf[st & 1][1][1], af[2 * st + 1], acc[1]);
    SBAR();
  });
  STAMP_AT(3);
  float val[8];
  quarter_reduce(c, (LAS f32x4*)(lds + 65536), acc, val);
  asm volatile("" : "+v"(resv));
  const u32x4 ru = __builtin_bit_cast(u32x4, resv);
  const u32x2 o0 = {cvtpk(val[0] + bf_lo(ru[0]), val[1] + bf_hi(ru[0])), cvtpk(val[2] + bf_lo(ru[1]), val[3] + bf_hi(ru[1]))};
  const u32x2 o1 = {cvtpk(val[4] + bf_lo(ru[2]), val[5] + bf_hi(ru[2])), cvtpk(val[6] + bf_lo(ru[3]), val[7] + bf_hi(ru[3]))};
  const __amdgpu_buffer_rsrc_t ro = make_rsrc(p.out, S * D * 2);
  const unsigned oo = (unsigned)(rt * 32 + (l & 31)) * 1024u + kstep * 32 + hh * 8;
  st8<false>(ro, oo, o0);
  st8<false>(ro, oo + 16, o1);
  STAMP_AT(4);
  STAMP_OUT(4, 5);
}

DI Ctx make_ctx(char* lds_g) {
  Ctx c;
  c.lds = (LAS char*)lds_g; c.lds0 = (unsigned)(uintptr_t)lds_g;
  c.tid = threadIdx.x; c.l = c.tid & 63; c.w = rfl(c.tid >> 6); c.hh = c.l >> 5; c.b = blockIdx.x; c.epoch = 0;
  return c;
}

__global__ __launch_bounds__(512, 2) void block_mega(Params p) {
  extern __shared__ __attribute__((aligned(16))) char lds_g[];
  Ctx c = make_ctx(lds_g);
  asm volatile("global_load_dword %0, %1, %2 sc1" : "=v"(c.epoch) : "v"(0u), "s"(p.ws + WS_FLAG) : "memory");
  phaseA<true>(p, c);
  phaseB<true>(p, c);
  u32x4 hkeep;
  phaseC<true>(p, c, hkeep);
  if (c.b < 224) phaseD<true>(p, c);
  phaseE<true>(p, c, hkeep);
  if (c.b == 0 && c.tid == 0) __hip_atomic_store((GAS unsigned*)(p.ws + WS_FLAG), c.epoch, __ATOMIC_RELAXED, __HIP_MEMORY_SCOPE_AGENT);
}

extern "C" void kernel_launch(void* const* d_in, const int* in_sizes, int n_in, void* d_out, int out_size, void* d_ws, size_t ws_size, hipStream_t stream) {
  Params p;
  p.x = (const u16*)d_in[0]; p.wqkv = (const u16*)d_in[1]; p.wo = (const u16*)d_in[2];
  p.w1 = (const u16*)d_in[4]; p.b1 = (const u16*)d_in[5]; p.w2 = (const u16*)d_in[6];
  p.ws = (char*)d_ws; p.out = (u16*)d_out;
  (void)hipFuncSetAttribute((const void*)block_mega, hipFuncAttributeMaxDynamicSharedMemorySize, LDS_BYTES);
  block_mega<<<256, 512, LDS_BYTES, stream>>>(p);
}
```

```cpp
#include <hip/hip_runtime.h>
#include <hip/hip_bf16.h>
#include <type_traits>
#define STAMP_DECL
#define STAMP_AT(i)
#define STAMP_OUT(k, n)
#define MEGA 1

typedef unsigned short u16;
using bf16x8 = __attribute__((ext_vector_type(8))) short;
using s16x4 = __attribute__((ext_vector_type(4))) short;
using f32x16 = __attribute__((ext_vector_type(16))) float;
using f32x4 = __attribute__((ext_vector_type(4))) float;
using f32x2 = __attribute__((ext_vector_type(2))) float;
using u32x4 = __attribute__((ext_vector_type(4))) unsigned;
using u32x2 = __attribute__((ext_vector_type(2))) unsigned;
using bf16x2_t = __attribute__((ext_vector_type(2))) __bf16;
#define DI __device__ __forceinline__
#define LAS __attribute__((address_space(3)))
#define GAS __attribute__((address_space(1)))
#define MFMA(a, b, c) __builtin_amdgcn_mfma_f32_32x32x16_bf16((a), (b), (c), 0, 0, 0)
#define SBAR() __builtin_amdgcn_sched_barrier(0)

constexpr int S = 2048, D = 512, NH = 32, F = 1792;
constexpr float LN_EPS = 1e-5f;
constexpr float LOG2E = 1.4426950408889634f;
constexpr float QSCALE = 0.25f * LOG2E;
constexpr float BOUND_SAFE = 40.f;
constexpr int LDS_BYTES = 163840;

constexpr unsigned WS_FLAG = 0;
constexpr unsigned FL_A = 4096, FL_B = FL_A + 256 * 64, FL_C = FL_B + 256 * 64, FL_D = FL_C + 256 * 64, FL_TMO = 64;
constexpr unsigned WS_KMAX = 128u << 10, WS_STAT = 256u << 10;
constexpr unsigned WS_Q = 1u << 20, WS_KV = 3u << 20  , WS_O = 7u << 20, WS_HF = 9u << 20, WS_ACT = 11u << 20;
constexpr unsigned WS_USED = 19u << 20;

struct Params { const u16 *x, *wqkv, *wo, *w1, *b1, *w2; char* ws; u16* out; };

template <int N> using IC = std::integral_constant<int, N>;
template <int B, int E, class Fn> DI void sfor(Fn&& f) { if constexpr (B < E) { f(IC<B>{}); sfor<B + 1, E>(f); } }

DI int rfl(int v) { return __builtin_amdgcn_readfirstlane(v); }
DI unsigned cvtpk(float lo, float hi) { const f32x2 v = {lo, hi}; return __builtin_bit_cast(unsigned, __builtin_convertvector(v, bf16x2_t)); }
DI float bf_lo(unsigned u) { return __uint_as_float(u << 16); }
DI float bf_hi(unsigned u) { return __uint_as_float(u & 0xffff0000u); }
DI float dot2(unsigned a, unsigned b, float c) {
  return __builtin_amdgcn_fdot2_f32_bf16(__builtin_bit_cast(bf16x2_t, a), __builtin_bit_cast(bf16x2_t, b), c, false);
}
constexpr unsigned ONES2 = 0x3F803F80u;
#define MFMA16(a, b, c) __builtin_amdgcn_mfma_f32_16x16x32_bf16((a), (b), (c), 0, 0, 0)
DI f32x2 unpk(unsigned u) { return f32x2{__uint_as_float(u << 16), __uint_as_float(u & 0xffff0000u)}; }
DI bf16x8 rowsum_sel(int l) { const unsigned on = ((l & 15) == ((l >> 4) & 1)) ? ONES2 : 0u; return __builtin_bit_cast(bf16x8, u32x4{on, on, on, on}); }
DI void dma16(const void* sbase, unsigned voff, unsigned lds) {
  asm volatile("s_mov_b32 m0, %2\n\ts_nop 0\n\tglobal_load_lds_dwordx4 %0, %1" ::"v"(voff), "s"(sbase), "s"(lds) : "memory");
}
DI void dma16_sc1(const void* sbase, unsigned voff, unsigned lds) {
  asm volatile("s_mov_b32 m0, %2\n\ts_nop 0\n\tglobal_load_lds_dwordx4 %0, %1 sc1" ::"v"(voff), "s"(sbase), "s"(lds) : "memory");
}
template <int IMM, bool SC1> DI void gload16(bf16x8& dst, const void* sbase, unsigned voff) {
  if constexpr (SC1) asm volatile("global_load_dwordx4 %0, %1, %2 offset:%3 sc1" : "=v"(dst) : "v"(voff), "s"(sbase), "i"(IMM) : "memory");
  else asm volatile("global_load_dwordx4 %0, %1, %2 offset:%3" : "=v"(dst) : "v"(voff), "s"(sbase), "i"(IMM) : "memory");
}
template <int IMM, bool SC1 = false> DI void gload8(u32x2& dst, const void* sbase, unsigned voff) {
  if constexpr (SC1) asm volatile("global_load_dwordx2 %0, %1, %2 offset:%3 sc1" : "=v"(dst) : "v"(voff), "s"(sbase), "i"(IMM) : "memory");
  else asm volatile("global_load_dwordx2 %0, %1, %2 offset:%3" : "=v"(dst) : "v"(voff), "s"(sbase), "i"(IMM) : "memory");
}
template <bool SC1> DI void gload4(float& dst, const void* sbase, unsigned voff) {
  if constexpr (SC1) asm volatile("global_load_dword %0, %1, %2 sc1" : "=v"(dst) : "v"(voff), "s"(sbase) : "memory");
  else asm volatile("global_load_dword %0, %1, %2" : "=v"(dst) : "v"(voff), "s"(sbase) : "memory");
}
typedef __attribute__((ext_vector_type(4))) unsigned v4u_;
typedef __attribute__((ext_vector_type(2))) unsigned v2u_;
struct WsBuf { __amdgpu_buffer_rsrc_t r; };
DI __amdgpu_buffer_rsrc_t make_rsrc(void* p, unsigned bytes) { return __builtin_amdgcn_make_buffer_rsrc(p, 0, bytes, 0x00020000); }
template <bool SC1> DI void st16(__amdgpu_buffer_rsrc_t r, unsigned off, bf16x8 v) { __builtin_amdgcn_raw_buffer_store_b128(__builtin_bit_cast(v4u_, v), r, off, 0, SC1 ? 16 : 0); }
template <bool SC1> DI void st8(__amdgpu_buffer_rsrc_t r, unsigned off, u32x2 v) { __builtin_amdgcn_raw_buffer_store_b64(v, r, off, 0, SC1 ? 16 : 0); }
template <bool SC1> DI void st4(__amdgpu_buffer_rsrc_t r, unsigned off, unsigned v) { __builtin_amdgcn_raw_buffer_store_b32(v, r, off, 0, SC1 ? 16 : 0); }

template <int N> DI void wait_bar() { asm volatile("s_waitcnt vmcnt(%0) lgkmcnt(0)\n\ts_barrier" ::"i"(N) : "memory"); }
DI void lds_bar() { asm volatile("s_waitcnt lgkmcnt(0)\n\ts_barrier" ::: "memory"); }
DI s16x4 trread(const LAS char* p) {
  typedef short v4i16_t __attribute__((ext_vector_type(4)));
  return __builtin_bit_cast(s16x4, __builtin_amdgcn_ds_read_tr16_b64_v4i16((LAS v4i16_t*)p));
}
DI bf16x8 cat8(s16x4 a, s16x4 b) { return __builtin_shufflevector(a, b, 0, 1, 2, 3, 4, 5, 6, 7); }
DI bf16x8 pack8(float a0, float a1, float a2, float a3, float a4, float a5, float a6, float a7) {
  u32x4 w = {cvtpk(a0, a1), cvtpk(a2, a3), cvtpk(a4, a5), cvtpk(a6, a7)};
  return __builtin_bit_cast(bf16x8, w);
}
template <int CTRL> DI float dpp(float v) { return __uint_as_float(__builtin_amdgcn_update_dpp(0u, __float_as_uint(v), CTRL, 0xf, 0xf, true)); }
DI float sum8(float v) { v += dpp<0xB1>(v); v += dpp<0x4E>(v); v += dpp<0x141>(v); return v; }
DI float sumx32(float v) { auto r = __builtin_amdgcn_permlane32_swap(__float_as_uint(v), __float_as_uint(v), false, false); return __uint_as_float(r[0]) + __uint_as_float(r[1]); }
DI float maxx32(float v) { auto r = __builtin_amdgcn_permlane32_swap(__float_as_uint(v), __float_as_uint(v), false, false); return fmaxf(__uint_as_float(r[0]), __uint_as_float(r[1])); }
DI float maxx16(float v) { auto r = __builtin_amdgcn_permlane16_swap(__float_as_uint(v), __float_as_uint(v), false, false); return fmaxf(__uint_as_float(r[0]), __uint_as_float(r[1])); }
DI float max16(float v) { v = fmaxf(v, dpp<0xB1>(v)); v = fmaxf(v, dpp<0x4E>(v)); v = fmaxf(v, dpp<0x141>(v)); v = fmaxf(v, dpp<0x140>(v)); return v; }

DI void publish(char* ws, unsigned flag_off, unsigned epoch) {
  asm volatile("s_waitcnt vmcnt(0) lgkmcnt(0)\n\ts_barrier" ::: "memory");
  if (threadIdx.x == 0) __hip_atomic_store((GAS unsigned*)(ws + flag_off), epoch, __ATOMIC_RELAXED, __HIP_MEMORY_SCOPE_AGENT);
}
DI void await(char* ws, unsigned flag_off, int n, unsigned epoch) {
  if (threadIdx.x >= 448) {
    const int l = threadIdx.x & 63;
    GAS unsigned* f = (GAS unsigned*)(ws + flag_off + (l < n ? l : 0) * 64);
    for (unsigned spins = 0; spins < (1u << 20); ++spins) {
      const unsigned v = __hip_atomic_load(f, __ATOMIC_RELAXED, __HIP_MEMORY_SCOPE_AGENT);
      if (__all(v == epoch)) break;
      __builtin_amdgcn_s_sleep(1);
    }
  }
  asm volatile("s_barrier" ::: "memory");
}

struct Ctx { LAS char* lds; unsigned lds0; int tid, l, w, hh, b; unsigned epoch; };

template <bool MG> DI void phaseA(const Params& p, Ctx& c) {
  STAMP_DECL STAMP_AT(0);
  char* ws = p.ws;
  LAS char* lds = c.lds;
  const int l = c.l, w = c.w, hh = c.hh, b = c.b;
  const int rb = 8 * ((b & 7) >> 1) + ((b >> 3) & 7), hg = 4 * (b & 1) + (b >> 6);
  if constexpr (MG) {
    if (w == 6) {
      unsigned long long pc; unsigned len;
      asm volatile("s_getpc_b64 %0\n.Lmega_here%=:\n\ts_mov_b32 %1, mega_code_end-.Lmega_here%=" : "=s"(pc), "=s"(len));
      const unsigned o0 = (unsigned)(b >> 3) * 1024u + l * 16u, o1 = o0 + 32768u;
      dma16((const void*)pc, o0 + 16 <= len ? o0 : 0u, c.lds0 + 147456);
      dma16((const void*)pc, o1 + 16 <= len ? o1 : 0u, c.lds0 + 147456);
    }
    if (w == 7) {
      const unsigned pg = (unsigned)(b >> 3) * 10u + (l < 10 ? l : 0);
      asm volatile("s_mov_b32 m0, %2\n\ts_nop 0\n\tglobal_load_lds_dword %0, %1" ::"v"(pg * 65536u < WS_USED ? pg * 65536u : 0u), "s"(ws), "s"(c.lds0 + 148480) : "memory");
      asm volatile("s_mov_b32 m0, %2\n\ts_nop 0\n\tglobal_load_lds_dword %0, %1" ::"v"((unsigned)(b >> 3) * 65536u), "s"(p.out), "s"(c.lds0 + 148480) : "memory");
    }
    dma16((const char*)p.x + (size_t)b * 8192, w * 1024 + l * 16, c.lds0 + 4 * 32768 + w * 1024);
    dma16((const char*)p.wqkv + (size_t)b * 6144, (w < 6 ? w : 5) * 1024 + l * 16, c.lds0 + 4 * 32768 + 8192 + w * 1024);
  }
  const unsigned xoff = (unsigned)(64 * rb + 8 * w + (l >> 3)) * 1024u + (unsigned)(((l & 7) ^ ((4 * w + (l >> 4)) & 7)) * 16);
  const unsigned woff = (unsigned)(8 * w + (l >> 3)) * 3072u + (unsigned)hg * 128u + (unsigned)(((l & 7) ^ (((l >> 4) & 1) << 2)) * 16);
  auto issue = [&](int st) {
    const unsigned dst = c.lds0 + (st % 5) * 32768 + w * 1024;
    dma16((const char*)p.x + st * 128, xoff, dst);
    dma16((const char*)p.wqkv + (size_t)st * 64 * 3072, woff, dst + 8192);
    dma16((const char*)p.wqkv + (size_t)st * 64 * 3072 + 1024, woff, dst + 16384);
    dma16((const char*)p.wqkv + (size_t)st * 64 * 3072 + 2048, woff, dst + 24576);
  };
  issue(0);
  asm volatile("s_barrier" ::: "memory");
  issue(1); issue(2); issue(3);
  if constexpr (MG) asm volatile("s_waitcnt vmcnt(16)\n\ts_barrier" ::: "memory");
  issue(4);
  STAMP_AT(1);
  const int q4 = (l & 15) >> 2, p4 = l & 3, blk = (l >> 4) & 1;
  LAS float* tmean = (LAS float*)(lds + 0);
  LAS float* trstd = tmean + 64;
  LAS float* tcs = tmean + 128;
  auto stage_sync = [&](auto ST) {
    constexpr int st = ST.value;
    constexpr int last = 4 + st > 7 ? 7 : 4 + st;
    wait_bar<4 * (last - (st + 1))>();
    if constexpr (st + 5 < 8) issue(st + 5);
  };
  f32x16 acc[2] = {};
  f32x2 rs = {0.f, 0.f}, rq = {0.f, 0.f};
  auto row_stats = [&](u32x4 sv) {
#pragma unroll
    for (int i = 0; i < 4; ++i) { const f32x2 v = unpk(sv[i]); rs += v; rq = __builtin_elementwise_fma(v, v, rq); }
    asm volatile("" : "+v"(rs), "+v"(rq));
  };
  const int ty = w >> 1, ct = w & 1;
  if (w < 6) {
    const bf16x8 sel = rowsum_sel(l);
    const int swz = (l >> 1) & 7;
    const int xrow = (l & 31) * 128;
    const int wa = 8192 + ty * 8192 + (8 * hh + q4) * 128 + (((4 * ct + 2 * blk + (p4 >> 1)) ^ (((q4 >> 1) & 1) << 2)) * 16) + (p4 & 1) * 8;
    bf16x8 xf[2][4][2], wf[2][4]; u32x4 sv[2];
    f32x4 cs = {};
    auto load_frags = [&](auto SET, int st) {
      constexpr int q = SET.value;
      const LAS char* sl = lds + (st % 5) * 32768;
#pragma unroll
      for (int i = 0; i < 4; ++i) {
        wf[q][i] = cat8(trread(sl + wa + i * 2048), trread(sl + wa + i * 2048 + 512));
        xf[q][i][0] = *(const LAS bf16x8*)(sl + xrow + (((2 * i + hh) ^ swz) * 16));
        xf[q][i][1] = *(const LAS bf16x8*)(sl + 4096 + xrow + (((2 * i + hh) ^ swz) * 16));
      }
      sv[q] = *(const LAS u32x4*)(sl + w * 1024 + l * 16);
    };
    auto run = [&](auto SWAP) {
      constexpr bool vt = SWAP.value;
      wait_bar<16>();
      STAMP_AT(2);
      load_frags(IC<0>{}, 0);
      sfor<0, 8>([&](auto ST) {
        constexpr int st = ST.value, q = st & 1;
        if constexpr (st + 1 < 8) stage_sync(ST);
        SBAR();
#pragma unroll
        for (int i = 0; i < 2; ++i) {
#pragma unroll
          for (int t = 0; t < 2; ++t) acc[t] = vt ? MFMA(xf[q][i][t], wf[q][i], acc[t]) : MFMA(wf[q][i], xf[q][i][t], acc[t]);
          cs = MFMA16(wf[q][i], sel, cs);
        }
        SBAR();
        if constexpr (st + 1 < 8) load_frags(IC<(st + 1) & 1>{}, st + 1);
        SBAR();
#pragma unroll
        for (int i = 2; i < 4; ++i) {
#pragma unroll
          for (int t = 0; t < 2; ++t) acc[t] = vt ? MFMA(xf[q][i][t], wf[q][i], acc[t]) : MFMA(wf[q][i], xf[q][i][t], acc[t]);
          cs = MFMA16(wf[q][i], sel, cs);
        }
        row_stats(sv[q]);
        SBAR();
      });
    };
    if (ty == 2) run(IC<1>{}); else run(IC<0>{});
    if ((l & 15) < 2) *(LAS f32x4*)(tcs + w * 32 + 16 * (l & 15) + 4 * (l >> 4)) = cs;
  } else {
    u32x4 sv[2];
    wait_bar<16>();
    sv[0] = *(const LAS u32x4*)(lds + w * 1024 + l * 16);
    sfor<0, 8>([&](auto ST) {
      constexpr int st = ST.value;
      if constexpr (st + 1 < 8) { stage_sync(ST); sv[(st + 1) & 1] = *(const LAS u32x4*)(lds + ((st + 1) % 5) * 32768 + w * 1024 + l * 16); }
      row_stats(sv[st & 1]);
    });
  }
  STAMP_AT(4);
  if constexpr (MG) { asm volatile("" : "+v"(c.epoch)); c.epoch += 1u; }
  { const float s1 = sum8(rs[0] + rs[1]), s2 = sum8(rq[0] + rq[1]);
    if ((l & 7) == 0) { const float mean = s1 * (1.f / D); tmean[8 * w + (l >> 3)] = mean; trstd[8 * w + (l >> 3)] = __builtin_amdgcn_rsqf(fmaxf(s2 * (1.f / D) - mean * mean, 0.f) + LN_EPS); } }
  lds_bar();
  const __amdgpu_buffer_rsrc_t rw = make_rsrc(ws, WS_USED);
  if (w < 4) {
    f32x4 cv[4];
#pragma unroll
    for (int g = 0; g < 4; ++g) cv[g] = *(const LAS f32x4*)(tcs + w * 32 + 8 * g + 4 * hh);
#pragma unroll
    for (int t = 0; t < 2; ++t) {
      const int tile = 2 * rb + t;
      const float mean_l = tmean[32 * t + (l & 31)], rstd_l = trstd[32 * t + (l & 31)];
      const float sc = ty == 0 ? rstd_l * QSCALE : rstd_l, nm = -mean_l * sc;
      const unsigned dst = (ty == 0 ? WS_Q + (unsigned)tile * 1024 : WS_KV + (unsigned)tile * 2048) + l * 16;
      const unsigned hstride = ty == 0 ? 65536u : 131072u;
#pragma unroll
      for (int s = 0; s < 2; ++s) {
        f32x2 v[4];
#pragma unroll
        for (int j = 0; j < 4; ++j) { const int r = 8 * s + 2 * j; const f32x2 a = {acc[t][r], acc[t][r + 1]}, cc = {cv[r >> 2][r & 3], cv[r >> 2][(r & 3) + 1]};
          v[j] = __builtin_elementwise_fma(a, f32x2{sc, sc}, cc * nm); }
        const int head = 4 * hg + 2 * ct + s;
        st16<MG>(rw, dst + (unsigned)head * hstride, pack8(v[0][0], v[0][1], v[1][0], v[1][1], v[2][0], v[2][1], v[3][0], v[3][1]));
        if (ty == 1) {
          f32x2 n2 = v[0] * v[0]; n2 = __builtin_elementwise_fma(v[1], v[1], n2); n2 = __builtin_elementwise_fma(v[2], v[2], n2); n2 = __builtin_elementwise_fma(v[3], v[3], n2);
          const float mx = maxx16(max16(sumx32(n2[0] + n2[1])));
          if (l == 0) st4<MG>(rw, WS_KMAX + (unsigned)(head * 64 + tile) * 4, __float_as_uint(mx));
        }
      }
    }
  } else if (w < 6) {
    const int n = l & 31, head = 4 * hg + 2 * ct + (n >> 4);
    const float ncs = -tcs[w * 32 + n];
#pragma unroll
    for (int t = 0; t < 2; ++t) {
      const int tile = 2 * rb + t;
      f32x4 mm[4], rr[4];
#pragma unroll
      for (int g = 0; g < 4; ++g) { mm[g] = *(const LAS f32x4*)(tmean + 32 * t + 8 * g + 4 * hh); rr[g] = *(const LAS f32x4*)(trstd + 32 * t + 8 * g + 4 * hh); }
#pragma unroll
      for (int s = 0; s < 2; ++s) {
        f32x2 v[4];
#pragma unroll
        for (int j = 0; j < 4; ++j) { const int r = 8 * s + 2 * j; const f32x2 a = {acc[t][r], acc[t][r + 1]}, m2 = {mm[r >> 2][r & 3], mm[r >> 2][(r & 3) + 1]}, r2 = {rr[r >> 2][r & 3], rr[r >> 2][(r & 3) + 1]};
          v[j] = __builtin_elementwise_fma(m2, f32x2{ncs, ncs}, a) * r2; }
        st16<MG>(rw, WS_KV + (unsigned)(head * 64 + tile) * 2048 + 1024 + s * 512 + hh * 256 + (n & 15) * 16,
                 pack8(v[0][0], v[0][1], v[1][0], v[1][1], v[2][0], v[2][1], v[3][0], v[3][1]));
      }
    }
  }
  STAMP_AT(5);
  if constexpr (MG) publish(ws, FL_A + (hg * 32 + rb) * 64, c.epoch);
  STAMP_AT(6);
  STAMP_OUT(0, 7);
}

DI unsigned wpiece_lane(int l, unsigned row_bytes) { return (unsigned)(l >> 3) * row_bytes + (unsigned)(((l & 7) ^ (((l >> 4) & 1) << 2)) * 16); }
template <int NW> DI void prefetch_wo(const Params& p, const Ctx& c) {
  if (c.w >= NW) return;
  const unsigned off = wpiece_lane(c.l, 1024) + (unsigned)(c.b >> 5) * 128u;
  for (int pc = c.w; pc < 64; pc += NW) dma16((const char*)p.wo + (size_t)(8 * pc) * 1024, off, c.lds0 + (pc >> 5) * 65536 + (pc & 31) * 1024);
}
DI constexpr unsigned w1_stage_base(int j) { return j < 4 ? 32768 + (j & 1) * 65536 + (j >> 1) * 16384 : j < 6 ? 131072 + (j - 4) * 16384 : (j - 6) * 16384; }
template <int NW> DI void prefetch_w1(const Params& p, const Ctx& c, int p0, int p1) {
  if (c.w >= NW) return;
  const unsigned off = wpiece_lane(c.l, F * 2) + (unsigned)(c.b >> 4) * 256u;
  for (int pc = p0 + c.w; pc < p1; pc += NW) {
    const int j = pc >> 4, seg = (pc >> 3) & 1, r8 = pc & 7;
    dma16((const char*)p.w1 + (size_t)(64 * j + 8 * r8) * (F * 2) + seg * 128, off, c.lds0 + w1_stage_base(j) + seg * 8192 + r8 * 1024);
  }
}
template <int NW> DI void prefetch_w2(const Params& p, const Ctx& c, int p0, int p1) {
  if (c.w >= NW) return;
  const unsigned off = wpiece_lane(c.l, 1024) + (unsigned)(c.b >> 5) * 128u;
  for (int pc = p0 + c.w; pc < p1; pc += NW) {
    const int st = pc >> 4, kq = (pc >> 2) & 3, r8 = pc & 3;
    dma16((const char*)p.w2 + (size_t)(448 * kq + 32 * st + 8 * r8) * 1024, off, c.lds0 + (st % 10) * 16384 + kq * 4096 + r8 * 1024);
  }
}

DI void warm_l2(const Params& p, const Ctx& c) {
  if (c.w >= 4) return;
  const unsigned off = (unsigned)(c.b >> 3) * 57344u + (unsigned)c.w * 14336u + (unsigned)c.l * 16u;
#pragma unroll
  for (int i = 0; i < 14; ++i) dma16((const char*)p.w2 + i * 1024, off, c.lds0 + 139520);
  const unsigned off1 = (unsigned)c.b * 7168u + (unsigned)c.w * 2048u + (unsigned)c.l * 16u;
  dma16((const char*)p.w1, off1, c.lds0 + 139520);
  if (c.w < 3) dma16((const char*)p.w1 + 1024, off1, c.lds0 + 139520);
}

constexpr int B_ONES = 131072;
template <bool MG> DI void phaseB(const Params& p, const Ctx& c) {
  STAMP_DECL STAMP_AT(0);
  char* ws = p.ws;
  LAS char* lds = c.lds;
  const int l = c.l, w = c.w, hh = c.hh, b = c.b, tid = c.tid;
  const int head = 4 * (b & 7) + ((b >> 3) & 3), qb = b >> 5;
  const int pr = w & 3, kh = w >> 2;
  for (int i = tid; i < (4096 + 512) / 16; i += 512) { const unsigned v = (i & 31) == 0 ? ONES2 : 0u; ((LAS u32x4*)(lds + B_ONES))[i] = u32x4{v, v, v, v}; }
  if constexpr (MG) await(ws, FL_A + (head >> 2) * 32 * 64, 32, c.epoch);
  STAMP_AT(1);
  const char* kvg = ws + WS_KV + (size_t)head * 131072 + kh * 65536 + pr * 4096;
  unsigned kvl = c.lds0 + kh * 65536 + pr * 4096;
  const unsigned l16 = l * 16;
#define KV_DMA4(SC) asm volatile("s_mov_b32 m0, %2\n\ts_nop 0\n\tglobal_load_lds_dwordx4 %0, %1" SC "\n\tglobal_load_lds_dwordx4 %0, %1 offset:1024" SC \
                "\n\tglobal_load_lds_dwordx4 %0, %1 offset:2048" SC "\n\tglobal_load_lds_dwordx4 %0, %1 offset:3072" SC ::"v"(l16), "s"(kvg), "s"(kvl) : "memory")
  if constexpr (MG) KV_DMA4(" sc1"); else KV_DMA4("");
  bf16x8 qa, qb_;
  float kmx;
  gload16<0, MG>(qa, ws + WS_Q + (size_t)(head * 64 + qb * 8 + pr) * 1024, l * 16);
  gload16<0, MG>(qb_, ws + WS_Q + (size_t)(head * 64 + qb * 8 + pr + 4) * 1024, l * 16);
  gload4<MG>(kmx, ws + WS_KMAX + head * 256, l * 4);
  asm volatile("s_waitcnt vmcnt(0)" : "+v"(qa), "+v"(qb_), "+v"(kmx)::"memory");
  kmx = maxx32(maxx16(max16(kmx)));
  auto qbound = [&](bf16x8 q) { const u32x4 u = __builtin_bit_cast(u32x4, q); f32x2 t = {0.f, 0.f}; for (int i = 0; i < 4; ++i) { const f32x2 v = unpk(u[i]); t = __builtin_elementwise_fma(v, v, t); }
    return __builtin_amdgcn_sqrtf(sumx32(t[0] + t[1]) * kmx) * 1.01f + 1e-6f; };
  const float bna = qbound(qa), bnb = qbound(qb_);
  const bool safe = __all(bna <= BOUND_SAFE && bnb <= BOUND_SAFE);
  f32x16 oa = {}, ob = {};
  float ca = bna, cb = bnb;
  const int lo5 = l & 31;
  const bool isv = lo5 < 16;
  const int va0 = isv ? kh * 65536 + 1024 + hh * 256 + lo5 * 16 : B_ONES + (lo5 == 16 ? 0 : (lo5 >= 20 && lo5 < 28) ? 64 : 16);
  lds_bar();
  STAMP_AT(2);
  auto mid_prefetch = [&]() { if constexpr (MG) { prefetch_wo<4>(p, c); if (b < 224) prefetch_w1<4>(p, c, 80, 96); } };
  auto late_prefetch = [&]() { if constexpr (MG) { if (b < 224) prefetch_w1<4>(p, c, 0, 32); } };
  if (safe) {
    f32x16 nga, ngb; for (int i = 0; i < 16; ++i) { nga[i] = -bna; ngb[i] = -bnb; }
    f32x16 s0, s1;
    bf16x8 pa, pb = {};
    bf16x8 kf, v0, v1 = {};
    unsigned ka = c.lds0 + kh * 65536 + l * 16, va = c.lds0 + va0;
    const unsigned vstep = isv ? 4096u : 0u;
    asm volatile("ds_read_b128 %1, %3\n\tds_read_b128 %2, %4\n\ts_waitcnt lgkmcnt(0)\n\t"
                 "v_mfma_f32_32x32x16_bf16 %0, %1, %5, %6\n\ts_nop 7\n\ts_nop 3"
                 : "=&{v[192:207]}"(s0), "=&v"(kf), "=&v"(v0) : "v"(ka), "v"(va), "v"(qa), "v"(nga) : "memory");
#define E_(b, i) "v_exp_f32 v[" #b "+" #i "], v[" #b "+" #i "]\n\t"
#define C_(d, i, b, j, k) "v_cvt_pk_bf16_f32 v[" #d "+" #i "], v[" #b "+" #j "], v[" #b "+" #k "]\n\t"
#define MID_(SC) E_(SC, 4) E_(SC, 5) E_(SC, 6) E_(SC, 7) C_(224, 0, SC, 0, 1) C_(224, 1, SC, 2, 3) E_(SC, 8) E_(SC, 9) C_(224, 2, SC, 4, 5) C_(224, 3, SC, 6, 7) E_(SC, 10) E_(SC, 11)
#define END_(SC) E_(SC, 12) E_(SC, 13) E_(SC, 14) E_(SC, 15) C_(228, 0, SC, 8, 9) C_(228, 1, SC, 10, 11) C_(228, 2, SC, 12, 13) C_(228, 3, SC, 14, 15)
#define UNIT_A(KOFF, V1OFF, X0, X1) \
      "v_mfma_f32_32x32x16_bf16 v[208:223], %[kf], %[qb], %[ngb]\n\t" \
      "ds_read_b128 %[kf], %[ka] offset:" #KOFF "\n\t" \
      E_(192, 0) E_(192, 1) E_(192, 2) E_(192, 3) \
      "v_mfma_f32_32x32x16_bf16 %[ob], %[v1], v[228:231], %[ob]\n\t" \
      "ds_read_b128 %[v1], %[va] offset:" #V1OFF "\n\t" \
      X0 MID_(192) \
      "s_waitcnt lgkmcnt(2)\n\t" \
      "v_mfma_f32_32x32x16_bf16 %[oa], %[v0], v[224:227], %[oa]\n\t" \
      X1 END_(192)
#define UNIT_B(V0OFF, X0, X1) \
      "s_waitcnt lgkmcnt(0)\n\t" \
      "v_mfma_f32_32x32x16_bf16 v[192:207], %[kf], %[qa], %[nga]\n\t" \
      E_(208, 0) E_(208, 1) E_(208, 2) E_(208, 3) \
      "v_mfma_f32_32x32x16_bf16 %[oa], %[v1], v[228:231], %[oa]\n\t" \
      X0 MID_(208) \
      "v_mfma_f32_32x32x16_bf16 %[ob], %[v0], v[224:227], %[ob]\n\t" \
      "ds_read_b128 %[v0], %[va] offset:" #V0OFF "\n\t" \
      X1 END_(208)
#define GROUP_(A0, A1, B0, B1, A2, A3, B2, B3) UNIT_A(2048, 512, A0, A1) UNIT_B(2048, B0, B1) UNIT_A(4096, 2560, A2, A3) UNIT_B(4096, B2, B3) \
        "v_add_u32 %[ka], 0x1000, %[ka]\n\tv_add_u32 %[va], %[va], %[vs]\n\t"
#define LOOP_TAIL_ "s_sub_u32 %[cnt], %[cnt], 1\n\ts_cmp_lg_u32 %[cnt], 0\n\ts_cbranch_scc1 1b\n\ts_waitcnt lgkmcnt(0)\n\ts_nop 7\n\ts_nop 3"
#define ATT_OUT_ "+{v[192:207]}"(s0), "=&{v[208:223]}"(s1), "=&{v[224:227]}"(pa), "+{v[228:231]}"(pb), [oa] "+v"(oa), [ob] "+v"(ob), [kf] "+v"(kf), [v0] "+v"(v0), [v1] "+v"(v1), \
          [ka] "+v"(ka), [va] "+v"(va)
#define ATT_IN_ [qa] "v"(qa), [qb] "v"(qb_), [nga] "v"(nga), [ngb] "v"(ngb), [vs] "v"(vstep)
#define RUN_GROUPS(NG) do { int cnt_ = (NG); \
      asm volatile("1:\n\t" GROUP_("", "", "", "", "", "", "", "") LOOP_TAIL_ : ATT_OUT_, [cnt] "+s"(cnt_) : ATT_IN_ : "memory", "scc"); } while (0)
    {
      unsigned ml = kvl; unsigned long long gb = (unsigned long long)kvg;
#define NXT_ "s_add_u32 s60, s60, 0x4000\n\ts_addc_u32 s61, s61, 0\n\ts_add_u32 %[ml], %[ml], 0x4000\n\ts_mov_b32 m0, %[ml]\n\ts_nop 0\n\t"
#define DM_(OFF, SC) "global_load_lds_dwordx4 %[l16], s[60:61] offset:" #OFF SC "\n\t"
#define STG_(SC) asm volatile(GROUP_(NXT_ DM_(0, SC) DM_(1024, SC), DM_(2048, SC), DM_(3072, SC), NXT_ DM_(0, SC) DM_(1024, SC), DM_(2048, SC) DM_(3072, SC), NXT_ DM_(0, SC), DM_(1024, SC) DM_(2048, SC), DM_(3072, SC)) \
        "s_waitcnt lgkmcnt(0)\n\ts_nop 7\n\ts_nop 3" \
        : ATT_OUT_, [ml] "+s"(ml), "+{s[60:61]}"(gb) : ATT_IN_, [l16] "v"(l16) : "memory", "scc")
      if constexpr (MG) STG_(" sc1"); else STG_("");
    }
    RUN_GROUPS(2);
    wait_bar<0>();
    STAMP_AT(3);
    if constexpr (MG) warm_l2(p, c);
    RUN_GROUPS(5);
    lds_bar();
    mid_prefetch();
    RUN_GROUPS(4);
    lds_bar();
    late_prefetch();
    RUN_GROUPS(4);
    ob = MFMA(v1, pb, ob);
  } else {
    for (int j = 1; j < 4; ++j) { kvg += 16384; kvl += 16384; const char* kvg_ = kvg; const unsigned kvl_ = kvl;
      if constexpr (MG) { dma16_sc1(kvg_, l16, kvl_); dma16_sc1(kvg_ + 1024, l16, kvl_ + 1024); dma16_sc1(kvg_ + 2048, l16, kvl_ + 2048); dma16_sc1(kvg_ + 3072, l16, kvl_ + 3072); }
      else { dma16(kvg_, l16, kvl_); dma16(kvg_ + 1024, l16, kvl_ + 1024); dma16(kvg_ + 2048, l16, kvl_ + 2048); dma16(kvg_ + 3072, l16, kvl_ + 3072); } }
    ca = -3.0e38f; cb = -3.0e38f;
    const f32x16 zero = {};
    auto online = [&](const bf16x8& kf, const bf16x8& v0, const bf16x8& v1, const bf16x8& q, f32x16& o, float& mrun) {
      f32x16 s = MFMA(kf, q, zero);
      float tm = s[0];
#pragma unroll
      for (int i = 1; i < 16; ++i) tm = fmaxf(tm, s[i]);
      tm = maxx32(tm);
      const float mn = fmaxf(mrun, tm), f = __builtin_amdgcn_exp2f(mrun - mn);
      mrun = mn;
#pragma unroll
      for (int i = 0; i < 16; ++i) { o[i] *= f; s[i] = __builtin_amdgcn_exp2f(s[i] - mn); }
      o = MFMA(v0, pack8(s[0], s[1], s[2], s[3], s[4], s[5], s[6], s[7]), o);
      o = MFMA(v1, pack8(s[8], s[9], s[10], s[11], s[12], s[13], s[14], s[15]), o);
    };
    for (int t = 0; t < 32; ++t) {
      if (t == 6) wait_bar<0>();
      if (t == 16) { lds_bar(); mid_prefetch(); }
      if (t == 24) { lds_bar(); late_prefetch(); }
      const bf16x8 kf = *(const LAS bf16x8*)(lds + kh * 65536 + l * 16 + t * 2048);
      const int vo = isv ? va0 + t * 2048 : va0;
      const bf16x8 v0 = *(const LAS bf16x8*)(lds + vo), v1 = *(const LAS bf16x8*)(lds + vo + 512);
      online(kf, v0, v1, qa, oa, ca);
      online(kf, v0, v1, qb_, ob, cb);
    }
  }
  STAMP_AT(4);
  oa[8] = sumx32(oa[8]); ob[8] = sumx32(ob[8]);
  {
    const f32x16& snd = kh == 0 ? ob : oa;
    LAS f32x4* x4 = (LAS f32x4*)(lds + 49152 + kh * 65536) + pr * 192;
    lds_bar();
    x4[l] = f32x4{snd[0], snd[1], snd[2], snd[3]};
    x4[64 + l] = f32x4{snd[4], snd[5], snd[6], snd[7]};
    x4[128 + l] = f32x4{snd[8], kh == 0 ? cb : ca, 0.f, 0.f};
    lds_bar();
    const LAS f32x4* y4 = (const LAS f32x4*)(lds + 49152 + (kh ^ 1) * 65536) + pr * 192;
    const f32x4 y0 = y4[l], y1 = y4[64 + l], y2 = y4[128 + l];
    const f32x16& own = kh == 0 ? oa : ob;
    const float c0 = kh == 0 ? ca : cb, cm = fmaxf(c0, y2[1]);
    const float f0 = __builtin_amdgcn_exp2f(c0 - cm), f1 = __builtin_amdgcn_exp2f(y2[1] - cm);
    const float inv = __builtin_amdgcn_rcpf(own[8] * f0 + y2[0] * f1);
    const float g0 = f0 * inv, g1 = f1 * inv;
    const __amdgpu_buffer_rsrc_t rw = make_rsrc(ws, WS_USED);
    st16<MG>(rw, WS_O + (unsigned)(((qb * 8 + pr + 4 * kh) * 32 + head) * 64 + l) * 16,
             pack8(own[0] * g0 + y0[0] * g1, own[1] * g0 + y0[1] * g1, own[2] * g0 + y0[2] * g1, own[3] * g0 + y0[3] * g1,
                   own[4] * g0 + y1[0] * g1, own[5] * g0 + y1[1] * g1, own[6] * g0 + y1[2] * g1, own[7] * g0 + y1[3] * g1));
  }
  STAMP_AT(5);
  if constexpr (MG) {
    publish(ws, FL_B + (qb * 32 + head) * 64, c.epoch);
    if (b < 224) prefetch_w1<7>(p, c, 32, 80);
  }
  STAMP_AT(6);
  STAMP_OUT(1, 7);
}

template <int KQ, int HGAP> DI void quarter_reduce_t(int m, int l, LAS f32x4* xch, const f32x16 (&acc)[2], float (&val)[8]) {
#pragma unroll
  for (int d = 0; d < 4; ++d) {
    if (d == KQ) continue;
    xch[(d >> 1) * HGAP + (((d * 4 + KQ) * 2 + m) * 2 + 0) * 64 + l] = f32x4{acc[d >> 1][8 * (d & 1)], acc[d >> 1][8 * (d & 1) + 1], acc[d >> 1][8 * (d & 1) + 2], acc[d >> 1][8 * (d & 1) + 3]};
    xch[(d >> 1) * HGAP + (((d * 4 + KQ) * 2 + m) * 2 + 1) * 64 + l] = f32x4{acc[d >> 1][8 * (d & 1) + 4], acc[d >> 1][8 * (d & 1) + 5], acc[d >> 1][8 * (d & 1) + 6], acc[d >> 1][8 * (d & 1) + 7]};
  }
  lds_bar();
  f32x4 t[4][2];
#pragma unroll
  for (int s = 0; s < 4; ++s) { if (s == KQ) continue; t[s][0] = xch[(KQ >> 1) * HGAP + (((KQ * 4 + s) * 2 + m) * 2 + 0) * 64 + l]; t[s][1] = xch[(KQ >> 1) * HGAP + (((KQ * 4 + s) * 2 + m) * 2 + 1) * 64 + l]; }
#pragma unroll
  for (int j = 0; j < 8; ++j) val[j] = acc[KQ >> 1][8 * (KQ & 1) + j];
#pragma unroll
  for (int s = 0; s < 4; ++s) { if (s == KQ) continue; for (int j = 0; j < 4; ++j) { val[j] += t[s][0][j]; val[4 + j] += t[s][1][j]; } }
}
template <int HGAP> DI void quarter_reduce(const Ctx& c, LAS f32x4* xch, const f32x16 (&acc)[2], float (&val)[8]) {
  const int m = c.w & 1, kq = c.w >> 1;
  if (kq == 0) quarter_reduce_t<0, HGAP>(m, c.l, xch, acc, val);
  else if (kq == 1) quarter_reduce_t<1, HGAP>(m, c.l, xch, acc, val);
  else if (kq == 2) quarter_reduce_t<2, HGAP>(m, c.l, xch, acc, val);
  else quarter_reduce_t<3, HGAP>(m, c.l, xch, acc, val);
}

template <bool MG> DI void phaseC(const Params& p, const Ctx& c, u32x4& hkeep) {
  STAMP_DECL STAMP_AT(0);
  char* ws = p.ws;
  LAS char* lds = c.lds;
  const int l = c.l, w = c.w, hh = c.hh, b = c.b;
  const int rb = (b & 7) * 4 + ((b >> 3) & 3), cb = b >> 5;
  const int m = w & 1, kq = w >> 1;
  const int rt = 2 * rb + m, kstep = 4 * cb + kq;
  u32x2 xr0, xr1;
  gload8<0>(xr0, (const char*)p.x + (size_t)rt * 32 * 1024 + kstep * 32, (l & 31) * 1024 + hh * 8);
  gload8<16>(xr1, (const char*)p.x + (size_t)rt * 32 * 1024 + kstep * 32, (l & 31) * 1024 + hh * 8);
  if constexpr (!MG) prefetch_wo<8>(p, c);
  if constexpr (MG) await(ws, FL_B + (rb >> 2) * 32 * 64, 32, c.epoch);
  STAMP_AT(1);
  const char* abase = ws + WS_O + ((size_t)rt * 32 + kq * 8) * 1024;
  bf16x8 af[8];
  sfor<0, 8>([&](auto I) { constexpr int i = I.value; gload16<(i & 3) * 1024, MG>(af[i], abase + (i >> 2) * 4096, l * 16); });
  f32x16 acc[2] = {};
  const int q4 = (l & 15) >> 2, p4 = l & 3, blk = (l >> 4) & 1;
  const int wa0 = (kq >> 1) * 65536 + (128 * (kq & 1) + 4 * hh + q4) * 128 + (((2 * blk + (p4 >> 1)) ^ (((q4 >> 1) & 1) << 2)) * 16) + (p4 & 1) * 8;
  const int wa1 = wa0 ^ 64;
  asm volatile("s_waitcnt vmcnt(8)\n\ts_barrier" : "+v"(xr0), "+v"(xr1)::"memory");
  bf16x8 wf[8][2];
#pragma unroll
  for (int i = 0; i < 8; ++i) {
    wf[i][0] = cat8(trread(lds + wa0 + i * 2048), trread(lds + wa0 + i * 2048 + 1024));
    wf[i][1] = cat8(trread(lds + wa1 + i * 2048), trread(lds + wa1 + i * 2048 + 1024));
  }
  lds_bar();
  STAMP_AT(2);
  sfor<0, 8>([&](auto I) {
    constexpr int i = I.value;
    asm volatile("s_waitcnt vmcnt(%1)" : "+v"(af[i]) : "i"(7 - i) : "memory");
    acc[0] = MFMA(wf[i][0], af[i], acc[0]);
    acc[1] = MFMA(wf[i][1], af[i], acc[1]);
    SBAR();
  });
  STAMP_AT(3);
  float val[8];
  quarter_reduce<2048>(c, (LAS f32x4*)lds, acc, val);
  const u32x4 o = {cvtpk(val[0] + bf_lo(xr0[0]), val[1] + bf_hi(xr0[0])), cvtpk(val[2] + bf_lo(xr0[1]), val[3] + bf_hi(xr0[1])),
                   cvtpk(val[4] + bf_lo(xr1[0]), val[5] + bf_hi(xr1[0])), cvtpk(val[6] + bf_lo(xr1[1]), val[7] + bf_hi(xr1[1]))};
  hkeep = o;
  const __amdgpu_buffer_rsrc_t rw = make_rsrc(ws, WS_USED);
  st16<MG>(rw, WS_HF + (unsigned)((rt * 32 + kstep) * 64 + l) * 16, __builtin_bit_cast(bf16x8, o));
  f32x2 sm = {0.f, 0.f}, sq = {0.f, 0.f};
#pragma unroll
  for (int i = 0; i < 4; ++i) { const f32x2 v = unpk(o[i]); sm += v; sq = __builtin_elementwise_fma(v, v, sq); }
  LAS f32x2* tst = (LAS f32x2*)(lds + 0);
  tst[((m * 4 + kq) * 2 + hh) * 32 + (l & 31)] = f32x2{sm[0] + sm[1], sq[0] + sq[1]};
  lds_bar();
  if (w < 2 && l < 32) {
    f32x2 t8[8];
#pragma unroll
    for (int i = 0; i < 8; ++i) t8[i] = tst[(w * 8 + i) * 32 + l];
    const f32x2 t = ((t8[0] + t8[1]) + (t8[2] + t8[3])) + ((t8[4] + t8[5]) + (t8[6] + t8[7]));
    st8<MG>(rw, WS_STAT + (unsigned)(((2 * rb + w) * 8 + cb) * 32 + l) * 8, __builtin_bit_cast(u32x2, t));
  }
  STAMP_AT(4);
  if constexpr (MG) {
    publish(ws, FL_C + ((rb >> 1) * 16 + (rb & 1) * 8 + cb) * 64, c.epoch);
    if (b < 224) prefetch_w1<7>(p, c, 96, 128);
  }
  STAMP_AT(5);
  STAMP_OUT(2, 6);
}

constexpr int D_WIN = 12;
template <bool MG> DI void phaseD(const Params& p, const Ctx& c) {
  STAMP_DECL STAMP_AT(0);
  char* ws = p.ws;
  LAS char* lds = c.lds;
  const int l = c.l, w = c.w, hh = c.hh, b = c.b;
  const int rb = 2 * (b & 7) + ((b >> 3) & 1), cb = b >> 4;
  const int m = w & 3, nh = w >> 2;
  const int rt = 4 * rb + m;
  LAS float* tcs = (LAS float*)(lds + 65536);
  LAS float* tb = tcs + 128;
  if (c.tid < 128) tcs[c.tid] = 0.f;
  bf16x8 bv;
  gload16<0, false>(bv, (const char*)p.b1 + cb * 256, (l & 15) * 16);
  if constexpr (!MG) prefetch_w1<8>(p, c, 0, 128);
  if constexpr (MG) await(ws, FL_C + rb * 16 * 64, 16, c.epoch);
  STAMP_AT(1);
  u32x2 st[4];
  sfor<0, 4>([&](auto I) { constexpr int i = I.value; gload8<i * 256, MG>(st[i], ws + WS_STAT + (size_t)rt * 2048, (l & 31) * 8 + hh * 1024); });
  const int par = m >> 1;
  const char* abase = ws + WS_HF + (size_t)rt * 32 * 1024;
  const unsigned aoff0 = l * 16 + par * 1024, aoff1 = l * 16 + (1 - par) * 1024;
  bf16x8 af[32];
  auto load_a = [&](auto I) { constexpr int i = I.value;
    if constexpr (i & 1) gload16<((i >> 1) & 1) * 2048, MG>(af[i], abase + (i >> 2) * 4096, aoff1);
    else gload16<((i >> 1) & 1) * 2048, MG>(af[i], abase + (i >> 2) * 4096, aoff0); };
  sfor<0, D_WIN>(load_a);
  f32x16 acc[2] = {};
  f32x4 cs = {};
  const bf16x8 sel = rowsum_sel(l);
  const int q4 = (l & 15) >> 2, p4 = l & 3, blk = (l >> 4) & 1;
  const int wl = nh * 8192 + (4 * hh + q4) * 128 + (((4 * (m & 1) + 2 * blk + (p4 >> 1)) ^ (((q4 >> 1) & 1) << 2)) * 16) + (p4 & 1) * 8;
  const int wa_e = wl + par * 2048, wa_o = wl + (1 - par) * 2048;
  asm volatile("s_waitcnt vmcnt(%1) lgkmcnt(0)\n\ts_barrier" : "+v"(bv) : "i"(D_WIN + 4) : "memory");
  bf16x8 wf[3][2];
  auto load_w = [&](auto I) {
    constexpr int i = I.value;
    constexpr int o = (int)w1_stage_base(i >> 2) + (i & 2) * 2048;
    const int wa = (i & 1) ? wa_o : wa_e;
    wf[i % 3][0] = cat8(trread(lds + wa + o), trread(lds + wa + o + 1024));
    wf[i % 3][1] = cat8(trread(lds + (wa ^ 64) + o), trread(lds + (wa ^ 64) + o + 1024));
  };
  load_w(IC<0>{}); load_w(IC<1>{});
  STAMP_AT(2);
  sfor<0, 32>([&](auto I) {
    constexpr int i = I.value;
    if constexpr (i + D_WIN < 32) load_a(IC<i + D_WIN>{});
    constexpr int issued = i + D_WIN < 32 ? i + D_WIN + 1 : 32;
    asm volatile("s_waitcnt vmcnt(%1)" : "+v"(af[i]) : "i"(issued - i - 1) : "memory");
    acc[0] = MFMA(wf[i % 3][0], af[i], acc[0]);
    acc[1] = MFMA(wf[i % 3][1], af[i], acc[1]);
    if constexpr ((i & 1) == 0) cs = MFMA16(wf[i % 3][0], sel, cs);
    if constexpr (i + 2 < 32) { SBAR(); load_w(IC<i + 2>{}); }
    SBAR();
  });
  STAMP_AT(3);
  if ((l & 15) < 2) {
    LAS float* d = tcs + nh * 64 + (m & 1) * 32 + 16 * (l & 15) + 4 * (l >> 4);
#pragma unroll
    for (int j = 0; j < 4; ++j) __hip_atomic_fetch_add(d + j, cs[j], __ATOMIC_RELAXED, __HIP_MEMORY_SCOPE_WORKGROUP);
  }
  if (w == 0 && l < 16) { const u32x4 u = __builtin_bit_cast(u32x4, bv);
    *(LAS f32x4*)(tb + l * 8) = f32x4{bf_lo(u[0]), bf_hi(u[0]), bf_lo(u[1]), bf_hi(u[1])}; *(LAS f32x4*)(tb + l * 8 + 4) = f32x4{bf_lo(u[2]), bf_hi(u[2]), bf_lo(u[3]), bf_hi(u[3])}; }
  asm volatile("" : "+v"(st[0]), "+v"(st[1]), "+v"(st[2]), "+v"(st[3]));
  const f32x2 ss = __builtin_bit_cast(f32x2, st[0]) + __builtin_bit_cast(f32x2, st[1]) + __builtin_bit_cast(f32x2, st[2]) + __builtin_bit_cast(f32x2, st[3]);
  const float sm = sumx32(ss[0]), sq = sumx32(ss[1]);
  const float mean = sm * (1.f / D), rstd = __builtin_amdgcn_rsqf(fmaxf(sq * (1.f / D) - mean * mean, 0.f) + LN_EPS);
  const float nmr = -mean * rstd;
  lds_bar();
  STAMP_AT(4);
  const __amdgpu_buffer_rsrc_t rw = make_rsrc(ws, WS_USED);
#pragma unroll
  for (int t = 0; t < 2; ++t) {
    const int ct = 2 * nh + ((m & 1) ^ t);
    f32x4 cv[4], bb[4];
#pragma unroll
    for (int g = 0; g < 4; ++g) { cv[g] = *(const LAS f32x4*)(tcs + 32 * ct + 8 * g + 4 * hh); bb[g] = *(const LAS f32x4*)(tb + 32 * ct + 8 * g + 4 * hh); }
#pragma unroll
    for (int s = 0; s < 2; ++s) {
      unsigned pk[4];
#pragma unroll
      for (int j = 0; j < 4; ++j) {
        const int r = 8 * s + 2 * j;
        const f32x2 a = {acc[t][r], acc[t][r + 1]}, c2 = {cv[r >> 2][r & 3], cv[r >> 2][(r & 3) + 1]}, b2 = {bb[r >> 2][r & 3], bb[r >> 2][(r & 3) + 1]};
        const f32x2 y = __builtin_elementwise_fma(a, f32x2{rstd, rstd}, __builtin_elementwise_fma(c2, f32x2{nmr, nmr}, b2));
        const f32x2 z = y * LOG2E;
        const f32x2 u = {__builtin_amdgcn_exp2f(z[0]), __builtin_amdgcn_exp2f(z[1])};
        const f32x2 d = __builtin_elementwise_fma(u, u + 2.f, f32x2{2.f, 2.f});
        const f32x2 r2 = {__builtin_amdgcn_rcpf(d[0]), __builtin_amdgcn_rcpf(d[1])};
        const f32x2 th = __builtin_elementwise_fma(r2, f32x2{-2.f, -2.f}, f32x2{1.f, 1.f});
        const f32x2 o = y * th;
        pk[j] = cvtpk(o[0], o[1]);
      }
      st16<MG>(rw, WS_ACT + (unsigned)((rt * 112 + cb * 8 + ct * 2 + s) * 64 + l) * 16, __builtin_bit_cast(bf16x8, u32x4{pk[0], pk[1], pk[2], pk[3]}));
      if constexpr (MG) {
        const int q = 2 * t + s;
        if (q < 2) prefetch_w2<8>(p, c, 24 * q, 24 * q + 24);
        else if (q == 2) { prefetch_w2<8>(p, c, 48, 64); prefetch_w2<8>(p, c, 80, 88); }
        else prefetch_w2<8>(p, c, 88, 112);
      }
    }
  }
  STAMP_AT(5);
  if constexpr (MG) publish(ws, FL_D + (rb * 16 + cb) * 64, c.epoch);
  STAMP_AT(6);
  STAMP_OUT(3, 7);
}

constexpr int E_NSLOT = 10, E_NST = 14, E_WIN = 12, E_PRE = 8;
struct ESched {
  int posA[28], posW[E_NST], n_top[E_NST], n_mid[E_NST];
  constexpr ESched() : posA(), posW(), n_top(), n_mid() {
    int n = 0;
    for (int i = 0; i < E_WIN; ++i) posA[i] = n++;
    for (int st = 0; st < E_NST; ++st) {
      n_top[st] = n;
      if (E_PRE + st < E_NST) { n += 2; posW[E_PRE + st] = n - 1; }
      for (int j = 0; j < 2; ++j) if (E_WIN + 2 * st + j < 28) posA[E_WIN + 2 * st + j] = n++;
      n_mid[st] = n;
    }
  }
};
template <bool MG> DI void phaseE(const Params& p, const Ctx& c, u32x4 hkeep) {
  STAMP_DECL STAMP_AT(0);
  constexpr ESched sch{};
  char* ws = p.ws;
  LAS char* lds = c.lds;
  const int l = c.l, w = c.w, hh = c.hh, b = c.b;
  const int rb = (b & 7) * 4 + ((b >> 3) & 3), cb = b >> 5;
  const int m = w & 1, kq = w >> 1;
  const int rt = 2 * rb + m, kstep = 4 * cb + kq;
  if (!MG || b >= 224) prefetch_w2<7>(p, c, 0, 16 * E_PRE);
  else { prefetch_w2<7>(p, c, 16 * 4, 16 * 5); prefetch_w2<7>(p, c, 16 * 7, 16 * 8); }
  bf16x8 resv = __builtin_bit_cast(bf16x8, hkeep);
  if constexpr (!MG) gload16<0, false>(resv, ws + WS_HF + (size_t)(rt * 32 + kstep) * 1024, l * 16);
  if constexpr (MG) await(ws, FL_D + (rb >> 1) * 16 * 64, 14, c.epoch);
  STAMP_AT(1);
  const char* abase = ws + WS_ACT + ((size_t)rt * 112 + kq * 28) * 1024;
  bf16x8 af[28];
  auto load_a = [&](auto I) { constexpr int i = I.value; gload16<(i & 3) * 1024, MG>(af[i], abase + (i >> 2) * 4096, l * 16); };
  sfor<0, E_WIN>(load_a);
  f32x16 acc[2] = {};
  const int q4 = (l & 15) >> 2, p4 = l & 3, blk = (l >> 4) & 1;
  const int wa0 = kq * 4096 + (4 * hh + q4) * 128 + (((2 * blk + (p4 >> 1)) ^ (((q4 >> 1) & 1) << 2)) * 16) + (p4 & 1) * 8;
  const int wa1 = wa0 ^ 64;
  bf16x8 wf[3][2][2];
  auto load_w = [&](auto ST) {
    constexpr int st = ST.value;
    const LAS char* sl = lds + (st % E_NSLOT) * 16384;
#pragma unroll
    for (int i = 0; i < 2; ++i) {
      wf[st % 3][i][0] = cat8(trread(sl + wa0 + i * 2048), trread(sl + wa0 + i * 2048 + 1024));
      wf[st % 3][i][1] = cat8(trread(sl + wa1 + i * 2048), trread(sl + wa1 + i * 2048 + 1024));
    }
  };
  asm volatile("s_waitcnt vmcnt(%0)\n\ts_barrier" ::"i"(E_WIN) : "memory");
  load_w(IC<0>{}); load_w(IC<1>{});
  STAMP_AT(2);
  sfor<0, E_NST>([&](auto ST) {
    constexpr int st = ST.value;
    if constexpr (st == 2) asm volatile("s_waitcnt lgkmcnt(0)\n\ts_barrier" ::: "memory");
    if constexpr (st == 6 || st == 8 || st == 10) asm volatile("s_waitcnt vmcnt(%0) lgkmcnt(0)\n\ts_barrier" ::"i"(sch.n_top[st] - sch.posW[st + 3] - 1) : "memory");
    if constexpr (E_PRE + st < E_NST) prefetch_w2<8>(p, c, 16 * (st + E_PRE), 16 * (st + E_PRE + 1));
    if constexpr (E_WIN + 2 * st < 28) load_a(IC<E_WIN + 2 * st>{});
    if constexpr (E_WIN + 2 * st + 1 < 28) load_a(IC<E_WIN + 2 * st + 1>{});
    asm volatile("s_waitcnt vmcnt(%2)" : "+v"(af[2 * st]), "+v"(af[2 * st + 1]) : "i"(sch.n_mid[st] - sch.posA[2 * st + 1] - 1) : "memory");
    acc[0] = MFMA(wf[st % 3][0][0], af[2 * st], acc[0]);
    acc[1] = MFMA(wf[st % 3][0][1], af[2 * st], acc[1]);
    if constexpr (st + 2 < E_NST) { SBAR(); load_w(IC<st + 2>{}); SBAR(); }
    acc[0] = MFMA(wf[st % 3][1][0], af[2 * st + 1], acc[0]);
    acc[1] = MFMA(wf[st % 3][1][1], af[2 * st + 1], acc[1]);
    SBAR();
  });
  STAMP_AT(3);
  float val[8];
  quarter_reduce<0>(c, (LAS f32x4*)(lds + 65536), acc, val);
  asm volatile("" : "+v"(resv));
  const u32x4 ru = __builtin_bit_cast(u32x4, resv);
  const u32x2 o0 = {cvtpk(val[0] + bf_lo(ru[0]), val[1] + bf_hi(ru[0])), cvtpk(val[2] + bf_lo(ru[1]), val[3] + bf_hi(ru[1]))};
  const u32x2 o1 = {cvtpk(val[4] + bf_lo(ru[2]), val[5] + bf_hi(ru[2])), cvtpk(val[6] + bf_lo(ru[3]), val[7] + bf_hi(ru[3]))};
  const __amdgpu_buffer_rsrc_t ro = make_rsrc(p.out, S * D * 2);
  const unsigned oo = (unsigned)(rt * 32 + (l & 31)) * 1024u + kstep * 32 + hh * 8;
  st8<false>(ro, oo, o0);
  st8<false>(ro, oo + 16, o1);
  STAMP_AT(4);
  STAMP_OUT(4, 5);
}

DI Ctx make_ctx(char* lds_g) {
  Ctx c;
  c.lds = (LAS char*)lds_g; c.lds0 = (unsigned)(uintptr_t)lds_g;
  c.tid = threadIdx.x; c.l = c.tid & 63; c.w = rfl(c.tid >> 6); c.hh = c.l >> 5; c.b = blockIdx.x; c.epoch = 0;
  return c;
}

__global__ __launch_bounds__(512, 2) void block_mega(Params p) {
  extern __shared__ __attribute__((aligned(16))) char lds_g[];
  Ctx c = make_ctx(lds_g);
  asm volatile("global_load_dword %0, %1, %2 sc1" : "=v"(c.epoch) : "v"(0u), "s"(p.ws + WS_FLAG) : "memory");
  phaseA<true>(p, c);
  phaseB<true>(p, c);
  u32x4 hkeep;
  phaseC<true>(p, c, hkeep);
  if (c.b < 224) phaseD<true>(p, c);
  phaseE<true>(p, c, hkeep);
  if (c.b == 0 && c.tid == 0) __hip_atomic_store((GAS unsigned*)(p.ws + WS_FLAG), c.epoch, __ATOMIC_RELAXED, __HIP_MEMORY_SCOPE_AGENT);
  asm volatile("mega_code_end:");
}

extern "C" void kernel_launch(void* const* d_in, const int* in_sizes, int n_in, void* d_out, int out_size, void* d_ws, size_t ws_size, hipStream_t stream) {
  Params p;
  p.x = (const u16*)d_in[0]; p.wqkv = (const u16*)d_in[1]; p.wo = (const u16*)d_in[2];
  p.w1 = (const u16*)d_in[4]; p.b1 = (const u16*)d_in[5]; p.w2 = (const u16*)d_in[6];
  p.ws = (char*)d_ws; p.out = (u16*)d_out;
  (void)hipFuncSetAttribute((const void*)block_mega, hipFuncAttributeMaxDynamicSharedMemorySize, LDS_BYTES);
  block_mega<<<256, 512, LDS_BYTES, stream>>>(p);
}
```

```cpp
#include <hip/hip_runtime.h>
#include <hip/hip_bf16.h>
#include <type_traits>
#define STAMP_DECL
#define STAMP_AT(i)
#define STAMP_OUT(k, n)
#define MEGA 1

typedef unsigned short u16;
using bf16x8 = __attribute__((ext_vector_type(8))) short;
using s16x4 = __attribute__((ext_vector_type(4))) short;
using f32x16 = __attribute__((ext_vector_type(16))) float;
using f32x4 = __attribute__((ext_vector_type(4))) float;
using f32x2 = __attribute__((ext_vector_type(2))) float;
using u32x4 = __attribute__((ext_vector_type(4))) unsigned;
using u32x2 = __attribute__((ext_vector_type(2))) unsigned;
using bf16x2_t = __attribute__((ext_vector_type(2))) __bf16;
#define DI __device__ __forceinline__
#define LAS __attribute__((address_space(3)))
#define GAS __attribute__((address_space(1)))
#define MFMA(a, b, c) __builtin_amdgcn_mfma_f32_32x32x16_bf16((a), (b), (c), 0, 0, 0)
#define SBAR() __builtin_amdgcn_sched_barrier(0)

constexpr int S = 2048, D = 512, NH = 32, F = 1792;
constexpr float LN_EPS = 1e-5f;
constexpr float LOG2E = 1.4426950408889634f;
constexpr float QSCALE = 0.25f * LOG2E;
constexpr float BOUND_SAFE = 40.f;
constexpr int LDS_BYTES = 163840;

constexpr unsigned WS_FLAG = 0;
constexpr unsigned FL_A = 4096, FL_B = FL_A + 256 * 64, FL_C = FL_B + 256 * 64, FL_D = FL_C + 256 * 64, FL_TMO = 64;
constexpr unsigned WS_KMAX = 128u << 10, WS_STAT = 256u << 10;
constexpr unsigned WS_Q = 1u << 20, WS_KV = 3u << 20  , WS_O = 7u << 20, WS_HF = 9u << 20, WS_ACT = 11u << 20;
constexpr unsigned WS_USED = 19u << 20;

struct Params { const u16 *x, *wqkv, *wo, *w1, *b1, *w2; char* ws; u16* out; };

template <int N> using IC = std::integral_constant<int, N>;
template <int B, int E, class Fn> DI void sfor(Fn&& f) { if constexpr (B < E) { f(IC<B>{}); sfor<B + 1, E>(f); } }

DI int rfl(int v) { return __builtin_amdgcn_readfirstlane(v); }
DI unsigned cvtpk(float lo, float hi) { const f32x2 v = {lo, hi}; return __builtin_bit_cast(unsigned, __builtin_convertvector(v, bf16x2_t)); }
DI float bf_lo(unsigned u) { return __uint_as_float(u << 16); }
DI float bf_hi(unsigned u) { return __uint_as_float(u & 0xffff0000u); }
DI float dot2(unsigned a, unsigned b, float c) {
  return __builtin_amdgcn_fdot2_f32_bf16(__builtin_bit_cast(bf16x2_t, a), __builtin_bit_cast(bf16x2_t, b), c, false);
}
constexpr unsigned ONES2 = 0x3F803F80u;
#define MFMA16(a, b, c) __builtin_amdgcn_mfma_f32_16x16x32_bf16((a), (b), (c), 0, 0, 0)
DI f32x2 unpk(unsigned u) { return f32x2{__uint_as_float(u << 16), __uint_as_float(u & 0xffff0000u)}; }
DI bf16x8 rowsum_sel(int l) { const unsigned on = ((l & 15) == ((l >> 4) & 1)) ? ONES2 : 0u; return __builtin_bit_cast(bf16x8, u32x4{on, on, on, on}); }
DI void dma16(const void* sbase, unsigned voff, unsigned lds) {
  asm volatile("s_mov_b32 m0, %2\n\ts_nop 0\n\tglobal_load_lds_dwordx4 %0, %1" ::"v"(voff), "s"(sbase), "s"(lds) : "memory");
}
DI void dma16_sc1(const void* sbase, unsigned voff, unsigned lds) {
  asm volatile("s_mov_b32 m0, %2\n\ts_nop 0\n\tglobal_load_lds_dwordx4 %0, %1 sc1" ::"v"(voff), "s"(sbase), "s"(lds) : "memory");
}
template <int IMM, bool SC1> DI void gload16(bf16x8& dst, const void* sbase, unsigned voff) {
  if constexpr (SC1) asm volatile("global_load_dwordx4 %0, %1, %2 offset:%3 sc1" : "=v"(dst) : "v"(voff), "s"(sbase), "i"(IMM) : "memory");
  else asm volatile("global_load_dwordx4 %0, %1, %2 offset:%3" : "=v"(dst) : "v"(voff), "s"(sbase), "i"(IMM) : "memory");
}
template <int IMM, bool SC1 = false> DI void gload8(u32x2& dst, const void* sbase, unsigned voff) {
  if constexpr (SC1) asm volatile("global_load_dwordx2 %0, %1, %2 offset:%3 sc1" : "=v"(dst) : "v"(voff), "s"(sbase), "i"(IMM) : "memory");
  else asm volatile("global_load_dwordx2 %0, %1, %2 offset:%3" : "=v"(dst) : "v"(voff), "s"(sbase), "i"(IMM) : "memory");
}
template <bool SC1> DI void gload4(float& dst, const void* sbase, unsigned voff) {
  if constexpr (SC1) asm volatile("global_load_dword %0, %1, %2 sc1" : "=v"(dst) : "v"(voff), "s"(sbase) : "memory");
  else asm volatile("global_load_dword %0, %1, %2" : "=v"(dst) : "v"(voff), "s"(sbase) : "memory");
}
typedef __attribute__((ext_vector_type(4))) unsigned v4u_;
typedef __attribute__((ext_vector_type(2))) unsigned v2u_;
struct WsBuf { __amdgpu_buffer_rsrc_t r; };
DI __amdgpu_buffer_rsrc_t make_rsrc(void* p, unsigned bytes) { return __builtin_amdgcn_make_buffer_rsrc(p, 0, bytes, 0x00020000); }
template <bool SC1> DI void st16(__amdgpu_buffer_rsrc_t r, unsigned off, bf16x8 v) { __builtin_amdgcn_raw_buffer_store_b128(__builtin_bit_cast(v4u_, v), r, off, 0, SC1 ? 16 : 0); }
template <bool SC1> DI void st8(__amdgpu_buffer_rsrc_t r, unsigned off, u32x2 v) { __builtin_amdgcn_raw_buffer_store_b64(v, r, off, 0, SC1 ? 16 : 0); }
template <bool SC1> DI void st4(__amdgpu_buffer_rsrc_t r, unsigned off, unsigned v) { __builtin_amdgcn_raw_buffer_store_b32(v, r, off, 0, SC1 ? 16 : 0); }

template <int N> DI void wait_bar() { asm volatile("s_waitcnt vmcnt(%0) lgkmcnt(0)\n\ts_barrier" ::"i"(N) : "memory"); }
DI void lds_bar() { asm volatile("s_waitcnt lgkmcnt(0)\n\ts_barrier" ::: "memory"); }
DI s16x4 trread(const LAS char* p) {
  typedef short v4i16_t __attribute__((ext_vector_type(4)));
  return __builtin_bit_cast(s16x4, __builtin_amdgcn_ds_read_tr16_b64_v4i16((LAS v4i16_t*)p));
}
DI bf16x8 cat8(s16x4 a, s16x4 b) { return __builtin_shufflevector(a, b, 0, 1, 2, 3, 4, 5, 6, 7); }
DI bf16x8 pack8(float a0, float a1, float a2, float a3, float a4, float a5, float a6, float a7) {
  u32x4 w = {cvtpk(a0, a1), cvtpk(a2, a3), cvtpk(a4, a5), cvtpk(a6, a7)};
  return __builtin_bit_cast(bf16x8, w);
}
template <int CTRL> DI float dpp(float v) { return __uint_as_float(__builtin_amdgcn_update_dpp(0u, __float_as_uint(v), CTRL, 0xf, 0xf, true)); }
DI float sum8(float v) { v += dpp<0xB1>(v); v += dpp<0x4E>(v); v += dpp<0x141>(v); return v; }
DI float sumx32(float v) { auto r = __builtin_amdgcn_permlane32_swap(__float_as_uint(v), __float_as_uint(v), false, false); return __uint_as_float(r[0]) + __uint_as_float(r[1]); }
DI float maxx32(float v) { auto r = __builtin_amdgcn_permlane32_swap(__float_as_uint(v), __float_as_uint(v), false, false); return fmaxf(__uint_as_float(r[0]), __uint_as_float(r[1])); }
DI float maxx16(float v) { auto r = __builtin_amdgcn_permlane16_swap(__float_as_uint(v), __float_as_uint(v), false, false); return fmaxf(__uint_as_float(r[0]), __uint_as_float(r[1])); }
DI float max16(float v) { v = fmaxf(v, dpp<0xB1>(v)); v = fmaxf(v, dpp<0x4E>(v)); v = fmaxf(v, dpp<0x141>(v)); v = fmaxf(v, dpp<0x140>(v)); return v; }

DI void publish(char* ws, unsigned flag_off, unsigned epoch) {
  asm volatile("s_waitcnt vmcnt(0) lgkmcnt(0)\n\ts_barrier" ::: "memory");
  if (threadIdx.x == 0) __hip_atomic_store((GAS unsigned*)(ws + flag_off), epoch, __ATOMIC_RELAXED, __HIP_MEMORY_SCOPE_AGENT);
}
DI void await(char* ws, unsigned flag_off, int n, unsigned epoch) {
  if (threadIdx.x >= 448) {
    const int l = threadIdx.x & 63;
    GAS unsigned* f = (GAS unsigned*)(ws + flag_off + (l < n ? l : 0) * 64);
    for (unsigned spins = 0; spins < (1u << 20); ++spins) {
      const unsigned v = __hip_atomic_load(f, __ATOMIC_RELAXED, __HIP_MEMORY_SCOPE_AGENT);
      if (__all(v == epoch)) break;
      __builtin_amdgcn_s_sleep(1);
    }
  }
  asm volatile("s_barrier" ::: "memory");
}

struct Ctx { LAS char* lds; unsigned lds0; int tid, l, w, hh, b; unsigned epoch; };

template <bool MG> DI void phaseA(const Params& p, Ctx& c) {
  STAMP_DECL STAMP_AT(0);
  char* ws = p.ws;
  LAS char* lds = c.lds;
  const int l = c.l, w = c.w, hh = c.hh, b = c.b;
  const int rb = 8 * ((b & 7) >> 1) + ((b >> 3) & 7), hg = 4 * (b & 1) + (b >> 6);
  bf16x8 ft[4] = {};
  if constexpr (MG) {
    gload16<0, false>(ft[0], (const char*)p.x + (size_t)b * 8192, w * 1024 + l * 16);
    gload16<0, false>(ft[1], (const char*)p.wqkv + (size_t)b * 6144, (w < 6 ? w : 5) * 1024 + l * 16);
    unsigned long long pc; unsigned len;
    asm volatile("s_getpc_b64 %0\n.Lmega_here%=:\n\ts_mov_b32 %1, mega_code_end-.Lmega_here%=" : "=s"(pc), "=s"(len));
    const unsigned j = (unsigned)(b >> 3);
    const unsigned oc = j * 1024u + (w & 1) * 32768u + l * 16u, pg = (j * 10u + (l < 10 ? l : 0)) * 65536u;
    const char* base = w < 2 ? (const char*)pc : w == 2 ? (const char*)ws : (const char*)p.out;
    const unsigned off = w < 2 ? (oc + 16 <= len ? oc : 0u) : w == 2 ? (pg < WS_USED ? pg : 0u) : j * 65536u;
    if (w < 4) gload16<0, false>(ft[2], base, off);
  }
  const int q4 = (l & 15) >> 2, p4 = l & 3, blk = (l >> 4) & 1;
  LAS float* tmean = (LAS float*)(lds + 0);
  LAS float* trstd = tmean + 64;
  LAS float* tcs = tmean + 128;
  f32x16 acc[3] = {};
  const int m = w & 1, g = (w >> 1) & 1;
  if (w < 4) {
    const bf16x8 sel = rowsum_sel(l);
    const int swz = (l >> 1) & 7;
    const int xrow = (32 * m + (l & 31)) * 128;
    const int wa = 8192 + (8 * hh + q4) * 128 + (((4 * g + 2 * blk + (p4 >> 1)) ^ (((q4 >> 1) & 1) << 2)) * 16) + (p4 & 1) * 8;
    int xo[4], wo_[2];
#pragma unroll
    for (int i = 0; i < 4; ++i) xo[i] = xrow + (((2 * (i ^ m) + hh) ^ swz) * 16);
    wo_[0] = wa + m * 2048; wo_[1] = wa + (1 - m) * 2048;
    bf16x8 xf[2][4], wf[2][4][3];
    f32x4 cs[3] = {};
    auto ld_w = [&](auto SET, const LAS char* sl, int i, int sgm) { constexpr int q = SET.value; const int o = wo_[i & 1] + (i & 2) * 2048 + sgm * 8192;
      wf[q][i][sgm] = cat8(trread(sl + o), trread(sl + o + 512)); };
    lds_bar();
    asm volatile("s_waitcnt vmcnt(0)" ::"v"(ft[0]), "v"(ft[1]), "v"(ft[2]) : "memory");
    STAMP_AT(2);
#pragma unroll
    for (int i = 0; i < 4; ++i) { xf[0][i] = *(const LAS bf16x8*)(lds + xo[i]); for (int sgm = 0; sgm < 3; ++sgm) ld_w(IC<0>{}, lds, i, sgm); }
    sfor<0, 8>([&](auto ST) {
      constexpr int st = ST.value, q = st & 1, qn = (st + 1) & 1;
      if constexpr (st + 1 < 8) lds_bar(); else asm volatile("s_waitcnt lgkmcnt(0)" ::: "memory");
      asm volatile("" : "+v"(xf[q][0]), "+v"(xf[q][1]), "+v"(xf[q][2]), "+v"(xf[q][3]), "+v"(wf[q][0][0]), "+v"(wf[q][0][1]), "+v"(wf[q][0][2]), "+v"(wf[q][1][0]), "+v"(wf[q][1][1]), "+v"(wf[q][1][2]),
                   "+v"(wf[q][2][0]), "+v"(wf[q][2][1]), "+v"(wf[q][2][2]), "+v"(wf[q][3][0]), "+v"(wf[q][3][1]), "+v"(wf[q][3][2]));
      const LAS char* sn = lds + ((st + 1) % 5) * 32768;
#pragma unroll
      for (int i = 0; i < 4; ++i) {
        acc[0] = MFMA(wf[q][i][0], xf[q][i], acc[0]);
        SBAR();
        if constexpr (st + 1 < 8) { ld_w(IC<qn>{}, sn, i, 0); xf[qn][i] = *(const LAS bf16x8*)(sn + xo[i]); }
        SBAR();
        acc[1] = MFMA(wf[q][i][1], xf[q][i], acc[1]);
        SBAR();
        if constexpr (st + 1 < 8) ld_w(IC<qn>{}, sn, i, 1);
        SBAR();
        acc[2] = MFMA(xf[q][i], wf[q][i][2], acc[2]);
        SBAR();
        if constexpr (st + 1 < 8) ld_w(IC<qn>{}, sn, i, 2);
        if ((i & 1) == 0) { cs[0] = MFMA16(wf[q][i][0], sel, cs[0]); cs[1] = MFMA16(wf[q][i][1], sel, cs[1]); cs[2] = MFMA16(wf[q][i][2], sel, cs[2]); }
        SBAR();
      }
    });
    if ((l & 15) < 2) {
#pragma unroll
      for (int t = 0; t < 3; ++t) *(LAS f32x4*)(tcs + (w * 3 + t) * 32 + 16 * (l & 15) + 4 * (l >> 4)) = cs[t];
    }
  } else {
    const int h = w - 4;
    const unsigned xo0 = (unsigned)(64 * rb + 16 * h + (l >> 3)) * 1024u + (unsigned)(((l & 7) ^ ((l >> 4) & 7)) * 16);
    const unsigned xo1 = (unsigned)(64 * rb + 16 * h + 8 + (l >> 3)) * 1024u + (unsigned)(((l & 7) ^ ((4 + (l >> 4)) & 7)) * 16);
    const unsigned wof = (unsigned)(16 * h + (l >> 3)) * 3072u + (unsigned)hg * 128u + (unsigned)(((l & 7) ^ (((l >> 4) & 1) << 2)) * 16);
    auto issue = [&](int st) {
      const unsigned dst = c.lds0 + (st % 5) * 32768 + h * 2048;
      const char* wsrc = (const char*)p.wqkv + (size_t)st * 64 * 3072;
      dma16((const char*)p.x + st * 128, xo0, dst);
      dma16((const char*)p.x + st * 128, xo1, dst + 1024);
#pragma unroll
      for (int sgm = 0; sgm < 3; ++sgm) { dma16(wsrc + sgm * 1024, wof, dst + 8192 + sgm * 8192); dma16(wsrc + sgm * 1024 + 8 * 3072, wof, dst + 8192 + sgm * 8192 + 1024); }
    };
    issue(0); issue(1); issue(2); issue(3); issue(4);
    STAMP_AT(1);
    f32x2 rs[2] = {}, rq[2] = {};
    u32x4 sv[2][2];
    wait_bar<32>();
    asm volatile("" ::"v"(ft[0]), "v"(ft[1]), "v"(ft[2]));
    sv[0][0] = *(const LAS u32x4*)(lds + h * 2048 + l * 16); sv[0][1] = *(const LAS u32x4*)(lds + h * 2048 + 1024 + l * 16);
    sfor<0, 8>([&](auto ST) {
      constexpr int st = ST.value, q = st & 1, qn = (st + 1) & 1;
      if constexpr (st + 1 < 8) {
        constexpr int last = 4 + st > 7 ? 7 : 4 + st;
        wait_bar<8 * (last - (st + 1))>();
        if constexpr (st + 5 < 8) issue(st + 5);
        const LAS char* sn = lds + ((st + 1) % 5) * 32768 + h * 2048 + l * 16;
        sv[qn][0] = *(const LAS u32x4*)sn; sv[qn][1] = *(const LAS u32x4*)(sn + 1024);
      }
#pragma unroll
      for (int r = 0; r < 2; ++r)
#pragma unroll
        for (int i = 0; i < 4; ++i) { const f32x2 v = unpk(sv[q][r][i]); rs[r] += v; rq[r] = __builtin_elementwise_fma(v, v, rq[r]); }
      asm volatile("" : "+v"(rs[0]), "+v"(rq[0]), "+v"(rs[1]), "+v"(rq[1]));
    });
#pragma unroll
    for (int r = 0; r < 2; ++r) {
      const float s1 = sum8(rs[r][0] + rs[r][1]), s2 = sum8(rq[r][0] + rq[r][1]);
      if ((l & 7) == 0) { const float mean = s1 * (1.f / D); const int row = 16 * h + 8 * r + (l >> 3);
        tmean[row] = mean; trstd[row] = __builtin_amdgcn_rsqf(fmaxf(s2 * (1.f / D) - mean * mean, 0.f) + LN_EPS); }
    }
  }
  STAMP_AT(4);
  if constexpr (MG) { asm volatile("s_waitcnt vmcnt(0)" : "+v"(c.epoch)::"memory"); c.epoch += 1u; }
  lds_bar();
  const __amdgpu_buffer_rsrc_t rw = make_rsrc(ws, WS_USED);
  if (w < 4) {
    const int tile = 2 * rb + m;
    const float mean_l = tmean[32 * m + (l & 31)], rstd_l = trstd[32 * m + (l & 31)];
    f32x4 cv[2][4], mm[4], rr[4];
#pragma unroll
    for (int gg = 0; gg < 4; ++gg) {
#pragma unroll
      for (int t = 0; t < 2; ++t) cv[t][gg] = *(const LAS f32x4*)(tcs + (w * 3 + t) * 32 + 8 * gg + 4 * hh) + *(const LAS f32x4*)(tcs + ((w ^ 1) * 3 + t) * 32 + 8 * gg + 4 * hh);
      mm[gg] = *(const LAS f32x4*)(tmean + 32 * m + 8 * gg + 4 * hh); rr[gg] = *(const LAS f32x4*)(trstd + 32 * m + 8 * gg + 4 * hh);
    }
    const int n = l & 31;
    const float ncs = -(tcs[(w * 3 + 2) * 32 + n] + tcs[((w ^ 1) * 3 + 2) * 32 + n]);
#pragma unroll
    for (int s = 0; s < 2; ++s) {
      f32x2 v[4];
#pragma unroll
      for (int j = 0; j < 4; ++j) { const int r = 8 * s + 2 * j; const f32x2 a = {acc[2][r], acc[2][r + 1]}, m2 = {mm[r >> 2][r & 3], mm[r >> 2][(r & 3) + 1]}, r2 = {rr[r >> 2][r & 3], rr[r >> 2][(r & 3) + 1]};
        v[j] = __builtin_elementwise_fma(m2, f32x2{ncs, ncs}, a) * r2; }
      st16<MG>(rw, WS_KV + (unsigned)((4 * hg + 2 * g + (n >> 4)) * 64 + tile) * 2048 + 1024 + s * 512 + hh * 256 + (n & 15) * 16,
               pack8(v[0][0], v[0][1], v[1][0], v[1][1], v[2][0], v[2][1], v[3][0], v[3][1]));
    }
#pragma unroll
    for (int t = 1; t >= 0; --t) {
      const float sc = t == 0 ? rstd_l * QSCALE : rstd_l, nm = -mean_l * sc;
#pragma unroll
      for (int s = 0; s < 2; ++s) {
        f32x2 v[4];
#pragma unroll
        for (int j = 0; j < 4; ++j) { const int r = 8 * s + 2 * j; const f32x2 a = {acc[t][r], acc[t][r + 1]}, cc = {cv[t][r >> 2][r & 3], cv[t][r >> 2][(r & 3) + 1]};
          v[j] = __builtin_elementwise_fma(a, f32x2{sc, sc}, cc * nm); }
        const int head = 4 * hg + 2 * g + s;
        st16<MG>(rw, (t == 0 ? WS_Q + (unsigned)(head * 64 + tile) * 1024 : WS_KV + (unsigned)(head * 64 + tile) * 2048) + l * 16, pack8(v[0][0], v[0][1], v[1][0], v[1][1], v[2][0], v[2][1], v[3][0], v[3][1]));
        if (t == 1) {
          f32x2 n2 = v[0] * v[0]; n2 = __builtin_elementwise_fma(v[1], v[1], n2); n2 = __builtin_elementwise_fma(v[2], v[2], n2); n2 = __builtin_elementwise_fma(v[3], v[3], n2);
          const float mx = maxx16(max16(sumx32(n2[0] + n2[1])));
          if (l == 0) st4<MG>(rw, WS_KMAX + (unsigned)(head * 64 + tile) * 4, __float_as_uint(mx));
        }
      }
    }
  }
  STAMP_AT(5);
  if constexpr (MG) publish(ws, FL_A + (hg * 32 + rb) * 64, c.epoch);
  STAMP_AT(6);
  STAMP_OUT(0, 7);
}

DI unsigned wpiece_lane(int l, unsigned row_bytes) { return (unsigned)(l >> 3) * row_bytes + (unsigned)(((l & 7) ^ (((l >> 4) & 1) << 2)) * 16); }
template <int NW, int W0 = 0> DI void prefetch_wo(const Params& p, const Ctx& c) {
  if (c.w < W0 || c.w >= W0 + NW) return;
  const unsigned off = wpiece_lane(c.l, 1024) + (unsigned)(c.b >> 5) * 128u;
  for (int pc = c.w - W0; pc < 64; pc += NW) dma16((const char*)p.wo + (size_t)(8 * pc) * 1024, off, c.lds0 + pc * 1024);
}
DI constexpr unsigned w1_stage_base(int j) { return j < 6 ? 65536 + j * 16384 : (j - 6) * 16384; }
template <int NW, int W0 = 0> DI void prefetch_w1(const Params& p, const Ctx& c, int p0, int p1) {
  if (c.w < W0 || c.w >= W0 + NW) return;
  const unsigned off = wpiece_lane(c.l, F * 2) + (unsigned)(c.b >> 4) * 256u;
  for (int pc = p0 + c.w - W0; pc < p1; pc += NW) {
    const int j = pc >> 4, seg = (pc >> 3) & 1, r8 = pc & 7;
    dma16((const char*)p.w1 + (size_t)(64 * j + 8 * r8) * (F * 2) + seg * 128, off, c.lds0 + w1_stage_base(j) + seg * 8192 + r8 * 1024);
  }
}
template <int NW> DI void prefetch_w2(const Params& p, const Ctx& c, int p0, int p1) {
  if (c.w >= NW) return;
  const unsigned off = wpiece_lane(c.l, 1024) + (unsigned)(c.b >> 5) * 128u;
  for (int pc = p0 + c.w; pc < p1; pc += NW) {
    const int st = pc >> 4, kq = (pc >> 2) & 3, r8 = pc & 3;
    dma16((const char*)p.w2 + (size_t)(448 * kq + 32 * st + 8 * r8) * 1024, off, c.lds0 + (st % 10) * 16384 + kq * 4096 + r8 * 1024);
  }
}

DI void warm_l2(const Params& p, const Ctx& c) {
  const int h = c.w - 4;
  const unsigned off = (unsigned)(c.b >> 3) * 57344u + (unsigned)h * 14336u + (unsigned)c.l * 16u;
#pragma unroll
  for (int i = 0; i < 14; ++i) dma16((const char*)p.w2 + i * 1024, off, c.lds0 + 139520);
  const unsigned off1 = (unsigned)c.b * 7168u + (unsigned)h * 2048u + (unsigned)c.l * 16u;
  dma16((const char*)p.w1, off1, c.lds0 + 139520);
  if (h < 3) dma16((const char*)p.w1 + 1024, off1, c.lds0 + 139520);
}

constexpr int B_ONES = 131072;
template <bool MG> DI void phaseB(const Params& p, const Ctx& c) {
  STAMP_DECL STAMP_AT(0);
  char* ws = p.ws;
  LAS char* lds = c.lds;
  const int l = c.l, w = c.w, hh = c.hh, b = c.b, tid = c.tid;
  const int head = 4 * (b & 7) + ((b >> 3) & 3), qb = b >> 5;
  for (int i = tid; i < (4096 + 512) / 16; i += 512) { const unsigned v = (i & 31) == 0 ? ONES2 : 0u; ((LAS u32x4*)(lds + B_ONES))[i] = u32x4{v, v, v, v}; }
  if constexpr (MG) await(ws, FL_A + (head >> 2) * 32 * 64, 32, c.epoch);
  STAMP_AT(1);
  const unsigned l16 = l * 16;
  if (w >= 4) {
    const char* kvg = ws + WS_KV + (size_t)head * 131072;
    const int h = w - 4;
    auto piece = [&](int pc) { if constexpr (MG) dma16_sc1(kvg + pc * 1024, l16, c.lds0 + pc * 1024); else dma16(kvg + pc * 1024, l16, c.lds0 + pc * 1024); };
#pragma unroll
    for (int i = 0; i < 4; ++i) piece(4 * h + i);
#pragma unroll
    for (int i = 0; i < 12; ++i) piece(16 + 4 * i + h);
    wait_bar<12>();
#pragma unroll
    for (int i = 0; i < 16; ++i) piece(64 + 4 * i + h);
    wait_bar<16>();
    if constexpr (MG) warm_l2(p, c);
    wait_bar<0>();
    if constexpr (MG) { if (b < 224) prefetch_w1<4, 4>(p, c, 80, 96); }
    lds_bar();
    if constexpr (MG) prefetch_wo<4, 4>(p, c);
    lds_bar();
    if constexpr (MG) { if (b < 224) prefetch_w1<4, 4>(p, c, 0, 32); }
  } else {
    bf16x8 qa, qb_;
    float kmx;
    gload4<MG>(kmx, ws + WS_KMAX + head * 256, l * 4);
    gload16<0, MG>(qa, ws + WS_Q + (size_t)(head * 64 + qb * 8 + w) * 1024, l * 16);
    gload16<0, MG>(qb_, ws + WS_Q + (size_t)(head * 64 + qb * 8 + w + 4) * 1024, l * 16);
    asm volatile("s_waitcnt vmcnt(0)" : "+v"(qa), "+v"(qb_), "+v"(kmx)::"memory");
    kmx = maxx32(maxx16(max16(kmx)));
    auto qbound = [&](bf16x8 q) { const u32x4 u = __builtin_bit_cast(u32x4, q); f32x2 t = {0.f, 0.f}; for (int i = 0; i < 4; ++i) { const f32x2 v = unpk(u[i]); t = __builtin_elementwise_fma(v, v, t); }
      return __builtin_amdgcn_sqrtf(sumx32(t[0] + t[1]) * kmx) * 1.01f + 1e-6f; };
    const float bna = qbound(qa), bnb = qbound(qb_);
    const bool safe = __all(bna <= BOUND_SAFE && bnb <= BOUND_SAFE);
    f32x16 oa = {}, ob = {};
    const int lo5 = l & 31;
    const bool isv = lo5 < 16;
    const int va0 = isv ? 1024 + hh * 256 + lo5 * 16 : B_ONES + (lo5 == 16 ? 0 : (lo5 >= 20 && lo5 < 28) ? 64 : 16);
    lds_bar();
    STAMP_AT(2);
    if (safe) {
      f32x16 nga, ngb; for (int i = 0; i < 16; ++i) { nga[i] = -bna; ngb[i] = -bnb; }
      f32x16 s0, s1;
      bf16x8 pa, pb = {};
      bf16x8 kf, v0, v1 = {};
      unsigned ka = c.lds0 + l * 16, va = c.lds0 + va0;
      const unsigned vstep = isv ? 4096u : 0u;
      asm volatile("ds_read_b128 %1, %3\n\tds_read_b128 %2, %4\n\ts_waitcnt lgkmcnt(0)\n\t"
                   "v_mfma_f32_32x32x16_bf16 %0, %1, %5, %6\n\ts_nop 7\n\ts_nop 3"
                   : "=&{v[192:207]}"(s0), "=&v"(kf), "=&v"(v0) : "v"(ka), "v"(va), "v"(qa), "v"(nga) : "memory");
#define E_(b, i) "v_exp_f32 v[" #b "+" #i "], v[" #b "+" #i "]\n\t"
#define C_(d, i, b, j, k) "v_cvt_pk_bf16_f32 v[" #d "+" #i "], v[" #b "+" #j "], v[" #b "+" #k "]\n\t"
#define MID_(SC) E_(SC, 4) E_(SC, 5) E_(SC, 6) E_(SC, 7) C_(224, 0, SC, 0, 1) C_(224, 1, SC, 2, 3) E_(SC, 8) E_(SC, 9) C_(224, 2, SC, 4, 5) C_(224, 3, SC, 6, 7) E_(SC, 10) E_(SC, 11)
#define END_(SC) E_(SC, 12) E_(SC, 13) E_(SC, 14) E_(SC, 15) C_(228, 0, SC, 8, 9) C_(228, 1, SC, 10, 11) C_(228, 2, SC, 12, 13) C_(228, 3, SC, 14, 15)
#define UNIT_A(KOFF, V1OFF) \
      "v_mfma_f32_32x32x16_bf16 v[208:223], %[kf], %[qb], %[ngb]\n\t" \
      "ds_read_b128 %[kf], %[ka] offset:" #KOFF "\n\t" \
      E_(192, 0) E_(192, 1) E_(192, 2) E_(192, 3) \
      "v_mfma_f32_32x32x16_bf16 %[ob], %[v1], v[228:231], %[ob]\n\t" \
      "ds_read_b128 %[v1], %[va] offset:" #V1OFF "\n\t" \
      MID_(192) \
      "s_waitcnt lgkmcnt(2)\n\t" \
      "v_mfma_f32_32x32x16_bf16 %[oa], %[v0], v[224:227], %[oa]\n\t" \
      END_(192)
#define UNIT_B(V0OFF) \
      "s_waitcnt lgkmcnt(0)\n\t" \
      "v_mfma_f32_32x32x16_bf16 v[192:207], %[kf], %[qa], %[nga]\n\t" \
      E_(208, 0) E_(208, 1) E_(208, 2) E_(208, 3) \
      "v_mfma_f32_32x32x16_bf16 %[oa], %[v1], v[228:231], %[oa]\n\t" \
      MID_(208) \
      "v_mfma_f32_32x32x16_bf16 %[ob], %[v0], v[224:227], %[ob]\n\t" \
      "ds_read_b128 %[v0], %[va] offset:" #V0OFF "\n\t" \
      END_(208)
#define RUN_GROUPS(NG) do { int cnt_ = (NG); \
      asm volatile("1:\n\t" UNIT_A(2048, 512) UNIT_B(2048) UNIT_A(4096, 2560) UNIT_B(4096) \
        "v_add_u32 %[ka], 0x1000, %[ka]\n\tv_add_u32 %[va], %[va], %[vs]\n\t" \
        "s_sub_u32 %[cnt], %[cnt], 1\n\ts_cmp_lg_u32 %[cnt], 0\n\ts_cbranch_scc1 1b\n\ts_waitcnt lgkmcnt(0)\n\ts_nop 7\n\ts_nop 3" \
        : "+{v[192:207]}"(s0), "=&{v[208:223]}"(s1), "=&{v[224:227]}"(pa), "+{v[228:231]}"(pb), [oa] "+v"(oa), [ob] "+v"(ob), [kf] "+v"(kf), [v0] "+v"(v0), [v1] "+v"(v1), \
          [ka] "+v"(ka), [va] "+v"(va), [cnt] "+s"(cnt_) \
        : [qa] "v"(qa), [qb] "v"(qb_), [nga] "v"(nga), [ngb] "v"(ngb), [vs] "v"(vstep) : "memory", "scc"); } while (0)
      RUN_GROUPS(3);
      lds_bar();
      STAMP_AT(3);
      RUN_GROUPS(12);
      lds_bar();
      RUN_GROUPS(1);
      lds_bar();
      RUN_GROUPS(8);
      lds_bar();
      RUN_GROUPS(8);
      ob = MFMA(v1, pb, ob);
    } else {
      float ca = -3.0e38f, cb = -3.0e38f;
      const f32x16 zero = {};
      auto online = [&](const bf16x8& kf, const bf16x8& v0, const bf16x8& v1, const bf16x8& q, f32x16& o, float& mrun) {
        f32x16 s = MFMA(kf, q, zero);
        float tm = s[0];
#pragma unroll
        for (int i = 1; i < 16; ++i) tm = fmaxf(tm, s[i]);
        tm = maxx32(tm);
        const float mn = fmaxf(mrun, tm), f = __builtin_amdgcn_exp2f(mrun - mn);
        mrun = mn;
#pragma unroll
        for (int i = 0; i < 16; ++i) { o[i] *= f; s[i] = __builtin_amdgcn_exp2f(s[i] - mn); }
        o = MFMA(v0, pack8(s[0], s[1], s[2], s[3], s[4], s[5], s[6], s[7]), o);
        o = MFMA(v1, pack8(s[8], s[9], s[10], s[11], s[12], s[13], s[14], s[15]), o);
      };
      for (int t = 0; t < 64; ++t) {
        if (t == 6 || t == 30 || t == 32 || t == 48) lds_bar();
        const bf16x8 kf = *(const LAS bf16x8*)(lds + l * 16 + t * 2048);
        const int vo = isv ? va0 + t * 2048 : va0;
        const bf16x8 v0 = *(const LAS bf16x8*)(lds + vo), v1 = *(const LAS bf16x8*)(lds + vo + 512);
        online(kf, v0, v1, qa, oa, ca);
        online(kf, v0, v1, qb_, ob, cb);
      }
    }
    STAMP_AT(4);
    const float ia = __builtin_amdgcn_rcpf(sumx32(oa[8])), ib = __builtin_amdgcn_rcpf(sumx32(ob[8]));
    const __amdgpu_buffer_rsrc_t rw = make_rsrc(ws, WS_USED);
    st16<MG>(rw, WS_O + (unsigned)(((qb * 8 + w) * 32 + head) * 64 + l) * 16, pack8(oa[0] * ia, oa[1] * ia, oa[2] * ia, oa[3] * ia, oa[4] * ia, oa[5] * ia, oa[6] * ia, oa[7] * ia));
    st16<MG>(rw, WS_O + (unsigned)(((qb * 8 + w + 4) * 32 + head) * 64 + l) * 16, pack8(ob[0] * ib, ob[1] * ib, ob[2] * ib, ob[3] * ib, ob[4] * ib, ob[5] * ib, ob[6] * ib, ob[7] * ib));
  }
  STAMP_AT(5);
  if constexpr (MG) {
    publish(ws, FL_B + (qb * 32 + head) * 64, c.epoch);
    if (b < 224) prefetch_w1<7>(p, c, 32, 80);
  }
  STAMP_AT(6);
  STAMP_OUT(1, 7);
}

template <int KQ, int HGAP> DI void quarter_reduce_t(int m, int l, LAS f32x4* xch, const f32x16 (&acc)[2], float (&val)[8]) {
#pragma unroll
  for (int d = 0; d < 4; ++d) {
    if (d == KQ) continue;
    xch[(d >> 1) * HGAP + (((d * 4 + KQ) * 2 + m) * 2 + 0) * 64 + l] = f32x4{acc[d >> 1][8 * (d & 1)], acc[d >> 1][8 * (d & 1) + 1], acc[d >> 1][8 * (d & 1) + 2], acc[d >> 1][8 * (d & 1) + 3]};
    xch[(d >> 1) * HGAP + (((d * 4 + KQ) * 2 + m) * 2 + 1) * 64 + l] = f32x4{acc[d >> 1][8 * (d & 1) + 4], acc[d >> 1][8 * (d & 1) + 5], acc[d >> 1][8 * (d & 1) + 6], acc[d >> 1][8 * (d & 1) + 7]};
  }
  lds_bar();
  f32x4 t[4][2];
#pragma unroll
  for (int s = 0; s < 4; ++s) { if (s == KQ) continue; t[s][0] = xch[(KQ >> 1) * HGAP + (((KQ * 4 + s) * 2 + m) * 2 + 0) * 64 + l]; t[s][1] = xch[(KQ >> 1) * HGAP + (((KQ * 4 + s) * 2 + m) * 2 + 1) * 64 + l]; }
#pragma unroll
  for (int j = 0; j < 8; ++j) val[j] = acc[KQ >> 1][8 * (KQ & 1) + j];
#pragma unroll
  for (int s = 0; s < 4; ++s) { if (s == KQ) continue; for (int j = 0; j < 4; ++j) { val[j] += t[s][0][j]; val[4 + j] += t[s][1][j]; } }
}
template <int HGAP> DI void quarter_reduce(const Ctx& c, LAS f32x4* xch, const f32x16 (&acc)[2], float (&val)[8]) {
  const int m = c.w & 1, kq = c.w >> 1;
  if (kq == 0) quarter_reduce_t<0, HGAP>(m, c.l, xch, acc, val);
  else if (kq == 1) quarter_reduce_t<1, HGAP>(m, c.l, xch, acc, val);
  else if (kq == 2) quarter_reduce_t<2, HGAP>(m, c.l, xch, acc, val);
  else quarter_reduce_t<3, HGAP>(m, c.l, xch, acc, val);
}

template <bool MG> DI void phaseC(const Params& p, const Ctx& c, u32x4& hkeep) {
  STAMP_DECL STAMP_AT(0);
  char* ws = p.ws;
  LAS char* lds = c.lds;
  const int l = c.l, w = c.w, hh = c.hh, b = c.b;
  const int rb = (b & 7) * 4 + ((b >> 3) & 3), cb = b >> 5;
  const int m = w & 1, kq = w >> 1;
  const int rt = 2 * rb + m, kstep = 4 * cb + kq;
  u32x2 xr0, xr1;
  gload8<0>(xr0, (const char*)p.x + (size_t)rt * 32 * 1024 + kstep * 32, (l & 31) * 1024 + hh * 8);
  gload8<16>(xr1, (const char*)p.x + (size_t)rt * 32 * 1024 + kstep * 32, (l & 31) * 1024 + hh * 8);
  if constexpr (!MG) prefetch_wo<8>(p, c);
  if constexpr (MG) await(ws, FL_B + (rb >> 2) * 32 * 64, 32, c.epoch);
  STAMP_AT(1);
  const char* abase = ws + WS_O + ((size_t)rt * 32 + kq * 8) * 1024;
  bf16x8 af[8];
  sfor<0, 8>([&](auto I) { constexpr int i = I.value; gload16<(i & 3) * 1024, MG>(af[i], abase + (i >> 2) * 4096, l * 16); });
  f32x16 acc[2] = {};
  const int q4 = (l & 15) >> 2, p4 = l & 3, blk = (l >> 4) & 1;
  const int wa0 = (128 * kq + 4 * hh + q4) * 128 + (((2 * blk + (p4 >> 1)) ^ (((q4 >> 1) & 1) << 2)) * 16) + (p4 & 1) * 8;
  const int wa1 = wa0 ^ 64;
  asm volatile("s_waitcnt vmcnt(8)\n\ts_barrier" : "+v"(xr0), "+v"(xr1)::"memory");
  bf16x8 wf[8][2];
#pragma unroll
  for (int i = 0; i < 8; ++i) {
    wf[i][0] = cat8(trread(lds + wa0 + i * 2048), trread(lds + wa0 + i * 2048 + 1024));
    wf[i][1] = cat8(trread(lds + wa1 + i * 2048), trread(lds + wa1 + i * 2048 + 1024));
  }
  lds_bar();
  STAMP_AT(2);
  sfor<0, 8>([&](auto I) {
    constexpr int i = I.value;
    asm volatile("s_waitcnt vmcnt(%1)" : "+v"(af[i]) : "i"(7 - i) : "memory");
    acc[0] = MFMA(wf[i][0], af[i], acc[0]);
    acc[1] = MFMA(wf[i][1], af[i], acc[1]);
    SBAR();
  });
  STAMP_AT(3);
  float val[8];
  quarter_reduce<0>(c, (LAS f32x4*)lds, acc, val);
  const u32x4 o = {cvtpk(val[0] + bf_lo(xr0[0]), val[1] + bf_hi(xr0[0])), cvtpk(val[2] + bf_lo(xr0[1]), val[3] + bf_hi(xr0[1])),
                   cvtpk(val[4] + bf_lo(xr1[0]), val[5] + bf_hi(xr1[0])), cvtpk(val[6] + bf_lo(xr1[1]), val[7] + bf_hi(xr1[1]))};
  hkeep = o;
  const __amdgpu_buffer_rsrc_t rw = make_rsrc(ws, WS_USED);
  st16<MG>(rw, WS_HF + (unsigned)((rt * 32 + kstep) * 64 + l) * 16, __builtin_bit_cast(bf16x8, o));
  f32x2 sm = {0.f, 0.f}, sq = {0.f, 0.f};
#pragma unroll
  for (int i = 0; i < 4; ++i) { const f32x2 v = unpk(o[i]); sm += v; sq = __builtin_elementwise_fma(v, v, sq); }
  LAS f32x2* tst = (LAS f32x2*)(lds + 0);
  tst[((m * 4 + kq) * 2 + hh) * 32 + (l & 31)] = f32x2{sm[0] + sm[1], sq[0] + sq[1]};
  lds_bar();
  if (w < 2 && l < 32) {
    f32x2 t8[8];
#pragma unroll
    for (int i = 0; i < 8; ++i) t8[i] = tst[(w * 8 + i) * 32 + l];
    const f32x2 t = ((t8[0] + t8[1]) + (t8[2] + t8[3])) + ((t8[4] + t8[5]) + (t8[6] + t8[7]));
    st8<MG>(rw, WS_STAT + (unsigned)(((2 * rb + w) * 8 + cb) * 32 + l) * 8, __builtin_bit_cast(u32x2, t));
  }
  STAMP_AT(4);
  if constexpr (MG) {
    publish(ws, FL_C + ((rb >> 1) * 16 + (rb & 1) * 8 + cb) * 64, c.epoch);
    if (b < 224) prefetch_w1<7>(p, c, 96, 128);
  }
  STAMP_AT(5);
  STAMP_OUT(2, 6);
}

constexpr int D_WIN = 12;
template <bool MG> DI void phaseD(const Params& p, const Ctx& c) {
  STAMP_DECL STAMP_AT(0);
  char* ws = p.ws;
  LAS char* lds = c.lds;
  const int l = c.l, w = c.w, hh = c.hh, b = c.b;
  const int rb = 2 * (b & 7) + ((b >> 3) & 1), cb = b >> 4;
  const int m = w & 3, nh = w >> 2;
  const int rt = 4 * rb + m;
  LAS float* tcs = (LAS float*)(lds + 49152);
  LAS float* tb = tcs + 128;
  if (c.tid < 128) tcs[c.tid] = 0.f;
  bf16x8 bv;
  gload16<0, false>(bv, (const char*)p.b1 + cb * 256, (l & 15) * 16);
  if constexpr (!MG) prefetch_w1<8>(p, c, 0, 128);
  if constexpr (MG) await(ws, FL_C + rb * 16 * 64, 16, c.epoch);
  STAMP_AT(1);
  u32x2 st[4];
  sfor<0, 4>([&](auto I) { constexpr int i = I.value; gload8<i * 256, MG>(st[i], ws + WS_STAT + (size_t)rt * 2048, (l & 31) * 8 + hh * 1024); });
  const int par = m >> 1;
  const char* abase = ws + WS_HF + (size_t)rt * 32 * 1024;
  const unsigned aoff0 = l * 16 + par * 1024, aoff1 = l * 16 + (1 - par) * 1024;
  bf16x8 af[32];
  auto load_a = [&](auto I) { constexpr int i = I.value;
    if constexpr (i & 1) gload16<((i >> 1) & 1) * 2048, MG>(af[i], abase + (i >> 2) * 4096, aoff1);
    else gload16<((i >> 1) & 1) * 2048, MG>(af[i], abase + (i >> 2) * 4096, aoff0); };
  sfor<0, D_WIN>(load_a);
  f32x16 acc[2] = {};
  f32x4 cs = {};
  const bf16x8 sel = rowsum_sel(l);
  const int q4 = (l & 15) >> 2, p4 = l & 3, blk = (l >> 4) & 1;
  const int wl = nh * 8192 + (4 * hh + q4) * 128 + (((4 * (m & 1) + 2 * blk + (p4 >> 1)) ^ (((q4 >> 1) & 1) << 2)) * 16) + (p4 & 1) * 8;
  const int wa_e = wl + par * 2048, wa_o = wl + (1 - par) * 2048;
  asm volatile("s_waitcnt vmcnt(%1) lgkmcnt(0)\n\ts_barrier" : "+v"(bv) : "i"(D_WIN + 4) : "memory");
  bf16x8 wf[3][2];
  auto load_w = [&](auto I) {
    constexpr int i = I.value;
    constexpr int o = (int)w1_stage_base(i >> 2) + (i & 2) * 2048;
    const int wa = (i & 1) ? wa_o : wa_e;
    wf[i % 3][0] = cat8(trread(lds + wa + o), trread(lds + wa + o + 1024));
    wf[i % 3][1] = cat8(trread(lds + (wa ^ 64) + o), trread(lds + (wa ^ 64) + o + 1024));
  };
  load_w(IC<0>{}); load_w(IC<1>{});
  STAMP_AT(2);
  sfor<0, 32>([&](auto I) {
    constexpr int i = I.value;
    if constexpr (i + D_WIN < 32) load_a(IC<i + D_WIN>{});
    constexpr int issued = i + D_WIN < 32 ? i + D_WIN + 1 : 32;
    asm volatile("s_waitcnt vmcnt(%1)" : "+v"(af[i]) : "i"(issued - i - 1) : "memory");
    acc[0] = MFMA(wf[i % 3][0], af[i], acc[0]);
    acc[1] = MFMA(wf[i % 3][1], af[i], acc[1]);
    if constexpr ((i & 1) == 0) cs = MFMA16(wf[i % 3][0], sel, cs);
    if constexpr (i + 2 < 32) { SBAR(); load_w(IC<i + 2>{}); }
    SBAR();
  });
  STAMP_AT(3);
  if ((l & 15) < 2) {
    LAS float* d = tcs + nh * 64 + (m & 1) * 32 + 16 * (l & 15) + 4 * (l >> 4);
#pragma unroll
    for (int j = 0; j < 4; ++j) __hip_atomic_fetch_add(d + j, cs[j], __ATOMIC_RELAXED, __HIP_MEMORY_SCOPE_WORKGROUP);
  }
  if (w == 0 && l < 16) { const u32x4 u = __builtin_bit_cast(u32x4, bv);
    *(LAS f32x4*)(tb + l * 8) = f32x4{bf_lo(u[0]), bf_hi(u[0]), bf_lo(u[1]), bf_hi(u[1])}; *(LAS f32x4*)(tb + l * 8 + 4) = f32x4{bf_lo(u[2]), bf_hi(u[2]), bf_lo(u[3]), bf_hi(u[3])}; }
  asm volatile("" : "+v"(st[0]), "+v"(st[1]), "+v"(st[2]), "+v"(st[3]));
  const f32x2 ss = __builtin_bit_cast(f32x2, st[0]) + __builtin_bit_cast(f32x2, st[1]) + __builtin_bit_cast(f32x2, st[2]) + __builtin_bit_cast(f32x2, st[3]);
  const float sm = sumx32(ss[0]), sq = sumx32(ss[1]);
  const float mean = sm * (1.f / D), rstd = __builtin_amdgcn_rsqf(fmaxf(sq * (1.f / D) - mean * mean, 0.f) + LN_EPS);
  const float nmr = -mean * rstd;
  lds_bar();
  STAMP_AT(4);
  const __amdgpu_buffer_rsrc_t rw = make_rsrc(ws, WS_USED);
#pragma unroll
  for (int t = 0; t < 2; ++t) {
    const int ct = 2 * nh + ((m & 1) ^ t);
    f32x4 cv[4], bb[4];
#pragma unroll
    for (int g = 0; g < 4; ++g) { cv[g] = *(const LAS f32x4*)(tcs + 32 * ct + 8 * g + 4 * hh); bb[g] = *(const LAS f32x4*)(tb + 32 * ct + 8 * g + 4 * hh); }
#pragma unroll
    for (int s = 0; s < 2; ++s) {
      unsigned pk[4];
#pragma unroll
      for (int j = 0; j < 4; ++j) {
        const int r = 8 * s + 2 * j;
        const f32x2 a = {acc[t][r], acc[t][r + 1]}, c2 = {cv[r >> 2][r & 3], cv[r >> 2][(r & 3) + 1]}, b2 = {bb[r >> 2][r & 3], bb[r >> 2][(r & 3) + 1]};
        const f32x2 y = __builtin_elementwise_fma(a, f32x2{rstd, rstd}, __builtin_elementwise_fma(c2, f32x2{nmr, nmr}, b2));
        const f32x2 z = y * LOG2E;
        const f32x2 u = {__builtin_amdgcn_exp2f(z[0]), __builtin_amdgcn_exp2f(z[1])};
        const f32x2 d = __builtin_elementwise_fma(u, u + 2.f, f32x2{2.f, 2.f});
        const f32x2 r2 = {__builtin_amdgcn_rcpf(d[0]), __builtin_amdgcn_rcpf(d[1])};
        const f32x2 th = __builtin_elementwise_fma(r2, f32x2{-2.f, -2.f}, f32x2{1.f, 1.f});
        const f32x2 o = y * th;
        pk[j] = cvtpk(o[0], o[1]);
      }
      st16<MG>(rw, WS_ACT + (unsigned)((rt * 112 + cb * 8 + ct * 2 + s) * 64 + l) * 16, __builtin_bit_cast(bf16x8, u32x4{pk[0], pk[1], pk[2], pk[3]}));
      if constexpr (MG) {
        const int q = 2 * t + s, st0 = q < 2 ? 0 : 4;
        prefetch_w2<8>(p, c, 16 * st0 + 24 * (q & 1), 16 * st0 + 24 * (q & 1) + 24);
      }
    }
  }
  STAMP_AT(5);
  if constexpr (MG) publish(ws, FL_D + (rb * 16 + cb) * 64, c.epoch);
  STAMP_AT(6);
  STAMP_OUT(3, 7);
}

constexpr int E_NSLOT = 10, E_NST = 14, E_WIN = 12, E_PRE = 8;
struct ESched {
  int posA[28], posW[E_NST], n_top[E_NST], n_mid[E_NST];
  constexpr ESched() : posA(), posW(), n_top(), n_mid() {
    int n = 0;
    for (int i = 0; i < E_WIN; ++i) posA[i] = n++;
    for (int st = 0; st < E_NST; ++st) {
      n_top[st] = n;
      if (E_PRE + st < E_NST) { n += 2; posW[E_PRE + st] = n - 1; }
      for (int j = 0; j < 2; ++j) if (E_WIN + 2 * st + j < 28) posA[E_WIN + 2 * st + j] = n++;
      n_mid[st] = n;
    }
  }
};
template <bool MG> DI void phaseE(const Params& p, const Ctx& c, u32x4 hkeep) {
  STAMP_DECL STAMP_AT(0);
  constexpr ESched sch{};
  char* ws = p.ws;
  LAS char* lds = c.lds;
  const int l = c.l, w = c.w, hh = c.hh, b = c.b;
  const int rb = (b & 7) * 4 + ((b >> 3) & 3), cb = b >> 5;
  const int m = w & 1, kq = w >> 1;
  const int rt = 2 * rb + m, kstep = 4 * cb + kq;
  if (!MG || b >= 224) prefetch_w2<7>(p, c, 0, 16 * E_PRE);
  else { prefetch_w2<7>(p, c, 16 * 3, 16 * 4); prefetch_w2<7>(p, c, 16 * 7, 16 * 8); }
  bf16x8 resv = __builtin_bit_cast(bf16x8, hkeep);
  if constexpr (!MG) gload16<0, false>(resv, ws + WS_HF + (size_t)(rt * 32 + kstep) * 1024, l * 16);
  if constexpr (MG) await(ws, FL_D + (rb >> 1) * 16 * 64, 14, c.epoch);
  STAMP_AT(1);
  const char* abase = ws + WS_ACT + ((size_t)rt * 112 + kq * 28) * 1024;
  bf16x8 af[28];
  auto load_a = [&](auto I) { constexpr int i = I.value; gload16<(i & 3) * 1024, MG>(af[i], abase + (i >> 2) * 4096, l * 16); };
  sfor<0, E_WIN>(load_a);
  f32x16 acc[2] = {};
  const int q4 = (l & 15) >> 2, p4 = l & 3, blk = (l >> 4) & 1;
  const int wa0 = kq * 4096 + (4 * hh + q4) * 128 + (((2 * blk + (p4 >> 1)) ^ (((q4 >> 1) & 1) << 2)) * 16) + (p4 & 1) * 8;
  const int wa1 = wa0 ^ 64;
  bf16x8 wf[3][2][2];
  auto load_w = [&](auto ST) {
    constexpr int st = ST.value;
    const LAS char* sl = lds + (st % E_NSLOT) * 16384;
#pragma unroll
    for (int i = 0; i < 2; ++i) {
      wf[st % 3][i][0] = cat8(trread(sl + wa0 + i * 2048), trread(sl + wa0 + i * 2048 + 1024));
      wf[st % 3][i][1] = cat8(trread(sl + wa1 + i * 2048), trread(sl + wa1 + i * 2048 + 1024));
    }
  };
  asm volatile("s_waitcnt vmcnt(%0)\n\ts_barrier" ::"i"(E_WIN) : "memory");
  load_w(IC<0>{}); load_w(IC<1>{});
  STAMP_AT(2);
  sfor<0, E_NST>([&](auto ST) {
    constexpr int st = ST.value;
    if constexpr (st == 2) asm volatile("s_waitcnt lgkmcnt(0)\n\ts_barrier" ::: "memory");
    if constexpr (st == 6 || st == 8 || st == 10) asm volatile("s_waitcnt vmcnt(%0) lgkmcnt(0)\n\ts_barrier" ::"i"(sch.n_top[st] - sch.posW[st + 3] - 1) : "memory");
    if constexpr (E_PRE + st < E_NST) prefetch_w2<8>(p, c, 16 * (st + E_PRE), 16 * (st + E_PRE + 1));
    if constexpr (E_WIN + 2 * st < 28) load_a(IC<E_WIN + 2 * st>{});
    if constexpr (E_WIN + 2 * st + 1 < 28) load_a(IC<E_WIN + 2 * st + 1>{});
    asm volatile("s_waitcnt vmcnt(%2)" : "+v"(af[2 * st]), "+v"(af[2 * st + 1]) : "i"(sch.n_mid[st] - sch.posA[2 * st + 1] - 1) : "memory");
    acc[0] = MFMA(wf[st % 3][0][0], af[2 * st], acc[0]);
    acc[1] = MFMA(wf[st % 3][0][1], af[2 * st], acc[1]);
    if constexpr (st + 2 < E_NST) { SBAR(); load_w(IC<st + 2>{}); SBAR(); }
    acc[0] = MFMA(wf[st % 3][1][0], af[2 * st + 1], acc[0]);
    acc[1] = MFMA(wf[st % 3][1][1], af[2 * st + 1], acc[1]);
    SBAR();
  });
  STAMP_AT(3);
  float val[8];
  quarter_reduce<0>(c, (LAS f32x4*)(lds + 65536), acc, val);
  asm volatile("" : "+v"(resv));
  const u32x4 ru = __builtin_bit_cast(u32x4, resv);
  const u32x2 o0 = {cvtpk(val[0] + bf_lo(ru[0]), val[1] + bf_hi(ru[0])), cvtpk(val[2] + bf_lo(ru[1]), val[3] + bf_hi(ru[1]))};
  const u32x2 o1 = {cvtpk(val[4] + bf_lo(ru[2]), val[5] + bf_hi(ru[2])), cvtpk(val[6] + bf_lo(ru[3]), val[7] + bf_hi(ru[3]))};
  const __amdgpu_buffer_rsrc_t ro = make_rsrc(p.out, S * D * 2);
  const unsigned oo = (unsigned)(rt * 32 + (l & 31)) * 1024u + kstep * 32 + hh * 8;
  st8<false>(ro, oo, o0);
  st8<false>(ro, oo + 16, o1);
  STAMP_AT(4);
  STAMP_OUT(4, 5);
}

DI Ctx make_ctx(char* lds_g) {
  Ctx c;
  c.lds = (LAS char*)lds_g; c.lds0 = (unsigned)(uintptr_t)lds_g;
  c.tid = threadIdx.x; c.l = c.tid & 63; c.w = rfl(c.tid >> 6); c.hh = c.l >> 5; c.b = blockIdx.x; c.epoch = 0;
  return c;
}

__global__ __launch_bounds__(512, 2) void block_mega(Params p) {
  extern __shared__ __attribute__((aligned(16))) char lds_g[];
  Ctx c = make_ctx(lds_g);
  asm volatile("global_load_dword %0, %1, %2 sc1" : "=v"(c.epoch) : "v"(0u), "s"(p.ws + WS_FLAG) : "memory");
  phaseA<true>(p, c);
  phaseB<true>(p, c);
  u32x4 hkeep;
  phaseC<true>(p, c, hkeep);
  if (c.b < 224) phaseD<true>(p, c);
  phaseE<true>(p, c, hkeep);
  if (c.b == 0 && c.tid == 0) __hip_atomic_store((GAS unsigned*)(p.ws + WS_FLAG), c.epoch, __ATOMIC_RELAXED, __HIP_MEMORY_SCOPE_AGENT);
  asm volatile("mega_code_end:");
}

extern "C" void kernel_launch(void* const* d_in, const int* in_sizes, int n_in, void* d_out, int out_size, void* d_ws, size_t ws_size, hipStream_t stream) {
  Params p;
  p.x = (const u16*)d_in[0]; p.wqkv = (const u16*)d_in[1]; p.wo = (const u16*)d_in[2];
  p.w1 = (const u16*)d_in[4]; p.b1 = (const u16*)d_in[5]; p.w2 = (const u16*)d_in[6];
  p.ws = (char*)d_ws; p.out = (u16*)d_out;
  (void)hipFuncSetAttribute((const void*)block_mega, hipFuncAttributeMaxDynamicSharedMemorySize, LDS_BYTES);
  block_mega<<<256, 512, LDS_BYTES, stream>>>(p);
}
```

```cpp
#include <hip/hip_runtime.h>
#include <hip/hip_bf16.h>
#include <type_traits>
#define STAMP_DECL
#define STAMP_AT(i)
#define STAMP_OUT(k, n)
#define MEGA 1

typedef unsigned short u16;
using bf16x8 = __attribute__((ext_vector_type(8))) short;
using s16x4 = __attribute__((ext_vector_type(4))) short;
using f32x16 = __attribute__((ext_vector_type(16))) float;
using f32x4 = __attribute__((ext_vector_type(4))) float;
using f32x2 = __attribute__((ext_vector_type(2))) float;
using u32x4 = __attribute__((ext_vector_type(4))) unsigned;
using u32x2 = __attribute__((ext_vector_type(2))) unsigned;
using bf16x2_t = __attribute__((ext_vector_type(2))) __bf16;
#define DI __device__ __forceinline__
#define LAS __attribute__((address_space(3)))
#define GAS __attribute__((address_space(1)))
#define MFMA(a, b, c) __builtin_amdgcn_mfma_f32_32x32x16_bf16((a), (b), (c), 0, 0, 0)
#define SBAR() __builtin_amdgcn_sched_barrier(0)

constexpr int S = 2048, D = 512, NH = 32, F = 1792;
constexpr float LN_EPS = 1e-5f;
constexpr float LOG2E = 1.4426950408889634f;
constexpr float QSCALE = 0.25f * LOG2E;
constexpr float BOUND_SAFE = 40.f;
constexpr int LDS_BYTES = 163840;

constexpr unsigned WS_FLAG = 0;
constexpr unsigned FL_A = 4096, FL_B = FL_A + 256 * 64, FL_C = FL_B + 256 * 64, FL_D = FL_C + 256 * 64, FL_TMO = 64;
constexpr unsigned WS_KMAX = 128u << 10, WS_STAT = 256u << 10;
constexpr unsigned WS_Q = 1u << 20, WS_KV = 3u << 20  , WS_O = 7u << 20, WS_HF = 9u << 20, WS_ACT = 11u << 20;
constexpr unsigned WS_USED = 19u << 20;

struct Params { const u16 *x, *wqkv, *wo, *w1, *b1, *w2; char* ws; u16* out; };

template <int N> using IC = std::integral_constant<int, N>;
template <int B, int E, class Fn> DI void sfor(Fn&& f) { if constexpr (B < E) { f(IC<B>{}); sfor<B + 1, E>(f); } }

DI int rfl(int v) { return __builtin_amdgcn_readfirstlane(v); }
DI unsigned cvtpk(float lo, float hi) { const f32x2 v = {lo, hi}; return __builtin_bit_cast(unsigned, __builtin_convertvector(v, bf16x2_t)); }
DI float bf_lo(unsigned u) { return __uint_as_float(u << 16); }
DI float bf_hi(unsigned u) { return __uint_as_float(u & 0xffff0000u); }
DI float dot2(unsigned a, unsigned b, float c) {
  return __builtin_amdgcn_fdot2_f32_bf16(__builtin_bit_cast(bf16x2_t, a), __builtin_bit_cast(bf16x2_t, b), c, false);
}
constexpr unsigned ONES2 = 0x3F803F80u;
#define MFMA16(a, b, c) __builtin_amdgcn_mfma_f32_16x16x32_bf16((a), (b), (c), 0, 0, 0)
DI f32x2 unpk(unsigned u) { return f32x2{__uint_as_float(u << 16), __uint_as_float(u & 0xffff0000u)}; }
DI bf16x8 rowsum_sel(int l) { const unsigned on = ((l & 15) == ((l >> 4) & 1)) ? ONES2 : 0u; return __builtin_bit_cast(bf16x8, u32x4{on, on, on, on}); }
DI void dma16(const void* sbase, unsigned voff, unsigned lds) {
  asm volatile("s_mov_b32 m0, %2\n\ts_nop 0\n\tglobal_load_lds_dwordx4 %0, %1" ::"v"(voff), "s"(sbase), "s"(lds) : "memory");
}
DI void dma16_sc1(const void* sbase, unsigned voff, unsigned lds) {
  asm volatile("s_mov_b32 m0, %2\n\ts_nop 0\n\tglobal_load_lds_dwordx4 %0, %1 sc1" ::"v"(voff), "s"(sbase), "s"(lds) : "memory");
}
template <int IMM, bool SC1> DI void gload16(bf16x8& dst, const void* sbase, unsigned voff) {
  if constexpr (SC1) asm volatile("global_load_dwordx4 %0, %1, %2 offset:%3 sc1" : "=v"(dst) : "v"(voff), "s"(sbase), "i"(IMM) : "memory");
  else asm volatile("global_load_dwordx4 %0, %1, %2 offset:%3" : "=v"(dst) : "v"(voff), "s"(sbase), "i"(IMM) : "memory");
}
template <int IMM, bool SC1 = false> DI void gload8(u32x2& dst, const void* sbase, unsigned voff) {
  if constexpr (SC1) asm volatile("global_load_dwordx2 %0, %1, %2 offset:%3 sc1" : "=v"(dst) : "v"(voff), "s"(sbase), "i"(IMM) : "memory");
  else asm volatile("global_load_dwordx2 %0, %1, %2 offset:%3" : "=v"(dst) : "v"(voff), "s"(sbase), "i"(IMM) : "memory");
}
template <bool SC1> DI void gload4(float& dst, const void* sbase, unsigned voff) {
  if constexpr (SC1) asm volatile("global_load_dword %0, %1, %2 sc1" : "=v"(dst) : "v"(voff), "s"(sbase) : "memory");
  else asm volatile("global_load_dword %0, %1, %2" : "=v"(dst) : "v"(voff), "s"(sbase) : "memory");
}
typedef __attribute__((ext_vector_type(4))) unsigned v4u_;
typedef __attribute__((ext_vector_type(2))) unsigned v2u_;
struct WsBuf { __amdgpu_buffer_rsrc_t r; };
DI __amdgpu_buffer_rsrc_t make_rsrc(void* p, unsigned bytes) { return __builtin_amdgcn_make_buffer_rsrc(p, 0, bytes, 0x00020000); }
template <bool SC1> DI void st16(__amdgpu_buffer_rsrc_t r, unsigned off, bf16x8 v) { __builtin_amdgcn_raw_buffer_store_b128(__builtin_bit_cast(v4u_, v), r, off, 0, SC1 ? 16 : 0); }
template <bool SC1> DI void st8(__amdgpu_buffer_rsrc_t r, unsigned off, u32x2 v) { __builtin_amdgcn_raw_buffer_store_b64(v, r, off, 0, SC1 ? 16 : 0); }
template <bool SC1> DI void st4(__amdgpu_buffer_rsrc_t r, unsigned off, unsigned v) { __builtin_amdgcn_raw_buffer_store_b32(v, r, off, 0, SC1 ? 16 : 0); }

template <int N> DI void wait_bar() { asm volatile("s_waitcnt vmcnt(%0) lgkmcnt(0)\n\ts_barrier" ::"i"(N) : "memory"); }
DI void lds_bar() { asm volatile("s_waitcnt lgkmcnt(0)\n\ts_barrier" ::: "memory"); }
DI s16x4 trread(const LAS char* p) {
  typedef short v4i16_t __attribute__((ext_vector_type(4)));
  return __builtin_bit_cast(s16x4, __builtin_amdgcn_ds_read_tr16_b64_v4i16((LAS v4i16_t*)p));
}
DI bf16x8 cat8(s16x4 a, s16x4 b) { return __builtin_shufflevector(a, b, 0, 1, 2, 3, 4, 5, 6, 7); }
DI bf16x8 pack8(float a0, float a1, float a2, float a3, float a4, float a5, float a6, float a7) {
  u32x4 w = {cvtpk(a0, a1), cvtpk(a2, a3), cvtpk(a4, a5), cvtpk(a6, a7)};
  return __builtin_bit_cast(bf16x8, w);
}
template <int CTRL> DI float dpp(float v) { return __uint_as_float(__builtin_amdgcn_update_dpp(0u, __float_as_uint(v), CTRL, 0xf, 0xf, true)); }
DI float sum8(float v) { v += dpp<0xB1>(v); v += dpp<0x4E>(v); v += dpp<0x141>(v); return v; }
DI float sumx32(float v) { auto r = __builtin_amdgcn_permlane32_swap(__float_as_uint(v), __float_as_uint(v), false, false); return __uint_as_float(r[0]) + __uint_as_float(r[1]); }
DI float maxx32(float v) { auto r = __builtin_amdgcn_permlane32_swap(__float_as_uint(v), __float_as_uint(v), false, false); return fmaxf(__uint_as_float(r[0]), __uint_as_float(r[1])); }
DI float maxx16(float v) { auto r = __builtin_amdgcn_permlane16_swap(__float_as_uint(v), __float_as_uint(v), false, false); return fmaxf(__uint_as_float(r[0]), __uint_as_float(r[1])); }
DI float max16(float v) { v = fmaxf(v, dpp<0xB1>(v)); v = fmaxf(v, dpp<0x4E>(v)); v = fmaxf(v, dpp<0x141>(v)); v = fmaxf(v, dpp<0x140>(v)); return v; }

DI void publish(char* ws, unsigned flag_off, unsigned epoch) {
  asm volatile("s_waitcnt vmcnt(0) lgkmcnt(0)\n\ts_barrier" ::: "memory");
  if (threadIdx.x == 0) __hip_atomic_store((GAS unsigned*)(ws + flag_off), epoch, __ATOMIC_RELAXED, __HIP_MEMORY_SCOPE_AGENT);
}
DI void await(char* ws, unsigned flag_off, int n, unsigned epoch) {
  if (threadIdx.x >= 448) {
    const int l = threadIdx.x & 63;
    GAS unsigned* f = (GAS unsigned*)(ws + flag_off + (l < n ? l : 0) * 64);
    for (unsigned spins = 0; spins < (1u << 20); ++spins) {
      const unsigned v = __hip_atomic_load(f, __ATOMIC_RELAXED, __HIP_MEMORY_SCOPE_AGENT);
      if (__all(v == epoch)) break;
      __builtin_amdgcn_s_sleep(1);
    }
  }
  asm volatile("s_waitcnt lgkmcnt(0)\n\ts_barrier" ::: "memory");
}

struct Ctx { LAS char* lds; unsigned lds0; int tid, l, w, hh, b; unsigned epoch; };

template <bool MG> DI void phaseA(const Params& p, Ctx& c) {
  STAMP_DECL STAMP_AT(0);
  char* ws = p.ws;
  LAS char* lds = c.lds;
  const int l = c.l, w = c.w, hh = c.hh, b = c.b;
  const int rb = 8 * ((b & 7) >> 1) + ((b >> 3) & 7), hg = 4 * (b & 1) + (b >> 6);
  bf16x8 ft[4] = {};
  if constexpr (MG) {
    ft[0] = *(const GAS bf16x8*)((const char*)p.x + (size_t)b * 8192 + w * 1024 + l * 16);
    ft[1] = *(const GAS bf16x8*)((const char*)p.wqkv + (size_t)b * 6144 + (w < 6 ? w : 5) * 1024 + l * 16);
    unsigned long long pc; unsigned len;
    asm volatile("s_getpc_b64 %0\n.Lmega_here%=:\n\ts_mov_b32 %1, mega_code_end-.Lmega_here%=" : "=s"(pc), "=s"(len));
    const unsigned j = (unsigned)(b >> 3);
    const unsigned oc = j * 1024u + (w & 1) * 32768u + l * 16u, pg = (j * 10u + (l < 10 ? l : 0)) * 65536u;
    const char* base = w < 2 ? (const char*)pc : w == 2 ? (const char*)ws : (const char*)p.out;
    const unsigned off = w < 2 ? (oc + 16 <= len ? oc : 0u) : w == 2 ? (pg < WS_USED ? pg : 0u) : j * 65536u;
    if (w < 4) ft[2] = *(const GAS bf16x8*)(base + off);
  }
  const int q4 = (l & 15) >> 2, p4 = l & 3, blk = (l >> 4) & 1;
  LAS float* tmean = (LAS float*)(lds + 0);
  LAS float* trstd = tmean + 64;
  LAS float* tcs = tmean + 128;
  f32x16 acc[3] = {};
  const int m = w & 1, g = (w >> 1) & 1;
  if (w < 4) {
    const bf16x8 sel = rowsum_sel(l);
    const int swz = (l >> 1) & 7;
    const int xrow = (32 * m + (l & 31)) * 128;
    const int wa = 8192 + (8 * hh + q4) * 128 + (((4 * g + 2 * blk + (p4 >> 1)) ^ (((q4 >> 1) & 1) << 2)) * 16) + (p4 & 1) * 8;
    int xo[4], wo_[2];
#pragma unroll
    for (int i = 0; i < 4; ++i) xo[i] = xrow + (((2 * (i ^ m) + hh) ^ swz) * 16);
    wo_[0] = wa + m * 2048; wo_[1] = wa + (1 - m) * 2048;
    bf16x8 xf[2][4], wf[2][4][3];
    f32x4 cs[3] = {};
    auto ld_w = [&](auto SET, const LAS char* sl, int i, int sgm) { constexpr int q = SET.value; const int o = wo_[i & 1] + (i & 2) * 2048 + sgm * 8192;
      wf[q][i][sgm] = cat8(trread(sl + o), trread(sl + o + 512)); };
    lds_bar();
    STAMP_AT(2);
#pragma unroll
    for (int i = 0; i < 4; ++i) { xf[0][i] = *(const LAS bf16x8*)(lds + xo[i]); for (int sgm = 0; sgm < 3; ++sgm) ld_w(IC<0>{}, lds, i, sgm); }
    sfor<0, 8>([&](auto ST) {
      constexpr int st = ST.value, q = st & 1, qn = (st + 1) & 1;
      if constexpr (st + 1 < 8) lds_bar(); else asm volatile("s_waitcnt lgkmcnt(0)" ::: "memory");
      asm volatile("" : "+v"(xf[q][0]), "+v"(xf[q][1]), "+v"(xf[q][2]), "+v"(xf[q][3]), "+v"(wf[q][0][0]), "+v"(wf[q][0][1]), "+v"(wf[q][0][2]), "+v"(wf[q][1][0]), "+v"(wf[q][1][1]), "+v"(wf[q][1][2]),
                   "+v"(wf[q][2][0]), "+v"(wf[q][2][1]), "+v"(wf[q][2][2]), "+v"(wf[q][3][0]), "+v"(wf[q][3][1]), "+v"(wf[q][3][2]));
      const LAS char* sn = lds + ((st + 1) % 5) * 32768;
#pragma unroll
      for (int i = 0; i < 4; ++i) {
        acc[0] = MFMA(wf[q][i][0], xf[q][i], acc[0]);
        SBAR();
        if constexpr (st + 1 < 8) { ld_w(IC<qn>{}, sn, i, 0); xf[qn][i] = *(const LAS bf16x8*)(sn + xo[i]); }
        SBAR();
        acc[1] = MFMA(wf[q][i][1], xf[q][i], acc[1]);
        SBAR();
        if constexpr (st + 1 < 8) ld_w(IC<qn>{}, sn, i, 1);
        SBAR();
        acc[2] = MFMA(xf[q][i], wf[q][i][2], acc[2]);
        SBAR();
        if constexpr (st + 1 < 8) ld_w(IC<qn>{}, sn, i, 2);
        if ((i & 1) == 0) { cs[0] = MFMA16(wf[q][i][0], sel, cs[0]); cs[1] = MFMA16(wf[q][i][1], sel, cs[1]); cs[2] = MFMA16(wf[q][i][2], sel, cs[2]); }
        SBAR();
      }
    });
    if ((l & 15) < 2) {
#pragma unroll
      for (int t = 0; t < 3; ++t) *(LAS f32x4*)(tcs + (w * 3 + t) * 32 + 16 * (l & 15) + 4 * (l >> 4)) = cs[t];
    }
  } else {
    const int h = w - 4;
    const unsigned xo0 = (unsigned)(64 * rb + 16 * h + (l >> 3)) * 1024u + (unsigned)(((l & 7) ^ ((l >> 4) & 7)) * 16);
    const unsigned xo1 = (unsigned)(64 * rb + 16 * h + 8 + (l >> 3)) * 1024u + (unsigned)(((l & 7) ^ ((4 + (l >> 4)) & 7)) * 16);
    const unsigned wof = (unsigned)(16 * h + (l >> 3)) * 3072u + (unsigned)hg * 128u + (unsigned)(((l & 7) ^ (((l >> 4) & 1) << 2)) * 16);
    auto issue = [&](int st) {
      const unsigned dst = c.lds0 + (st % 5) * 32768 + h * 2048;
      const char* wsrc = (const char*)p.wqkv + (size_t)st * 64 * 3072;
      dma16((const char*)p.x + st * 128, xo0, dst);
      dma16((const char*)p.x + st * 128, xo1, dst + 1024);
#pragma unroll
      for (int sgm = 0; sgm < 3; ++sgm) { dma16(wsrc + sgm * 1024, wof, dst + 8192 + sgm * 8192); dma16(wsrc + sgm * 1024 + 8 * 3072, wof, dst + 8192 + sgm * 8192 + 1024); }
    };
    issue(0); issue(1); issue(2); issue(3); issue(4);
    STAMP_AT(1);
    f32x2 rs[2] = {}, rq[2] = {};
    u32x4 sv[2][2];
    wait_bar<32>();
    sv[0][0] = *(const LAS u32x4*)(lds + h * 2048 + l * 16); sv[0][1] = *(const LAS u32x4*)(lds + h * 2048 + 1024 + l * 16);
    sfor<0, 8>([&](auto ST) {
      constexpr int st = ST.value, q = st & 1, qn = (st + 1) & 1;
      if constexpr (st + 1 < 8) {
        constexpr int last = 4 + st > 7 ? 7 : 4 + st;
        wait_bar<8 * (last - (st + 1))>();
        if constexpr (st + 5 < 8) issue(st + 5);
        const LAS char* sn = lds + ((st + 1) % 5) * 32768 + h * 2048 + l * 16;
        sv[qn][0] = *(const LAS u32x4*)sn; sv[qn][1] = *(const LAS u32x4*)(sn + 1024);
      }
#pragma unroll
      for (int r = 0; r < 2; ++r)
#pragma unroll
        for (int i = 0; i < 4; ++i) { const f32x2 v = unpk(sv[q][r][i]); rs[r] += v; rq[r] = __builtin_elementwise_fma(v, v, rq[r]); }
      asm volatile("" : "+v"(rs[0]), "+v"(rq[0]), "+v"(rs[1]), "+v"(rq[1]));
    });
#pragma unroll
    for (int r = 0; r < 2; ++r) {
      const float s1 = sum8(rs[r][0] + rs[r][1]), s2 = sum8(rq[r][0] + rq[r][1]);
      if ((l & 7) == 0) { const float mean = s1 * (1.f / D); const int row = 16 * h + 8 * r + (l >> 3);
        tmean[row] = mean; trstd[row] = __builtin_amdgcn_rsqf(fmaxf(s2 * (1.f / D) - mean * mean, 0.f) + LN_EPS); }
    }
  }
  STAMP_AT(4);
  if constexpr (MG) { asm volatile("" : "+v"(c.epoch), "+v"(ft[0]), "+v"(ft[1]), "+v"(ft[2])); c.epoch += 1u; }
  lds_bar();
  const __amdgpu_buffer_rsrc_t rw = make_rsrc(ws, WS_USED);
  if (w < 4) {
    const int tile = 2 * rb + m;
    const float mean_l = tmean[32 * m + (l & 31)], rstd_l = trstd[32 * m + (l & 31)];
    f32x4 cv[2][4], mm[4], rr[4];
#pragma unroll
    for (int gg = 0; gg < 4; ++gg) {
#pragma unroll
      for (int t = 0; t < 2; ++t) cv[t][gg] = *(const LAS f32x4*)(tcs + (w * 3 + t) * 32 + 8 * gg + 4 * hh) + *(const LAS f32x4*)(tcs + ((w ^ 1) * 3 + t) * 32 + 8 * gg + 4 * hh);
      mm[gg] = *(const LAS f32x4*)(tmean + 32 * m + 8 * gg + 4 * hh); rr[gg] = *(const LAS f32x4*)(trstd + 32 * m + 8 * gg + 4 * hh);
    }
    const int n = l & 31;
    const float ncs = -(tcs[(w * 3 + 2) * 32 + n] + tcs[((w ^ 1) * 3 + 2) * 32 + n]);
#pragma unroll
    for (int s = 0; s < 2; ++s) {
      f32x2 v[4];
#pragma unroll
      for (int j = 0; j < 4; ++j) { const int r = 8 * s + 2 * j; const f32x2 a = {acc[2][r], acc[2][r + 1]}, m2 = {mm[r >> 2][r & 3], mm[r >> 2][(r & 3) + 1]}, r2 = {rr[r >> 2][r & 3], rr[r >> 2][(r & 3) + 1]};
        v[j] = __builtin_elementwise_fma(m2, f32x2{ncs, ncs}, a) * r2; }
      st16<MG>(rw, WS_KV + (unsigned)((4 * hg + 2 * g + (n >> 4)) * 64 + tile) * 2048 + 1024 + s * 512 + hh * 256 + (n & 15) * 16,
               pack8(v[0][0], v[0][1], v[1][0], v[1][1], v[2][0], v[2][1], v[3][0], v[3][1]));
    }
#pragma unroll
    for (int t = 1; t >= 0; --t) {
      const float sc = t == 0 ? rstd_l * QSCALE : rstd_l, nm = -mean_l * sc;
#pragma unroll
      for (int s = 0; s < 2; ++s) {
        f32x2 v[4];
#pragma unroll
        for (int j = 0; j < 4; ++j) { const int r = 8 * s + 2 * j; const f32x2 a = {acc[t][r], acc[t][r + 1]}, cc = {cv[t][r >> 2][r & 3], cv[t][r >> 2][(r & 3) + 1]};
          v[j] = __builtin_elementwise_fma(a, f32x2{sc, sc}, cc * nm); }
        const int head = 4 * hg + 2 * g + s;
        st16<MG>(rw, (t == 0 ? WS_Q + (unsigned)(head * 64 + tile) * 1024 : WS_KV + (unsigned)(head * 64 + tile) * 2048) + l * 16, pack8(v[0][0], v[0][1], v[1][0], v[1][1], v[2][0], v[2][1], v[3][0], v[3][1]));
        if (t == 1) {
          f32x2 n2 = v[0] * v[0]; n2 = __builtin_elementwise_fma(v[1], v[1], n2); n2 = __builtin_elementwise_fma(v[2], v[2], n2); n2 = __builtin_elementwise_fma(v[3], v[3], n2);
          const float mx = maxx16(max16(sumx32(n2[0] + n2[1])));
          if (l == 0) st4<MG>(rw, WS_KMAX + (unsigned)(head * 64 + tile) * 4, __float_as_uint(mx));
        }
      }
    }
  }
  STAMP_AT(5);
  if constexpr (MG) publish(ws, FL_A + (hg * 32 + rb) * 64, c.epoch);
  STAMP_AT(6);
  STAMP_OUT(0, 7);
}

DI unsigned wpiece_lane(int l, unsigned row_bytes) { return (unsigned)(l >> 3) * row_bytes + (unsigned)(((l & 7) ^ (((l >> 4) & 1) << 2)) * 16); }
template <int NW, int W0 = 0> DI void prefetch_wo(const Params& p, const Ctx& c) {
  if (c.w < W0 || c.w >= W0 + NW) return;
  const unsigned off = wpiece_lane(c.l, 1024) + (unsigned)(c.b >> 5) * 128u;
  for (int pc = c.w - W0; pc < 64; pc += NW) dma16((const char*)p.wo + (size_t)(8 * pc) * 1024, off, c.lds0 + pc * 1024);
}
DI constexpr unsigned w1_stage_base(int j) { return j < 6 ? 65536 + j * 16384 : (j - 6) * 16384; }
template <int NW, int W0 = 0> DI void prefetch_w1(const Params& p, const Ctx& c, int p0, int p1) {
  if (c.w < W0 || c.w >= W0 + NW) return;
  const unsigned off = wpiece_lane(c.l, F * 2) + (unsigned)(c.b >> 4) * 256u;
  for (int pc = p0 + c.w - W0; pc < p1; pc += NW) {
    const int j = pc >> 4, seg = (pc >> 3) & 1, r8 = pc & 7;
    dma16((const char*)p.w1 + (size_t)(64 * j + 8 * r8) * (F * 2) + seg * 128, off, c.lds0 + w1_stage_base(j) + seg * 8192 + r8 * 1024);
  }
}
template <int NW> DI void prefetch_w2(const Params& p, const Ctx& c, int p0, int p1) {
  if (c.w >= NW) return;
  const unsigned off = wpiece_lane(c.l, 1024) + (unsigned)(c.b >> 5) * 128u;
  for (int pc = p0 + c.w; pc < p1; pc += NW) {
    const int st = pc >> 4, kq = (pc >> 2) & 3, r8 = pc & 3;
    dma16((const char*)p.w2 + (size_t)(448 * kq + 32 * st + 8 * r8) * 1024, off, c.lds0 + ((st + 4) % 10) * 16384 + kq * 4096 + r8 * 1024);
  }
}

DI void warm_first(const Params& p, const Ctx& c) {
  const int h = c.w - 4;
  const unsigned off1 = (unsigned)c.b * 7168u + (unsigned)h * 2048u + (unsigned)c.l * 16u;
  dma16((const char*)p.w1, off1, c.lds0 + 139520);
  dma16((const char*)p.w2, off1, c.lds0 + 139520);
  if (h < 3) { dma16((const char*)p.w1 + 1024, off1, c.lds0 + 139520); dma16((const char*)p.w2 + 1024, off1, c.lds0 + 139520); }
}
DI void warm_w1_late(const Params& p, const Ctx& c) {
  const int h = c.w - 4;
  const unsigned j = (unsigned)(c.b >> 3);
  for (int pc = h; pc < 35; pc += 4)
    dma16((const char*)p.w1, (pc < 21 ? 128u * 3584u + j * 21504u + pc * 1024u : 384u * 3584u + j * 14336u + (pc - 21) * 1024u) + c.l * 16u, c.lds0 + 139520);
}
DI void warm_w2(const Params& p, const Ctx& c) {
  const int h = c.w - 4;
  const unsigned off = (unsigned)(c.b >> 3) * 57344u + (unsigned)h * 14336u + (unsigned)c.l * 16u;
#pragma unroll
  for (int i = 0; i < 14; ++i) dma16((const char*)p.w2 + i * 1024, off, c.lds0 + 139520);
}

constexpr int B_ONES = 131072;
template <bool MG> DI void phaseB(const Params& p, const Ctx& c) {
  STAMP_DECL STAMP_AT(0);
  char* ws = p.ws;
  LAS char* lds = c.lds;
  const int l = c.l, w = c.w, hh = c.hh, b = c.b, tid = c.tid;
  const int head = 4 * (b & 7) + ((b >> 3) & 3), qb = b >> 5;
  for (int i = tid; i < (4096 + 512) / 16; i += 512) { const unsigned v = (i & 31) == 0 ? ONES2 : 0u; ((LAS u32x4*)(lds + B_ONES))[i] = u32x4{v, v, v, v}; }
  if constexpr (MG) await(ws, FL_A + (head >> 2) * 32 * 64, 32, c.epoch);
  STAMP_AT(1);
  const unsigned l16 = l * 16;
  if (w >= 4) {
    const char* kvg = ws + WS_KV + (size_t)head * 131072;
    const int h = w - 4;
    auto piece = [&](int pc) { if constexpr (MG) dma16_sc1(kvg + pc * 1024, l16, c.lds0 + pc * 1024); else dma16(kvg + pc * 1024, l16, c.lds0 + pc * 1024); };
#pragma unroll
    for (int i = 0; i < 4; ++i) piece(4 * h + i);
#pragma unroll
    for (int i = 0; i < 12; ++i) piece(16 + 4 * i + h);
    wait_bar<12>();
#pragma unroll
    for (int i = 0; i < 16; ++i) piece(64 + 4 * i + h);
    wait_bar<16>();
    if constexpr (MG) warm_first(p, c);
    wait_bar<0>();
    if constexpr (MG) { if (b < 224) prefetch_w1<4, 4>(p, c, 80, 96); }
    lds_bar();
    if constexpr (MG) { prefetch_wo<4, 4>(p, c); warm_w1_late(p, c); }
    lds_bar();
    if constexpr (MG) { if (b < 224) prefetch_w1<4, 4>(p, c, 0, 32); warm_w2(p, c); }
  } else {
    bf16x8 qa, qb_;
    float kmx;
    gload4<MG>(kmx, ws + WS_KMAX + head * 256, l * 4);
    gload16<0, MG>(qa, ws + WS_Q + (size_t)(head * 64 + qb * 8 + w) * 1024, l * 16);
    gload16<0, MG>(qb_, ws + WS_Q + (size_t)(head * 64 + qb * 8 + w + 4) * 1024, l * 16);
    asm volatile("s_waitcnt vmcnt(0)" : "+v"(qa), "+v"(qb_), "+v"(kmx)::"memory");
    kmx = maxx32(maxx16(max16(kmx)));
    auto qbound = [&](bf16x8 q) { const u32x4 u = __builtin_bit_cast(u32x4, q); f32x2 t = {0.f, 0.f}; for (int i = 0; i < 4; ++i) { const f32x2 v = unpk(u[i]); t = __builtin_elementwise_fma(v, v, t); }
      return __builtin_amdgcn_sqrtf(sumx32(t[0] + t[1]) * kmx) * 1.01f + 1e-6f; };
    const float bna = qbound(qa), bnb = qbound(qb_);
    const bool safe = __all(bna <= BOUND_SAFE && bnb <= BOUND_SAFE);
    f32x16 oa = {}, ob = {};
    const int lo5 = l & 31;
    const bool isv = lo5 < 16;
    const int va0 = isv ? 1024 + hh * 256 + lo5 * 16 : B_ONES + (lo5 == 16 ? 0 : (lo5 >= 20 && lo5 < 28) ? 64 : 16);
    lds_bar();
    STAMP_AT(2);
    if (safe) {
      f32x16 nga, ngb; for (int i = 0; i < 16; ++i) { nga[i] = -bna; ngb[i] = -bnb; }
      f32x16 s0, s1;
      bf16x8 pa, pb = {};
      bf16x8 kf, v0, v1 = {};
      unsigned ka = c.lds0 + l * 16, va = c.lds0 + va0;
      const unsigned vstep = isv ? 4096u : 0u;
      asm volatile("ds_read_b128 %1, %3\n\tds_read_b128 %2, %4\n\ts_waitcnt lgkmcnt(0)\n\t"
                   "v_mfma_f32_32x32x16_bf16 %0, %1, %5, %6\n\ts_nop 7\n\ts_nop 3"
                   : "=&{v[192:207]}"(s0), "=&v"(kf), "=&v"(v0) : "v"(ka), "v"(va), "v"(qa), "v"(nga) : "memory");
#define E_(b, i) "v_exp_f32 v[" #b "+" #i "], v[" #b "+" #i "]\n\t"
#define C_(d, i, b, j, k) "v_cvt_pk_bf16_f32 v[" #d "+" #i "], v[" #b "+" #j "], v[" #b "+" #k "]\n\t"
#define MID_(SC) E_(SC, 4) E_(SC, 5) E_(SC, 6) E_(SC, 7) C_(224, 0, SC, 0, 1) C_(224, 1, SC, 2, 3) E_(SC, 8) E_(SC, 9) C_(224, 2, SC, 4, 5) C_(224, 3, SC, 6, 7) E_(SC, 10) E_(SC, 11)
#define END_(SC) E_(SC, 12) E_(SC, 13) E_(SC, 14) E_(SC, 15) C_(228, 0, SC, 8, 9) C_(228, 1, SC, 10, 11) C_(228, 2, SC, 12, 13) C_(228, 3, SC, 14, 15)
#define UNIT_A(KOFF, V1OFF) \
      "v_mfma_f32_32x32x16_bf16 v[208:223], %[kf], %[qb], %[ngb]\n\t" \
      "ds_read_b128 %[kf], %[ka] offset:" #KOFF "\n\t" \
      E_(192, 0) E_(192, 1) E_(192, 2) E_(192, 3) \
      "v_mfma_f32_32x32x16_bf16 %[ob], %[v1], v[228:231], %[ob]\n\t" \
      "ds_read_b128 %[v1], %[va] offset:" #V1OFF "\n\t" \
      MID_(192) \
      "s_waitcnt lgkmcnt(2)\n\t" \
      "v_mfma_f32_32x32x16_bf16 %[oa], %[v0], v[224:227], %[oa]\n\t" \
      END_(192)
#define UNIT_B(V0OFF) \
      "s_waitcnt lgkmcnt(0)\n\t" \
      "v_mfma_f32_32x32x16_bf16 v[192:207], %[kf], %[qa], %[nga]\n\t" \
      E_(208, 0) E_(208, 1) E_(208, 2) E_(208, 3) \
      "v_mfma_f32_32x32x16_bf16 %[oa], %[v1], v[228:231], %[oa]\n\t" \
      MID_(208) \
      "v_mfma_f32_32x32x16_bf16 %[ob], %[v0], v[224:227], %[ob]\n\t" \
      "ds_read_b128 %[v0], %[va] offset:" #V0OFF "\n\t" \
      END_(208)
#define RUN_GROUPS(NG) do { int cnt_ = (NG); \
      asm volatile("1:\n\t" UNIT_A(2048, 512) UNIT_B(2048) UNIT_A(4096, 2560) UNIT_B(4096) \
        "v_add_u32 %[ka], 0x1000, %[ka]\n\tv_add_u32 %[va], %[va], %[vs]\n\t" \
        "s_sub_u32 %[cnt], %[cnt], 1\n\ts_cmp_lg_u32 %[cnt], 0\n\ts_cbranch_scc1 1b\n\ts_waitcnt lgkmcnt(0)\n\ts_nop 7\n\ts_nop 3" \
        : "+{v[192:207]}"(s0), "=&{v[208:223]}"(s1), "=&{v[224:227]}"(pa), "+{v[228:231]}"(pb), [oa] "+v"(oa), [ob] "+v"(ob), [kf] "+v"(kf), [v0] "+v"(v0), [v1] "+v"(v1), \
          [ka] "+v"(ka), [va] "+v"(va), [cnt] "+s"(cnt_) \
        : [qa] "v"(qa), [qb] "v"(qb_), [nga] "v"(nga), [ngb] "v"(ngb), [vs] "v"(vstep) : "memory", "scc"); } while (0)
      RUN_GROUPS(3);
      lds_bar();
      STAMP_AT(3);
      RUN_GROUPS(12);
      lds_bar();
      RUN_GROUPS(1);
      lds_bar();
      RUN_GROUPS(8);
      lds_bar();
      RUN_GROUPS(8);
      ob = MFMA(v1, pb, ob);
    } else {
      float ca = -3.0e38f, cb = -3.0e38f;
      const f32x16 zero = {};
      auto online = [&](const bf16x8& kf, const bf16x8& v0, const bf16x8& v1, const bf16x8& q, f32x16& o, float& mrun) {
        f32x16 s = MFMA(kf, q, zero);
        float tm = s[0];
#pragma unroll
        for (int i = 1; i < 16; ++i) tm = fmaxf(tm, s[i]);
        tm = maxx32(tm);
        const float mn = fmaxf(mrun, tm), f = __builtin_amdgcn_exp2f(mrun - mn);
        mrun = mn;
#pragma unroll
        for (int i = 0; i < 16; ++i) { o[i] *= f; s[i] = __builtin_amdgcn_exp2f(s[i] - mn); }
        o = MFMA(v0, pack8(s[0], s[1], s[2], s[3], s[4], s[5], s[6], s[7]), o);
        o = MFMA(v1, pack8(s[8], s[9], s[10], s[11], s[12], s[13], s[14], s[15]), o);
      };
      for (int t = 0; t < 64; ++t) {
        if (t == 6 || t == 30 || t == 32 || t == 48) lds_bar();
        const bf16x8 kf = *(const LAS bf16x8*)(lds + l * 16 + t * 2048);
        const int vo = isv ? va0 + t * 2048 : va0;
        const bf16x8 v0 = *(const LAS bf16x8*)(lds + vo), v1 = *(const LAS bf16x8*)(lds + vo + 512);
        online(kf, v0, v1, qa, oa, ca);
        online(kf, v0, v1, qb_, ob, cb);
      }
    }
    STAMP_AT(4);
    const float ia = __builtin_amdgcn_rcpf(sumx32(oa[8])), ib = __builtin_amdgcn_rcpf(sumx32(ob[8]));
    const __amdgpu_buffer_rsrc_t rw = make_rsrc(ws, WS_USED);
    st16<MG>(rw, WS_O + (unsigned)(((qb * 8 + w) * 32 + head) * 64 + l) * 16, pack8(oa[0] * ia, oa[1] * ia, oa[2] * ia, oa[3] * ia, oa[4] * ia, oa[5] * ia, oa[6] * ia, oa[7] * ia));
    st16<MG>(rw, WS_O + (unsigned)(((qb * 8 + w + 4) * 32 + head) * 64 + l) * 16, pack8(ob[0] * ib, ob[1] * ib, ob[2] * ib, ob[3] * ib, ob[4] * ib, ob[5] * ib, ob[6] * ib, ob[7] * ib));
  }
  STAMP_AT(5);
  if constexpr (MG) {
    publish(ws, FL_B + (qb * 32 + head) * 64, c.epoch);
    if (b < 224) prefetch_w1<7>(p, c, 32, 80);
  }
  STAMP_AT(6);
  STAMP_OUT(1, 7);
}

template <int KQ, int HGAP> DI void quarter_reduce_t(int m, int l, LAS f32x4* xch, const f32x16 (&acc)[2], float (&val)[8]) {
#pragma unroll
  for (int d = 0; d < 4; ++d) {
    if (d == KQ) continue;
    xch[(d >> 1) * HGAP + (((d * 4 + KQ) * 2 + m) * 2 + 0) * 64 + l] = f32x4{acc[d >> 1][8 * (d & 1)], acc[d >> 1][8 * (d & 1) + 1], acc[d >> 1][8 * (d & 1) + 2], acc[d >> 1][8 * (d & 1) + 3]};
    xch[(d >> 1) * HGAP + (((d * 4 + KQ) * 2 + m) * 2 + 1) * 64 + l] = f32x4{acc[d >> 1][8 * (d & 1) + 4], acc[d >> 1][8 * (d & 1) + 5], acc[d >> 1][8 * (d & 1) + 6], acc[d >> 1][8 * (d & 1) + 7]};
  }
  lds_bar();
  f32x4 t[4][2];
#pragma unroll
  for (int s = 0; s < 4; ++s) { if (s == KQ) continue; t[s][0] = xch[(KQ >> 1) * HGAP + (((KQ * 4 + s) * 2 + m) * 2 + 0) * 64 + l]; t[s][1] = xch[(KQ >> 1) * HGAP + (((KQ * 4 + s) * 2 + m) * 2 + 1) * 64 + l]; }
#pragma unroll
  for (int j = 0; j < 8; ++j) val[j] = acc[KQ >> 1][8 * (KQ & 1) + j];
#pragma unroll
  for (int s = 0; s < 4; ++s) { if (s == KQ) continue; for (int j = 0; j < 4; ++j) { val[j] += t[s][0][j]; val[4 + j] += t[s][1][j]; } }
}
template <int HGAP> DI void quarter_reduce(const Ctx& c, LAS f32x4* xch, const f32x16 (&acc)[2], float (&val)[8]) {
  const int m = c.w & 1, kq = c.w >> 1;
  if (kq == 0) quarter_reduce_t<0, HGAP>(m, c.l, xch, acc, val);
  else if (kq == 1) quarter_reduce_t<1, HGAP>(m, c.l, xch, acc, val);
  else if (kq == 2) quarter_reduce_t<2, HGAP>(m, c.l, xch, acc, val);
  else quarter_reduce_t<3, HGAP>(m, c.l, xch, acc, val);
}

template <bool MG> DI void phaseC(const Params& p, const Ctx& c, u32x4& hkeep) {
  STAMP_DECL STAMP_AT(0);
  char* ws = p.ws;
  LAS char* lds = c.lds;
  const int l = c.l, w = c.w, hh = c.hh, b = c.b;
  const int rb = (b & 7) * 4 + ((b >> 3) & 3), cb = b >> 5;
  const int m = w & 1, kq = w >> 1;
  const int rt = 2 * rb + m, kstep = 4 * cb + kq;
  const GAS u32x2* xp = (const GAS u32x2*)((const char*)p.x + (size_t)rt * 32 * 1024 + kstep * 32 + (l & 31) * 1024 + hh * 8);
  const u32x2 xr0 = xp[0], xr1 = xp[2];
  const int q4 = (l & 15) >> 2, p4 = l & 3, blk = (l >> 4) & 1;
  const int wa0 = (128 * kq + 4 * hh + q4) * 128 + (((2 * blk + (p4 >> 1)) ^ (((q4 >> 1) & 1) << 2)) * 16) + (p4 & 1) * 8;
  const int wa1 = wa0 ^ 64;
  if constexpr (!MG) { prefetch_wo<8>(p, c); wait_bar<0>(); }
  bf16x8 wf[8][2];
#pragma unroll
  for (int i = 0; i < 8; ++i) {
    wf[i][0] = cat8(trread(lds + wa0 + i * 2048), trread(lds + wa0 + i * 2048 + 1024));
    wf[i][1] = cat8(trread(lds + wa1 + i * 2048), trread(lds + wa1 + i * 2048 + 1024));
  }
  if constexpr (MG) await(ws, FL_B + (rb >> 2) * 32 * 64, 32, c.epoch); else lds_bar();
  STAMP_AT(1);
  const char* abase = ws + WS_O + ((size_t)rt * 32 + kq * 8) * 1024;
  bf16x8 af[8];
  sfor<0, 8>([&](auto I) { constexpr int i = I.value; gload16<(i & 3) * 1024, MG>(af[i], abase + (i >> 2) * 4096, l * 16); });
  f32x16 acc[2] = {};
  STAMP_AT(2);
  sfor<0, 8>([&](auto I) {
    constexpr int i = I.value;
    asm volatile("s_waitcnt vmcnt(%1)" : "+v"(af[i]) : "i"(7 - i) : "memory");
    acc[0] = MFMA(wf[i][0], af[i], acc[0]);
    acc[1] = MFMA(wf[i][1], af[i], acc[1]);
    SBAR();
  });
  STAMP_AT(3);
  float val[8];
  quarter_reduce<0>(c, (LAS f32x4*)lds, acc, val);
  const u32x4 o = {cvtpk(val[0] + bf_lo(xr0[0]), val[1] + bf_hi(xr0[0])), cvtpk(val[2] + bf_lo(xr0[1]), val[3] + bf_hi(xr0[1])),
                   cvtpk(val[4] + bf_lo(xr1[0]), val[5] + bf_hi(xr1[0])), cvtpk(val[6] + bf_lo(xr1[1]), val[7] + bf_hi(xr1[1]))};
  hkeep = o;
  const __amdgpu_buffer_rsrc_t rw = make_rsrc(ws, WS_USED);
  st16<MG>(rw, WS_HF + (unsigned)((rt * 32 + kstep) * 64 + l) * 16, __builtin_bit_cast(bf16x8, o));
  f32x2 sm = {0.f, 0.f}, sq = {0.f, 0.f};
#pragma unroll
  for (int i = 0; i < 4; ++i) { const f32x2 v = unpk(o[i]); sm += v; sq = __builtin_elementwise_fma(v, v, sq); }
  LAS f32x2* tst = (LAS f32x2*)(lds + 0);
  tst[((m * 4 + kq) * 2 + hh) * 32 + (l & 31)] = f32x2{sm[0] + sm[1], sq[0] + sq[1]};
  lds_bar();
  if (w < 2 && l < 32) {
    f32x2 t8[8];
#pragma unroll
    for (int i = 0; i < 8; ++i) t8[i] = tst[(w * 8 + i) * 32 + l];
    const f32x2 t = ((t8[0] + t8[1]) + (t8[2] + t8[3])) + ((t8[4] + t8[5]) + (t8[6] + t8[7]));
    st8<MG>(rw, WS_STAT + (unsigned)(((2 * rb + w) * 8 + cb) * 32 + l) * 8, __builtin_bit_cast(u32x2, t));
  }
  STAMP_AT(4);
  if constexpr (MG) {
    publish(ws, FL_C + ((rb >> 1) * 16 + (rb & 1) * 8 + cb) * 64, c.epoch);
    if (b < 224) prefetch_w1<7>(p, c, 96, 128);
  }
  STAMP_AT(5);
  STAMP_OUT(2, 6);
}

constexpr int D_WIN = 12;
template <bool MG> DI void phaseD(const Params& p, const Ctx& c) {
  STAMP_DECL STAMP_AT(0);
  char* ws = p.ws;
  LAS char* lds = c.lds;
  const int l = c.l, w = c.w, hh = c.hh, b = c.b;
  const int rb = 2 * (b & 7) + ((b >> 3) & 1), cb = b >> 4;
  const int m = w & 3, nh = w >> 2;
  const int rt = 4 * rb + m;
  LAS float* tcs = (LAS float*)(lds + 49152);
  LAS float* tb = tcs + 128;
  if (c.tid < 128) tcs[c.tid] = 0.f;
  const bf16x8 bv = *(const GAS bf16x8*)((const char*)p.b1 + cb * 256 + (l & 15) * 16);
  if constexpr (!MG) { prefetch_w1<8>(p, c, 0, 128); wait_bar<0>(); }
  const int par = m >> 1;
  const int q4 = (l & 15) >> 2, p4 = l & 3, blk = (l >> 4) & 1;
  const int wl = nh * 8192 + (4 * hh + q4) * 128 + (((4 * (m & 1) + 2 * blk + (p4 >> 1)) ^ (((q4 >> 1) & 1) << 2)) * 16) + (p4 & 1) * 8;
  const int wa_e = wl + par * 2048, wa_o = wl + (1 - par) * 2048;
  bf16x8 wf[3][2];
  auto load_w = [&](auto I) {
    constexpr int i = I.value;
    constexpr int o = (int)w1_stage_base(i >> 2) + (i & 2) * 2048;
    const int wa = (i & 1) ? wa_o : wa_e;
    wf[i % 3][0] = cat8(trread(lds + wa + o), trread(lds + wa + o + 1024));
    wf[i % 3][1] = cat8(trread(lds + (wa ^ 64) + o), trread(lds + (wa ^ 64) + o + 1024));
  };
  load_w(IC<0>{}); load_w(IC<1>{});
  if constexpr (MG) await(ws, FL_C + rb * 16 * 64, 16, c.epoch); else lds_bar();
  STAMP_AT(1);
  const char* abase = ws + WS_HF + (size_t)rt * 32 * 1024;
  const unsigned aoff0 = l * 16 + par * 1024, aoff1 = l * 16 + (1 - par) * 1024;
  bf16x8 af[32];
  auto load_a = [&](auto I) { constexpr int i = I.value;
    if constexpr (i & 1) gload16<((i >> 1) & 1) * 2048, MG>(af[i], abase + (i >> 2) * 4096, aoff1);
    else gload16<((i >> 1) & 1) * 2048, MG>(af[i], abase + (i >> 2) * 4096, aoff0); };
  sfor<0, 4>(load_a);
  u32x2 st[4];
  sfor<0, 4>([&](auto I) { constexpr int i = I.value; gload8<i * 256, MG>(st[i], ws + WS_STAT + (size_t)rt * 2048, (l & 31) * 8 + hh * 1024); });
  sfor<4, D_WIN>(load_a);
  f32x16 acc[2] = {};
  f32x4 cs = {};
  const bf16x8 sel = rowsum_sel(l);
  STAMP_AT(2);
  sfor<0, 32>([&](auto I) {
    constexpr int i = I.value;
    if constexpr (i + D_WIN < 32) load_a(IC<i + D_WIN>{});
    constexpr int issued = i + D_WIN < 32 ? i + D_WIN + 1 : 32;
    asm volatile("s_waitcnt vmcnt(%1)" : "+v"(af[i]) : "i"(issued - i - 1 + (i < 4 ? 4 : 0)) : "memory");
    if constexpr (i == 20) asm volatile("s_barrier" ::: "memory");
    acc[0] = MFMA(wf[i % 3][0], af[i], acc[0]);
    acc[1] = MFMA(wf[i % 3][1], af[i], acc[1]);
    if constexpr ((i & 1) == 0) cs = MFMA16(wf[i % 3][0], sel, cs);
    if constexpr (i + 2 < 32) { SBAR(); load_w(IC<i + 2>{}); }
    SBAR();
  });
  STAMP_AT(3);
  if ((l & 15) < 2) {
    LAS float* d = tcs + nh * 64 + (m & 1) * 32 + 16 * (l & 15) + 4 * (l >> 4);
#pragma unroll
    for (int j = 0; j < 4; ++j) __hip_atomic_fetch_add(d + j, cs[j], __ATOMIC_RELAXED, __HIP_MEMORY_SCOPE_WORKGROUP);
  }
  if (w == 0 && l < 16) { const u32x4 u = __builtin_bit_cast(u32x4, bv);
    *(LAS f32x4*)(tb + l * 8) = f32x4{bf_lo(u[0]), bf_hi(u[0]), bf_lo(u[1]), bf_hi(u[1])}; *(LAS f32x4*)(tb + l * 8 + 4) = f32x4{bf_lo(u[2]), bf_hi(u[2]), bf_lo(u[3]), bf_hi(u[3])}; }
  asm volatile("" : "+v"(st[0]), "+v"(st[1]), "+v"(st[2]), "+v"(st[3]));
  const f32x2 ss = __builtin_bit_cast(f32x2, st[0]) + __builtin_bit_cast(f32x2, st[1]) + __builtin_bit_cast(f32x2, st[2]) + __builtin_bit_cast(f32x2, st[3]);
  const float sm = sumx32(ss[0]), sq = sumx32(ss[1]);
  const float mean = sm * (1.f / D), rstd = __builtin_amdgcn_rsqf(fmaxf(sq * (1.f / D) - mean * mean, 0.f) + LN_EPS);
  const float nmr = -mean * rstd;
  lds_bar();
  STAMP_AT(4);
  const __amdgpu_buffer_rsrc_t rw = make_rsrc(ws, WS_USED);
#pragma unroll
  for (int t = 0; t < 2; ++t) {
    const int ct = 2 * nh + ((m & 1) ^ t);
    f32x4 cv[4], bb[4];
#pragma unroll
    for (int g = 0; g < 4; ++g) { cv[g] = *(const LAS f32x4*)(tcs + 32 * ct + 8 * g + 4 * hh); bb[g] = *(const LAS f32x4*)(tb + 32 * ct + 8 * g + 4 * hh); }
#pragma unroll
    for (int s = 0; s < 2; ++s) {
      unsigned pk[4];
#pragma unroll
      for (int j = 0; j < 4; ++j) {
        const int r = 8 * s + 2 * j;
        const f32x2 a = {acc[t][r], acc[t][r + 1]}, c2 = {cv[r >> 2][r & 3], cv[r >> 2][(r & 3) + 1]}, b2 = {bb[r >> 2][r & 3], bb[r >> 2][(r & 3) + 1]};
        const f32x2 y = __builtin_elementwise_fma(a, f32x2{rstd, rstd}, __builtin_elementwise_fma(c2, f32x2{nmr, nmr}, b2));
        const f32x2 z = y * LOG2E;
        const f32x2 u = {__builtin_amdgcn_exp2f(z[0]), __builtin_amdgcn_exp2f(z[1])};
        const f32x2 d = __builtin_elementwise_fma(u, u + 2.f, f32x2{2.f, 2.f});
        const f32x2 r2 = {__builtin_amdgcn_rcpf(d[0]), __builtin_amdgcn_rcpf(d[1])};
        const f32x2 th = __builtin_elementwise_fma(r2, f32x2{-2.f, -2.f}, f32x2{1.f, 1.f});
        const f32x2 o = y * th;
        pk[j] = cvtpk(o[0], o[1]);
      }
      st16<MG>(rw, WS_ACT + (unsigned)((rt * 112 + cb * 8 + ct * 2 + s) * 64 + l) * 16, __builtin_bit_cast(bf16x8, u32x4{pk[0], pk[1], pk[2], pk[3]}));
      if constexpr (MG) {
        const int q = 2 * t + s;
        prefetch_w2<8>(p, c, 24 * q, 24 * q + 24);
      }
    }
  }
  STAMP_AT(5);
  if constexpr (MG) publish(ws, FL_D + (rb * 16 + cb) * 64, c.epoch);
  STAMP_AT(6);
  STAMP_OUT(3, 7);
}

constexpr int E_NSLOT = 10, E_NST = 14, E_WIN = 12, E_PRE = 8;
struct ESched {
  int posA[28], posW[E_NST], n_top[E_NST], n_mid[E_NST];
  constexpr ESched() : posA(), posW(), n_top(), n_mid() {
    int n = 0;
    for (int i = 0; i < E_WIN; ++i) posA[i] = n++;
    for (int st = 0; st < E_NST; ++st) {
      n_top[st] = n;
      if (E_PRE + st < E_NST) { n += 2; posW[E_PRE + st] = n - 1; }
      for (int j = 0; j < 2; ++j) if (E_WIN + 2 * st + j < 28) posA[E_WIN + 2 * st + j] = n++;
      n_mid[st] = n;
    }
  }
};
template <bool MG> DI void phaseE(const Params& p, const Ctx& c, u32x4 hkeep) {
  STAMP_DECL STAMP_AT(0);
  constexpr ESched sch{};
  char* ws = p.ws;
  LAS char* lds = c.lds;
  const int l = c.l, w = c.w, hh = c.hh, b = c.b;
  const int rb = (b & 7) * 4 + ((b >> 3) & 3), cb = b >> 5;
  const int m = w & 1, kq = w >> 1;
  const int rt = 2 * rb + m, kstep = 4 * cb + kq;
  const bool early = MG && b < 224;
  if (early) prefetch_w2<7>(p, c, 16 * 6, 16 * 8); else prefetch_w2<7>(p, c, 0, 16 * E_PRE);
  bf16x8 resv = __builtin_bit_cast(bf16x8, hkeep);
  if constexpr (!MG) resv = *(const GAS bf16x8*)(ws + WS_HF + (size_t)(rt * 32 + kstep) * 1024 + l * 16);
  const int q4 = (l & 15) >> 2, p4 = l & 3, blk = (l >> 4) & 1;
  const int wa0 = kq * 4096 + (4 * hh + q4) * 128 + (((2 * blk + (p4 >> 1)) ^ (((q4 >> 1) & 1) << 2)) * 16) + (p4 & 1) * 8;
  const int wa1 = wa0 ^ 64;
  bf16x8 wf[3][2][2];
  auto load_w = [&](auto ST) {
    constexpr int st = ST.value;
    const LAS char* sl = lds + ((st + 4) % E_NSLOT) * 16384;
#pragma unroll
    for (int i = 0; i < 2; ++i) {
      wf[st % 3][i][0] = cat8(trread(sl + wa0 + i * 2048), trread(sl + wa0 + i * 2048 + 1024));
      wf[st % 3][i][1] = cat8(trread(sl + wa1 + i * 2048), trread(sl + wa1 + i * 2048 + 1024));
    }
  };
  if (!early) wait_bar<0>();
  load_w(IC<0>{}); load_w(IC<1>{});
  if constexpr (MG) await(ws, FL_D + (rb >> 1) * 16 * 64, 14, c.epoch);
  STAMP_AT(1);
  const char* abase = ws + WS_ACT + ((size_t)rt * 112 + kq * 28) * 1024;
  bf16x8 af[28];
  auto load_a = [&](auto I) { constexpr int i = I.value; gload16<(i & 3) * 1024, MG>(af[i], abase + (i >> 2) * 4096, l * 16); };
  sfor<0, E_WIN>(load_a);
  f32x16 acc[2] = {};
  STAMP_AT(2);
  sfor<0, E_NST>([&](auto ST) {
    constexpr int st = ST.value;
    if constexpr (st == 2) asm volatile("s_waitcnt lgkmcnt(0)\n\ts_barrier" ::: "memory");
    if constexpr (st == 6 || st == 8 || st == 10) asm volatile("s_waitcnt vmcnt(%0) lgkmcnt(0)\n\ts_barrier" ::"i"(sch.n_top[st] - sch.posW[st + 3] - 1) : "memory");
    if constexpr (E_PRE + st < E_NST) prefetch_w2<8>(p, c, 16 * (st + E_PRE), 16 * (st + E_PRE + 1));
    if constexpr (E_WIN + 2 * st < 28) load_a(IC<E_WIN + 2 * st>{});
    if constexpr (E_WIN + 2 * st + 1 < 28) load_a(IC<E_WIN + 2 * st + 1>{});
    asm volatile("s_waitcnt vmcnt(%2)" : "+v"(af[2 * st]), "+v"(af[2 * st + 1]) : "i"(sch.n_mid[st] - sch.posA[2 * st + 1] - 1) : "memory");
    acc[0] = MFMA(wf[st % 3][0][0], af[2 * st], acc[0]);
    acc[1] = MFMA(wf[st % 3][0][1], af[2 * st], acc[1]);
    if constexpr (st + 2 < E_NST) { SBAR(); load_w(IC<st + 2>{}); SBAR(); }
    acc[0] = MFMA(wf[st % 3][1][0], af[2 * st + 1], acc[0]);
    acc[1] = MFMA(wf[st % 3][1][1], af[2 * st + 1], acc[1]);
    SBAR();
  });
  STAMP_AT(3);
  float val[8];
  quarter_reduce<0>(c, (LAS f32x4*)lds, acc, val);
  asm volatile("" : "+v"(resv));
  const u32x4 ru = __builtin_bit_cast(u32x4, resv);
  const u32x2 o0 = {cvtpk(val[0] + bf_lo(ru[0]), val[1] + bf_hi(ru[0])), cvtpk(val[2] + bf_lo(ru[1]), val[3] + bf_hi(ru[1]))};
  const u32x2 o1 = {cvtpk(val[4] + bf_lo(ru[2]), val[5] + bf_hi(ru[2])), cvtpk(val[6] + bf_lo(ru[3]), val[7] + bf_hi(ru[3]))};
  const __amdgpu_buffer_rsrc_t ro = make_rsrc(p.out, S * D * 2);
  const unsigned oo = (unsigned)(rt * 32 + (l & 31)) * 1024u + kstep * 32 + hh * 8;
  st8<false>(ro, oo, o0);
  st8<false>(ro, oo + 16, o1);
  STAMP_AT(4);
  STAMP_OUT(4, 5);
}

DI Ctx make_ctx(char* lds_g) {
  Ctx c;
  c.lds = (LAS char*)lds_g; c.lds0 = (unsigned)(uintptr_t)lds_g;
  c.tid = threadIdx.x; c.l = c.tid & 63; c.w = rfl(c.tid >> 6); c.hh = c.l >> 5; c.b = blockIdx.x; c.epoch = 0;
  return c;
}

__global__ __launch_bounds__(512, 2) void block_mega(Params p) {
  extern __shared__ __attribute__((aligned(16))) char lds_g[];
  Ctx c = make_ctx(lds_g);
  c.epoch = __hip_atomic_load((GAS unsigned*)(p.ws + WS_FLAG) + (threadIdx.x >> 12), __ATOMIC_RELAXED, __HIP_MEMORY_SCOPE_AGENT);
  phaseA<true>(p, c);
  phaseB<true>(p, c);
  u32x4 hkeep;
  phaseC<true>(p, c, hkeep);
  if (c.b < 224) phaseD<true>(p, c);
  phaseE<true>(p, c, hkeep);
  if (c.b == 0 && c.tid == 0) __hip_atomic_store((GAS unsigned*)(p.ws + WS_FLAG), c.epoch, __ATOMIC_RELAXED, __HIP_MEMORY_SCOPE_AGENT);
  asm volatile("mega_code_end:");
}

extern "C" void kernel_launch(void* const* d_in, const int* in_sizes, int n_in, void* d_out, int out_size, void* d_ws, size_t ws_size, hipStream_t stream) {
  Params p;
  p.x = (const u16*)d_in[0]; p.wqkv = (const u16*)d_in[1]; p.wo = (const u16*)d_in[2];
  p.w1 = (const u16*)d_in[4]; p.b1 = (const u16*)d_in[5]; p.w2 = (const u16*)d_in[6];
  p.ws = (char*)d_ws; p.out = (u16*)d_out;
  (void)hipFuncSetAttribute((const void*)block_mega, hipFuncAttributeMaxDynamicSharedMemorySize, LDS_BYTES);
  block_mega<<<256, 512, LDS_BYTES, stream>>>(p);
}
```

```cpp
#include <hip/hip_runtime.h>
#include <hip/hip_bf16.h>
#include <type_traits>

typedef unsigned short u16;
using bf16x8 = __attribute__((ext_vector_type(8))) short;
using s16x4 = __attribute__((ext_vector_type(4))) short;
using f32x16 = __attribute__((ext_vector_type(16))) float;
using f32x4 = __attribute__((ext_vector_type(4))) float;
using f32x2 = __attribute__((ext_vector_type(2))) float;
using u32x4 = __attribute__((ext_vector_type(4))) unsigned;
using u32x2 = __attribute__((ext_vector_type(2))) unsigned;
using bf16x2_t = __attribute__((ext_vector_type(2))) __bf16;
#define DI __device__ __forceinline__
#define LAS __attribute__((address_space(3)))
#define GAS __attribute__((address_space(1)))
#define MFMA(a, b, c) __builtin_amdgcn_mfma_f32_32x32x16_bf16((a), (b), (c), 0, 0, 0)
#define SBAR() __builtin_amdgcn_sched_barrier(0)

constexpr int S = 2048, D = 512, F = 1792;
constexpr float LN_EPS = 1e-5f;
constexpr float LOG2E = 1.4426950408889634f;
constexpr float QSCALE = 0.25f * LOG2E;
constexpr float BOUND_SAFE = 40.f;
constexpr int LDS_BYTES = 163840;

constexpr unsigned WS_FLAG = 0;
constexpr unsigned FL_A = 4096, FL_B = FL_A + 256 * 64, FL_C = FL_B + 256 * 64, FL_D = FL_C + 256 * 64;
constexpr unsigned WS_KMAX = 128u << 10, WS_STAT = 256u << 10;
constexpr unsigned WS_Q = 1u << 20, WS_KV = 3u << 20  , WS_O = 7u << 20, WS_HF = 9u << 20, WS_ACT = 11u << 20;
constexpr unsigned WS_USED = 19u << 20;

struct Params { const u16 *x, *wqkv, *wo, *w1, *b1, *w2; char* ws; u16* out; };

template <int N> using IC = std::integral_constant<int, N>;
template <int B, int E, class Fn> DI void sfor(Fn&& f) { if constexpr (B < E) { f(IC<B>{}); sfor<B + 1, E>(f); } }

DI int rfl(int v) { return __builtin_amdgcn_readfirstlane(v); }
DI unsigned cvtpk(float lo, float hi) { const f32x2 v = {lo, hi}; return __builtin_bit_cast(unsigned, __builtin_convertvector(v, bf16x2_t)); }
DI float bf_lo(unsigned u) { return __uint_as_float(u << 16); }
DI float bf_hi(unsigned u) { return __uint_as_float(u & 0xffff0000u); }
constexpr unsigned ONES2 = 0x3F803F80u;
#define MFMA16(a, b, c) __builtin_amdgcn_mfma_f32_16x16x32_bf16((a), (b), (c), 0, 0, 0)
DI f32x2 unpk(unsigned u) { return f32x2{__uint_as_float(u << 16), __uint_as_float(u & 0xffff0000u)}; }
DI bf16x8 rowsum_sel(int l) { const unsigned on = ((l & 15) == ((l >> 4) & 1)) ? ONES2 : 0u; return __builtin_bit_cast(bf16x8, u32x4{on, on, on, on}); }
DI void dma16(const void* sbase, unsigned voff, unsigned lds) {
  asm volatile("s_mov_b32 m0, %2\n\ts_nop 0\n\tglobal_load_lds_dwordx4 %0, %1" ::"v"(voff), "s"(sbase), "s"(lds) : "memory");
}
DI void dma16_sc1(const void* sbase, unsigned voff, unsigned lds) {
  asm volatile("s_mov_b32 m0, %2\n\ts_nop 0\n\tglobal_load_lds_dwordx4 %0, %1 sc1" ::"v"(voff), "s"(sbase), "s"(lds) : "memory");
}
template <int IMM, bool SC1> DI void gload16(bf16x8& dst, const void* sbase, unsigned voff) {
  if constexpr (SC1) asm volatile("global_load_dwordx4 %0, %1, %2 offset:%3 sc1" : "=v"(dst) : "v"(voff), "s"(sbase), "i"(IMM) : "memory");
  else asm volatile("global_load_dwordx4 %0, %1, %2 offset:%3" : "=v"(dst) : "v"(voff), "s"(sbase), "i"(IMM) : "memory");
}
template <int IMM, bool SC1 = false> DI void gload8(u32x2& dst, const void* sbase, unsigned voff) {
  if constexpr (SC1) asm volatile("global_load_dwordx2 %0, %1, %2 offset:%3 sc1" : "=v"(dst) : "v"(voff), "s"(sbase), "i"(IMM) : "memory");
  else asm volatile("global_load_dwordx2 %0, %1, %2 offset:%3" : "=v"(dst) : "v"(voff), "s"(sbase), "i"(IMM) : "memory");
}
template <bool SC1> DI void gload4(float& dst, const void* sbase, unsigned voff) {
  if constexpr (SC1) asm volatile("global_load_dword %0, %1, %2 sc1" : "=v"(dst) : "v"(voff), "s"(sbase) : "memory");
  else asm volatile("global_load_dword %0, %1, %2" : "=v"(dst) : "v"(voff), "s"(sbase) : "memory");
}
typedef __attribute__((ext_vector_type(4))) unsigned v4u_;
DI __amdgpu_buffer_rsrc_t make_rsrc(void* p, unsigned bytes) { return __builtin_amdgcn_make_buffer_rsrc(p, 0, bytes, 0x00020000); }
template <bool SC1> DI void st16(__amdgpu_buffer_rsrc_t r, unsigned off, bf16x8 v) { __builtin_amdgcn_raw_buffer_store_b128(__builtin_bit_cast(v4u_, v), r, off, 0, SC1 ? 16 : 0); }
template <bool SC1> DI void st8(__amdgpu_buffer_rsrc_t r, unsigned off, u32x2 v) { __builtin_amdgcn_raw_buffer_store_b64(v, r, off, 0, SC1 ? 16 : 0); }
template <bool SC1> DI void st4(__amdgpu_buffer_rsrc_t r, unsigned off, unsigned v) { __builtin_amdgcn_raw_buffer_store_b32(v, r, off, 0, SC1 ? 16 : 0); }

template <int N> DI void wait_bar() { asm volatile("s_waitcnt vmcnt(%0) lgkmcnt(0)\n\ts_barrier" ::"i"(N) : "memory"); }
DI void lds_bar() { asm volatile("s_waitcnt lgkmcnt(0)\n\ts_barrier" ::: "memory"); }
DI s16x4 trread(const LAS char* p) {
  typedef short v4i16_t __attribute__((ext_vector_type(4)));
  return __builtin_bit_cast(s16x4, __builtin_amdgcn_ds_read_tr16_b64_v4i16((LAS v4i16_t*)p));
}
DI bf16x8 cat8(s16x4 a, s16x4 b) { return __builtin_shufflevector(a, b, 0, 1, 2, 3, 4, 5, 6, 7); }
DI bf16x8 pack8(float a0, float a1, float a2, float a3, float a4, float a5, float a6, float a7) {
  u32x4 w = {cvtpk(a0, a1), cvtpk(a2, a3), cvtpk(a4, a5), cvtpk(a6, a7)};
  return __builtin_bit_cast(bf16x8, w);
}
template <int CTRL> DI float dpp(float v) { return __uint_as_float(__builtin_amdgcn_update_dpp(0u, __float_as_uint(v), CTRL, 0xf, 0xf, true)); }
DI float sum8(float v) { v += dpp<0xB1>(v); v += dpp<0x4E>(v); v += dpp<0x141>(v); return v; }
DI float sumx32(float v) { auto r = __builtin_amdgcn_permlane32_swap(__float_as_uint(v), __float_as_uint(v), false, false); return __uint_as_float(r[0]) + __uint_as_float(r[1]); }
DI float maxx32(float v) { auto r = __builtin_amdgcn_permlane32_swap(__float_as_uint(v), __float_as_uint(v), false, false); return fmaxf(__uint_as_float(r[0]), __uint_as_float(r[1])); }
DI float maxx16(float v) { auto r = __builtin_amdgcn_permlane16_swap(__float_as_uint(v), __float_as_uint(v), false, false); return fmaxf(__uint_as_float(r[0]), __uint_as_float(r[1])); }
DI float max16(float v) { v = fmaxf(v, dpp<0xB1>(v)); v = fmaxf(v, dpp<0x4E>(v)); v = fmaxf(v, dpp<0x141>(v)); v = fmaxf(v, dpp<0x140>(v)); return v; }

DI void publish(char* ws, unsigned flag_off, unsigned epoch) {
  asm volatile("s_waitcnt vmcnt(0) lgkmcnt(0)\n\ts_barrier" ::: "memory");
  if (threadIdx.x == 0) __hip_atomic_store((GAS unsigned*)(ws + flag_off), epoch, __ATOMIC_RELAXED, __HIP_MEMORY_SCOPE_AGENT);
}
DI void await(char* ws, unsigned flag_off, int n, unsigned epoch) {
  if (threadIdx.x >= 448) {
    const int l = threadIdx.x & 63;
    GAS unsigned* f = (GAS unsigned*)(ws + flag_off + (l < n ? l : 0) * 64);
    for (unsigned spins = 0; spins < (1u << 20); ++spins) {
      const unsigned v = __hip_atomic_load(f, __ATOMIC_RELAXED, __HIP_MEMORY_SCOPE_AGENT);
      if (__all(v == epoch)) break;
      __builtin_amdgcn_s_sleep(1);
    }
  }
  asm volatile("s_waitcnt lgkmcnt(0)\n\ts_barrier" ::: "memory");
}

struct Ctx { LAS char* lds; unsigned lds0; int tid, l, w, hh, b; unsigned epoch; };

template <bool MG> DI void phaseA(const Params& p, Ctx& c) {
  char* ws = p.ws;
  LAS char* lds = c.lds;
  const int l = c.l, w = c.w, hh = c.hh, b = c.b;
  const int rb = 8 * ((b & 7) >> 1) + ((b >> 3) & 7), hg = 4 * (b & 1) + (b >> 6);
  bf16x8 ft[3] = {};
  unsigned ftw = 0;
  if constexpr (MG) {
    unsigned long long pc; unsigned len;
    asm volatile("s_getpc_b64 %0\n.Lmega_here%=:\n\ts_mov_b32 %1, mega_code_end-.Lmega_here%=" : "=s"(pc), "=s"(len));
    const unsigned j = (unsigned)(b >> 3);
    const unsigned oc = j * 1024u + (w & 1) * 32768u + l * 16u, pg = (j * 10u + (l < 10 ? l : 0)) * 65536u;
    ft[2] = *(const GAS bf16x8*)((const char*)pc + (w < 2 && oc + 16 <= len ? oc : 0u));
    ftw = __hip_atomic_load((const GAS unsigned*)(w == 3 ? (const char*)p.out + j * 65536u : (const char*)ws + (w == 2 && pg < WS_USED ? pg : 0u)), __ATOMIC_RELAXED, __HIP_MEMORY_SCOPE_AGENT);
    ft[0] = *(const GAS bf16x8*)((const char*)p.x + (size_t)b * 8192 + w * 1024 + l * 16);
    ft[1] = *(const GAS bf16x8*)((const char*)p.wqkv + (size_t)b * 6144 + (w < 6 ? w : 5) * 1024 + l * 16);
  }
  const int q4 = (l & 15) >> 2, p4 = l & 3, blk = (l >> 4) & 1;
  LAS float* tmean = (LAS float*)(lds + 0);
  LAS float* trstd = tmean + 64;
  LAS float* tcs = tmean + 128;
  f32x16 acc[3] = {};
  const int m = w & 1, g = (w >> 1) & 1;
  if (w < 4) {
    const bf16x8 sel = rowsum_sel(l);
    const int swz = (l >> 1) & 7;
    const int xrow = (32 * m + (l & 31)) * 128;
    const int wa = 8192 + (8 * hh + q4) * 128 + (((4 * g + 2 * blk + (p4 >> 1)) ^ (((q4 >> 1) & 1) << 2)) * 16) + (p4 & 1) * 8;
    int xo[4], wo_[2];
#pragma unroll
    for (int i = 0; i < 4; ++i) xo[i] = xrow + (((2 * (i ^ m) + hh) ^ swz) * 16);
    wo_[0] = wa + m * 2048; wo_[1] = wa + (1 - m) * 2048;
    bf16x8 xf[2][4], wf[2][4][3];
    f32x4 cs[3] = {};
    auto ld_w = [&](auto SET, const LAS char* sl, int i, int sgm) { constexpr int q = SET.value; const int o = wo_[i & 1] + (i & 2) * 2048 + sgm * 8192;
      wf[q][i][sgm] = cat8(trread(sl + o), trread(sl + o + 512)); };
    lds_bar();
#pragma unroll
    for (int i = 0; i < 4; ++i) { xf[0][i] = *(const LAS bf16x8*)(lds + xo[i]); for (int sgm = 0; sgm < 3; ++sgm) ld_w(IC<0>{}, lds, i, sgm); }
    sfor<0, 8>([&](auto ST) {
      constexpr int st = ST.value, q = st & 1, qn = (st + 1) & 1;
      if constexpr (st + 1 < 8) lds_bar(); else asm volatile("s_waitcnt lgkmcnt(0)" ::: "memory");
      asm volatile("" : "+v"(xf[q][0]), "+v"(xf[q][1]), "+v"(xf[q][2]), "+v"(xf[q][3]), "+v"(wf[q][0][0]), "+v"(wf[q][0][1]), "+v"(wf[q][0][2]), "+v"(wf[q][1][0]), "+v"(wf[q][1][1]), "+v"(wf[q][1][2]),
                   "+v"(wf[q][2][0]), "+v"(wf[q][2][1]), "+v"(wf[q][2][2]), "+v"(wf[q][3][0]), "+v"(wf[q][3][1]), "+v"(wf[q][3][2]));
      const LAS char* sn = lds + ((st + 1) % 5) * 32768;
#pragma unroll
      for (int i = 0; i < 4; ++i) {
        acc[0] = MFMA(wf[q][i][0], xf[q][i], acc[0]);
        SBAR();
        if constexpr (st + 1 < 8) { ld_w(IC<qn>{}, sn, i, 0); xf[qn][i] = *(const LAS bf16x8*)(sn + xo[i]); }
        SBAR();
        acc[1] = MFMA(wf[q][i][1], xf[q][i], acc[1]);
        SBAR();
        if constexpr (st + 1 < 8) ld_w(IC<qn>{}, sn, i, 1);
        SBAR();
        acc[2] = MFMA(xf[q][i], wf[q][i][2], acc[2]);
        SBAR();
        if constexpr (st + 1 < 8) ld_w(IC<qn>{}, sn, i, 2);
        if ((i & 1) == 0) { cs[0] = MFMA16(wf[q][i][0], sel, cs[0]); cs[1] = MFMA16(wf[q][i][1], sel, cs[1]); cs[2] = MFMA16(wf[q][i][2], sel, cs[2]); }
        SBAR();
      }
    });
    if ((l & 15) < 2) {
#pragma unroll
      for (int t = 0; t < 3; ++t) *(LAS f32x4*)(tcs + (w * 3 + t) * 32 + 16 * (l & 15) + 4 * (l >> 4)) = cs[t];
    }
  } else {
    const int h = w - 4;
    const unsigned xo0 = (unsigned)(64 * rb + 16 * h + (l >> 3)) * 1024u + (unsigned)(((l & 7) ^ ((l >> 4) & 7)) * 16);
    const unsigned xo1 = (unsigned)(64 * rb + 16 * h + 8 + (l >> 3)) * 1024u + (unsigned)(((l & 7) ^ ((4 + (l >> 4)) & 7)) * 16);
    const unsigned wof = (unsigned)(16 * h + (l >> 3)) * 3072u + (unsigned)hg * 128u + (unsigned)(((l & 7) ^ (((l >> 4) & 1) << 2)) * 16);
    auto issue = [&](int st) {
      const unsigned dst = c.lds0 + (st % 5) * 32768 + h * 2048;
      const char* wsrc = (const char*)p.wqkv + (size_t)st * 64 * 3072;
      dma16((const char*)p.x + st * 128, xo0, dst);
      dma16((const char*)p.x + st * 128, xo1, dst + 1024);
#pragma unroll
      for (int sgm = 0; sgm < 3; ++sgm) { dma16(wsrc + sgm * 1024, wof, dst + 8192 + sgm * 8192); dma16(wsrc + sgm * 1024 + 8 * 3072, wof, dst + 8192 + sgm * 8192 + 1024); }
    };
    issue(0); issue(1); issue(2); issue(3); issue(4);
    f32x2 rs[2] = {}, rq[2] = {};
    u32x4 sv[2][2];
    wait_bar<32>();
    sv[0][0] = *(const LAS u32x4*)(lds + h * 2048 + l * 16); sv[0][1] = *(const LAS u32x4*)(lds + h * 2048 + 1024 + l * 16);
    sfor<0, 8>([&](auto ST) {
      constexpr int st = ST.value, q = st & 1, qn = (st + 1) & 1;
      if constexpr (st + 1 < 8) {
        constexpr int last = 4 + st > 7 ? 7 : 4 + st;
        wait_bar<8 * (last - (st + 1))>();
        if constexpr (st + 5 < 8) issue(st + 5);
        const LAS char* sn = lds + ((st + 1) % 5) * 32768 + h * 2048 + l * 16;
        sv[qn][0] = *(const LAS u32x4*)sn; sv[qn][1] = *(const LAS u32x4*)(sn + 1024);
      }
#pragma unroll
      for (int r = 0; r < 2; ++r)
#pragma unroll
        for (int i = 0; i < 4; ++i) { const f32x2 v = unpk(sv[q][r][i]); rs[r] += v; rq[r] = __builtin_elementwise_fma(v, v, rq[r]); }
      asm volatile("" : "+v"(rs[0]), "+v"(rq[0]), "+v"(rs[1]), "+v"(rq[1]));
    });
#pragma unroll
    for (int r = 0; r < 2; ++r) {
      const float s1 = sum8(rs[r][0] + rs[r][1]), s2 = sum8(rq[r][0] + rq[r][1]);
      if ((l & 7) == 0) { const float mean = s1 * (1.f / D); const int row = 16 * h + 8 * r + (l >> 3);
        tmean[row] = mean; trstd[row] = __builtin_amdgcn_rsqf(fmaxf(s2 * (1.f / D) - mean * mean, 0.f) + LN_EPS); }
    }
  }
  if constexpr (MG) { asm volatile("" : "+v"(c.epoch), "+v"(ft[0]), "+v"(ft[1]), "+v"(ft[2]), "+v"(ftw)); c.epoch += 1u; }
  lds_bar();
  const __amdgpu_buffer_rsrc_t rw = make_rsrc(ws, WS_USED);
  if (w < 4) {
    const int tile = 2 * rb + m;
    const float mean_l = tmean[32 * m + (l & 31)], rstd_l = trstd[32 * m + (l & 31)];
    f32x4 cv[2][4], mm[4], rr[4];
#pragma unroll
    for (int gg = 0; gg < 4; ++gg) {
#pragma unroll
      for (int t = 0; t < 2; ++t) cv[t][gg] = *(const LAS f32x4*)(tcs + (w * 3 + t) * 32 + 8 * gg + 4 * hh) + *(const LAS f32x4*)(tcs + ((w ^ 1) * 3 + t) * 32 + 8 * gg + 4 * hh);
      mm[gg] = *(const LAS f32x4*)(tmean + 32 * m + 8 * gg + 4 * hh); rr[gg] = *(const LAS f32x4*)(trstd + 32 * m + 8 * gg + 4 * hh);
    }
    const int n = l & 31;
    const float ncs = -(tcs[(w * 3 + 2) * 32 + n] + tcs[((w ^ 1) * 3 + 2) * 32 + n]);
#pragma unroll
    for (int s = 0; s < 2; ++s) {
      f32x2 v[4];
#pragma unroll
      for (int j = 0; j < 4; ++j) { const int r = 8 * s + 2 * j; const f32x2 a = {acc[2][r], acc[2][r + 1]}, m2 = {mm[r >> 2][r & 3], mm[r >> 2][(r & 3) + 1]}, r2 = {rr[r >> 2][r & 3], rr[r >> 2][(r & 3) + 1]};
        v[j] = __builtin_elementwise_fma(m2, f32x2{ncs, ncs}, a) * r2; }
      st16<MG>(rw, WS_KV + (unsigned)((4 * hg + 2 * g + (n >> 4)) * 64 + tile) * 2048 + 1024 + s * 512 + hh * 256 + (n & 15) * 16,
               pack8(v[0][0], v[0][1], v[1][0], v[1][1], v[2][0], v[2][1], v[3][0], v[3][1]));
    }
#pragma unroll
    for (int t = 1; t >= 0; --t) {
      const float sc = t == 0 ? rstd_l * QSCALE : rstd_l, nm = -mean_l * sc;
#pragma unroll
      for (int s = 0; s < 2; ++s) {
        f32x2 v[4];
#pragma unroll
        for (int j = 0; j < 4; ++j) { const int r = 8 * s + 2 * j; const f32x2 a = {acc[t][r], acc[t][r + 1]}, cc = {cv[t][r >> 2][r & 3], cv[t][r >> 2][(r & 3) + 1]};
          v[j] = __builtin_elementwise_fma(a, f32x2{sc, sc}, cc * nm); }
        const int head = 4 * hg + 2 * g + s;
        st16<MG>(rw, (t == 0 ? WS_Q + (unsigned)(head * 64 + tile) * 1024 : WS_KV + (unsigned)(head * 64 + tile) * 2048) + l * 16, pack8(v[0][0], v[0][1], v[1][0], v[1][1], v[2][0], v[2][1], v[3][0], v[3][1]));
        if (t == 1) {
          f32x2 n2 = v[0] * v[0]; n2 = __builtin_elementwise_fma(v[1], v[1], n2); n2 = __builtin_elementwise_fma(v[2], v[2], n2); n2 = __builtin_elementwise_fma(v[3], v[3], n2);
          const float mx = maxx16(max16(sumx32(n2[0] + n2[1])));
          if (l == 0) st4<MG>(rw, WS_KMAX + (unsigned)(head * 64 + tile) * 4, __float_as_uint(mx));
        }
      }
    }
  }
  if constexpr (MG) publish(ws, FL_A + (hg * 32 + rb) * 64, c.epoch);
}

DI unsigned wpiece_lane(int l, unsigned row_bytes) { return (unsigned)(l >> 3) * row_bytes + (unsigned)(((l & 7) ^ (((l >> 4) & 1) << 2)) * 16); }
template <int NW, int W0 = 0> DI void prefetch_wo(const Params& p, const Ctx& c) {
  if (c.w < W0 || c.w >= W0 + NW) return;
  const unsigned off = wpiece_lane(c.l, 1024) + (unsigned)(c.b >> 5) * 128u;
  for (int pc = c.w - W0; pc < 64; pc += NW) dma16((const char*)p.wo + (size_t)(8 * pc) * 1024, off, c.lds0 + pc * 1024);
}
DI constexpr unsigned w1_stage_base(int j) { return j < 6 ? 65536 + j * 16384 : (j - 6) * 16384; }
template <int NW, int W0 = 0> DI void prefetch_w1(const Params& p, const Ctx& c, int p0, int p1) {
  if (c.w < W0 || c.w >= W0 + NW) return;
  const unsigned off = wpiece_lane(c.l, F * 2) + (unsigned)(c.b >> 4) * 256u;
  for (int pc = p0 + c.w - W0; pc < p1; pc += NW) {
    const int j = pc >> 4, seg = (pc >> 3) & 1, r8 = pc & 7;
    dma16((const char*)p.w1 + (size_t)(64 * j + 8 * r8) * (F * 2) + seg * 128, off, c.lds0 + w1_stage_base(j) + seg * 8192 + r8 * 1024);
  }
}
template <int NW> DI void prefetch_w2(const Params& p, const Ctx& c, int p0, int p1) {
  if (c.w >= NW) return;
  const unsigned off = wpiece_lane(c.l, 1024) + (unsigned)(c.b >> 5) * 128u;
  for (int pc = p0 + c.w; pc < p1; pc += NW) {
    const int st = pc >> 4, kq = (pc >> 2) & 3, r8 = pc & 3;
    dma16((const char*)p.w2 + (size_t)(448 * kq + 32 * st + 8 * r8) * 1024, off, c.lds0 + ((st + 4) % 10) * 16384 + kq * 4096 + r8 * 1024);
  }
}

DI void warm_first(const Params& p, const Ctx& c) {
  const int h = c.w - 4;
  const unsigned off1 = (unsigned)c.b * 7168u + (unsigned)h * 2048u + (unsigned)c.l * 16u;
  dma16((const char*)p.w1, off1, c.lds0 + 139520);
  dma16((const char*)p.w2, off1, c.lds0 + 139520);
  if (h < 3) { dma16((const char*)p.w1 + 1024, off1, c.lds0 + 139520); dma16((const char*)p.w2 + 1024, off1, c.lds0 + 139520); }
}
DI void warm_w1_late(const Params& p, const Ctx& c) {
  const int h = c.w - 4;
  const unsigned j = (unsigned)(c.b >> 3);
  for (int pc = h; pc < 35; pc += 4)
    dma16((const char*)p.w1, (pc < 21 ? 128u * 3584u + j * 21504u + pc * 1024u : 384u * 3584u + j * 14336u + (pc - 21) * 1024u) + c.l * 16u, c.lds0 + 139520);
}
DI void warm_w2(const Params& p, const Ctx& c) {
  const int h = c.w - 4;
  const unsigned off = (unsigned)(c.b >> 3) * 57344u + (unsigned)h * 14336u + (unsigned)c.l * 16u;
#pragma unroll
  for (int i = 0; i < 14; ++i) dma16((const char*)p.w2 + i * 1024, off, c.lds0 + 139520);
}

constexpr int B_ONES = 131072;
template <bool MG> DI void phaseB(const Params& p, const Ctx& c) {
  char* ws = p.ws;
  LAS char* lds = c.lds;
  const int l = c.l, w = c.w, hh = c.hh, b = c.b, tid = c.tid;
  const int head = 4 * (b & 7) + ((b >> 3) & 3), qb = b >> 5;
  for (int i = tid; i < (4096 + 512) / 16; i += 512) { const unsigned v = (i & 31) == 0 ? ONES2 : 0u; ((LAS u32x4*)(lds + B_ONES))[i] = u32x4{v, v, v, v}; }
  if constexpr (MG) await(ws, FL_A + (head >> 2) * 32 * 64, 32, c.epoch);
  const unsigned l16 = l * 16;
  if (w >= 4) {
    const char* kvg = ws + WS_KV + (size_t)head * 131072;
    const int h = w - 4;
    auto piece = [&](int pc) { if constexpr (MG) dma16_sc1(kvg + pc * 1024, l16, c.lds0 + pc * 1024); else dma16(kvg + pc * 1024, l16, c.lds0 + pc * 1024); };
#pragma unroll
    for (int i = 0; i < 4; ++i) piece(4 * h + i);
#pragma unroll
    for (int i = 0; i < 12; ++i) piece(16 + 4 * i + h);
    wait_bar<12>();
#pragma unroll
    for (int i = 0; i < 16; ++i) piece(64 + 4 * i + h);
    wait_bar<24>();
    wait_bar<16>();
    if constexpr (MG) warm_first(p, c);
    wait_bar<0>();
    if constexpr (MG) { if (b < 224) prefetch_w1<4, 4>(p, c, 80, 96); }
    lds_bar();
    if constexpr (MG) { prefetch_wo<4, 4>(p, c); warm_w1_late(p, c); }
    lds_bar();
    if constexpr (MG) { if (b < 224) prefetch_w1<4, 4>(p, c, 0, 32); warm_w2(p, c); }
  } else {
    bf16x8 qa, qb_;
    float kmx;
    gload4<MG>(kmx, ws + WS_KMAX + head * 256, l * 4);
    gload16<0, MG>(qa, ws + WS_Q + (size_t)(head * 64 + qb * 8 + w) * 1024, l * 16);
    gload16<0, MG>(qb_, ws + WS_Q + (size_t)(head * 64 + qb * 8 + w + 4) * 1024, l * 16);
    asm volatile("s_waitcnt vmcnt(0)" : "+v"(qa), "+v"(qb_), "+v"(kmx)::"memory");
    kmx = maxx32(maxx16(max16(kmx)));
    auto qbound = [&](bf16x8 q) { const u32x4 u = __builtin_bit_cast(u32x4, q); f32x2 t = {0.f, 0.f}; for (int i = 0; i < 4; ++i) { const f32x2 v = unpk(u[i]); t = __builtin_elementwise_fma(v, v, t); }
      return __builtin_amdgcn_sqrtf(sumx32(t[0] + t[1]) * kmx) * 1.01f + 1e-6f; };
    const float bna = qbound(qa), bnb = qbound(qb_);
    const bool safe = __all(bna <= BOUND_SAFE && bnb <= BOUND_SAFE);
    f32x16 oa = {}, ob = {};
    const int lo5 = l & 31;
    const bool isv = lo5 < 16;
    const int va0 = isv ? 1024 + hh * 256 + lo5 * 16 : B_ONES + (lo5 == 16 ? 0 : (lo5 >= 20 && lo5 < 28) ? 64 : 16);
    lds_bar();
    if (safe) {
      f32x16 nga, ngb; for (int i = 0; i < 16; ++i) { nga[i] = -bna; ngb[i] = -bnb; }
      f32x16 s0, s1;
      bf16x8 pa, pb = {};
      bf16x8 kf, v0, v1 = {};
      unsigned ka = c.lds0 + l * 16, va = c.lds0 + va0;
      const unsigned vstep = isv ? 4096u : 0u;
      asm volatile("ds_read_b128 %1, %3\n\tds_read_b128 %2, %4\n\ts_waitcnt lgkmcnt(0)\n\t"
                   "v_mfma_f32_32x32x16_bf16 %0, %1, %5, %6\n\ts_nop 7\n\ts_nop 3"
                   : "=&{v[192:207]}"(s0), "=&v"(kf), "=&v"(v0) : "v"(ka), "v"(va), "v"(qa), "v"(nga) : "memory");
#define E_(b, i) "v_exp_f32 v[" #b "+" #i "], v[" #b "+" #i "]\n\t"
#define C_(d, i, b, j, k) "v_cvt_pk_bf16_f32 v[" #d "+" #i "], v[" #b "+" #j "], v[" #b "+" #k "]\n\t"
#define MID_(SC) E_(SC, 4) E_(SC, 5) E_(SC, 6) E_(SC, 7) C_(224, 0, SC, 0, 1) C_(224, 1, SC, 2, 3) E_(SC, 8) E_(SC, 9) C_(224, 2, SC, 4, 5) C_(224, 3, SC, 6, 7) E_(SC, 10) E_(SC, 11)
#define END_(SC) E_(SC, 12) E_(SC, 13) E_(SC, 14) E_(SC, 15) C_(228, 0, SC, 8, 9) C_(228, 1, SC, 10, 11) C_(228, 2, SC, 12, 13) C_(228, 3, SC, 14, 15)
#define UNIT_A(KOFF, V1OFF) \
      "v_mfma_f32_32x32x16_bf16 v[208:223], %[kf], %[qb], %[ngb]\n\t" \
      "ds_read_b128 %[kf], %[ka] offset:" #KOFF "\n\t" \
      E_(192, 0) E_(192, 1) E_(192, 2) E_(192, 3) \
      "v_mfma_f32_32x32x16_bf16 %[ob], %[v1], v[228:231], %[ob]\n\t" \
      "ds_read_b128 %[v1], %[va] offset:" #V1OFF "\n\t" \
      MID_(192) \
      "s_waitcnt lgkmcnt(2)\n\t" \
      "v_mfma_f32_32x32x16_bf16 %[oa], %[v0], v[224:227], %[oa]\n\t" \
      END_(192)
#define UNIT_B(V0OFF) \
      "s_waitcnt lgkmcnt(0)\n\t" \
      "v_mfma_f32_32x32x16_bf16 v[192:207], %[kf], %[qa], %[nga]\n\t" \
      E_(208, 0) E_(208, 1) E_(208, 2) E_(208, 3) \
      "v_mfma_f32_32x32x16_bf16 %[oa], %[v1], v[228:231], %[oa]\n\t" \
      MID_(208) \
      "v_mfma_f32_32x32x16_bf16 %[ob], %[v0], v[224:227], %[ob]\n\t" \
      "ds_read_b128 %[v0], %[va] offset:" #V0OFF "\n\t" \
      END_(208)
#define RUN_GROUPS(NG) do { int cnt_ = (NG); \
      asm volatile("1:\n\t" UNIT_A(2048, 512) UNIT_B(2048) UNIT_A(4096, 2560) UNIT_B(4096) \
        "v_add_u32 %[ka], 0x1000, %[ka]\n\tv_add_u32 %[va], %[va], %[vs]\n\t" \
        "s_sub_u32 %[cnt], %[cnt], 1\n\ts_cmp_lg_u32 %[cnt], 0\n\ts_cbranch_scc1 1b\n\ts_waitcnt lgkmcnt(0)\n\ts_nop 7\n\ts_nop 3" \
        : "+{v[192:207]}"(s0), "=&{v[208:223]}"(s1), "=&{v[224:227]}"(pa), "+{v[228:231]}"(pb), [oa] "+v"(oa), [ob] "+v"(ob), [kf] "+v"(kf), [v0] "+v"(v0), [v1] "+v"(v1), \
          [ka] "+v"(ka), [va] "+v"(va), [cnt] "+s"(cnt_) \
        : [qa] "v"(qa), [qb] "v"(qb_), [nga] "v"(nga), [ngb] "v"(ngb), [vs] "v"(vstep) : "memory", "scc"); } while (0)
      RUN_GROUPS(3);
      lds_bar();
      RUN_GROUPS(4);
      lds_bar();
      RUN_GROUPS(8);
      lds_bar();
      RUN_GROUPS(1);
      lds_bar();
      RUN_GROUPS(8);
      lds_bar();
      RUN_GROUPS(8);
      ob = MFMA(v1, pb, ob);
    } else {
      float ca = -3.0e38f, cb = -3.0e38f;
      const f32x16 zero = {};
      auto online = [&](const bf16x8& kf, const bf16x8& v0, const bf16x8& v1, const bf16x8& q, f32x16& o, float& mrun) {
        f32x16 s = MFMA(kf, q, zero);
        float tm = s[0];
#pragma unroll
        for (int i = 1; i < 16; ++i) tm = fmaxf(tm, s[i]);
        tm = maxx32(tm);
        const float mn = fmaxf(mrun, tm), f = __builtin_amdgcn_exp2f(mrun - mn);
        mrun = mn;
#pragma unroll
        for (int i = 0; i < 16; ++i) { o[i] *= f; s[i] = __builtin_amdgcn_exp2f(s[i] - mn); }
        o = MFMA(v0, pack8(s[0], s[1], s[2], s[3], s[4], s[5], s[6], s[7]), o);
        o = MFMA(v1, pack8(s[8], s[9], s[10], s[11], s[12], s[13], s[14], s[15]), o);
      };
      for (int t = 0; t < 64; ++t) {
        if (t == 6 || t == 14 || t == 30 || t == 32 || t == 48) lds_bar();
        const bf16x8 kf = *(const LAS bf16x8*)(lds + l * 16 + t * 2048);
        const int vo = isv ? va0 + t * 2048 : va0;
        const bf16x8 v0 = *(const LAS bf16x8*)(lds + vo), v1 = *(const LAS bf16x8*)(lds + vo + 512);
        online(kf, v0, v1, qa, oa, ca);
        online(kf, v0, v1, qb_, ob, cb);
      }
    }
    const float ia = __builtin_amdgcn_rcpf(sumx32(oa[8])), ib = __builtin_amdgcn_rcpf(sumx32(ob[8]));
    const __amdgpu_buffer_rsrc_t rw = make_rsrc(ws, WS_USED);
    st16<MG>(rw, WS_O + (unsigned)(((qb * 8 + w) * 32 + head) * 64 + l) * 16, pack8(oa[0] * ia, oa[1] * ia, oa[2] * ia, oa[3] * ia, oa[4] * ia, oa[5] * ia, oa[6] * ia, oa[7] * ia));
    st16<MG>(rw, WS_O + (unsigned)(((qb * 8 + w + 4) * 32 + head) * 64 + l) * 16, pack8(ob[0] * ib, ob[1] * ib, ob[2] * ib, ob[3] * ib, ob[4] * ib, ob[5] * ib, ob[6] * ib, ob[7] * ib));
  }
  if constexpr (MG) {
    publish(ws, FL_B + (qb * 32 + head) * 64, c.epoch);
    if (b < 224) prefetch_w1<7>(p, c, 32, 80);
  }
}

template <int KQ, int HGAP> DI void quarter_reduce_t(int m, int l, LAS f32x4* xch, const f32x16 (&acc)[2], float (&val)[8]) {
#pragma unroll
  for (int d = 0; d < 4; ++d) {
    if (d == KQ) continue;
    xch[(d >> 1) * HGAP + (((d * 4 + KQ) * 2 + m) * 2 + 0) * 64 + l] = f32x4{acc[d >> 1][8 * (d & 1)], acc[d >> 1][8 * (d & 1) + 1], acc[d >> 1][8 * (d & 1) + 2], acc[d >> 1][8 * (d & 1) + 3]};
    xch[(d >> 1) * HGAP + (((d * 4 + KQ) * 2 + m) * 2 + 1) * 64 + l] = f32x4{acc[d >> 1][8 * (d & 1) + 4], acc[d >> 1][8 * (d & 1) + 5], acc[d >> 1][8 * (d & 1) + 6], acc[d >> 1][8 * (d & 1) + 7]};
  }
  lds_bar();
  f32x4 t[4][2];
#pragma unroll
  for (int s = 0; s < 4; ++s) { if (s == KQ) continue; t[s][0] = xch[(KQ >> 1) * HGAP + (((KQ * 4 + s) * 2 + m) * 2 + 0) * 64 + l]; t[s][1] = xch[(KQ >> 1) * HGAP + (((KQ * 4 + s) * 2 + m) * 2 + 1) * 64 + l]; }
#pragma unroll
  for (int j = 0; j < 8; ++j) val[j] = acc[KQ >> 1][8 * (KQ & 1) + j];
#pragma unroll
  for (int s = 0; s < 4; ++s) { if (s == KQ) continue; for (int j = 0; j < 4; ++j) { val[j] += t[s][0][j]; val[4 + j] += t[s][1][j]; } }
}
template <int HGAP> DI void quarter_reduce(const Ctx& c, LAS f32x4* xch, const f32x16 (&acc)[2], float (&val)[8]) {
  const int m = c.w & 1, kq = c.w >> 1;
  if (kq == 0) quarter_reduce_t<0, HGAP>(m, c.l, xch, acc, val);
  else if (kq == 1) quarter_reduce_t<1, HGAP>(m, c.l, xch, acc, val);
  else if (kq == 2) quarter_reduce_t<2, HGAP>(m, c.l, xch, acc, val);
  else quarter_reduce_t<3, HGAP>(m, c.l, xch, acc, val);
}

template <bool MG> DI void phaseC(const Params& p, const Ctx& c, u32x4& hkeep) {
  char* ws = p.ws;
  LAS char* lds = c.lds;
  const int l = c.l, w = c.w, hh = c.hh, b = c.b;
  const int rb = (b & 7) * 4 + ((b >> 3) & 3), cb = b >> 5;
  const int m = w & 1, kq = w >> 1;
  const int rt = 2 * rb + m, kstep = 4 * cb + kq;
  const GAS u32x2* xp = (const GAS u32x2*)((const char*)p.x + (size_t)rt * 32 * 1024 + kstep * 32 + (l & 31) * 1024 + hh * 8);
  const u32x2 xr0 = xp[0], xr1 = xp[2];
  const int q4 = (l & 15) >> 2, p4 = l & 3, blk = (l >> 4) & 1;
  const int wa0 = (128 * kq + 4 * hh + q4) * 128 + (((2 * blk + (p4 >> 1)) ^ (((q4 >> 1) & 1) << 2)) * 16) + (p4 & 1) * 8;
  const int wa1 = wa0 ^ 64;
  if constexpr (!MG) { prefetch_wo<8>(p, c); wait_bar<0>(); }
  bf16x8 wf[8][2];
#pragma unroll
  for (int i = 0; i < 8; ++i) {
    wf[i][0] = cat8(trread(lds + wa0 + i * 2048), trread(lds + wa0 + i * 2048 + 1024));
    wf[i][1] = cat8(trread(lds + wa1 + i * 2048), trread(lds + wa1 + i * 2048 + 1024));
  }
  if constexpr (MG) await(ws, FL_B + (rb >> 2) * 32 * 64, 32, c.epoch); else lds_bar();
  const char* abase = ws + WS_O + ((size_t)rt * 32 + kq * 8) * 1024;
  bf16x8 af[8];
  sfor<0, 8>([&](auto I) { constexpr int i = I.value; gload16<(i & 3) * 1024, MG>(af[i], abase + (i >> 2) * 4096, l * 16); });
  f32x16 acc[2] = {};
  sfor<0, 8>([&](auto I) {
    constexpr int i = I.value;
    asm volatile("s_waitcnt vmcnt(%1)" : "+v"(af[i]) : "i"(7 - i) : "memory");
    acc[0] = MFMA(wf[i][0], af[i], acc[0]);
    acc[1] = MFMA(wf[i][1], af[i], acc[1]);
    SBAR();
  });
  float val[8];
  quarter_reduce<0>(c, (LAS f32x4*)lds, acc, val);
  const u32x4 o = {cvtpk(val[0] + bf_lo(xr0[0]), val[1] + bf_hi(xr0[0])), cvtpk(val[2] + bf_lo(xr0[1]), val[3] + bf_hi(xr0[1])),
                   cvtpk(val[4] + bf_lo(xr1[0]), val[5] + bf_hi(xr1[0])), cvtpk(val[6] + bf_lo(xr1[1]), val[7] + bf_hi(xr1[1]))};
  hkeep = o;
  const __amdgpu_buffer_rsrc_t rw = make_rsrc(ws, WS_USED);
  st16<MG>(rw, WS_HF + (unsigned)((rt * 32 + kstep) * 64 + l) * 16, __builtin_bit_cast(bf16x8, o));
  f32x2 sm = {0.f, 0.f}, sq = {0.f, 0.f};
#pragma unroll
  for (int i = 0; i < 4; ++i) { const f32x2 v = unpk(o[i]); sm += v; sq = __builtin_elementwise_fma(v, v, sq); }
  LAS f32x2* tst = (LAS f32x2*)(lds + 0);
  tst[((m * 4 + kq) * 2 + hh) * 32 + (l & 31)] = f32x2{sm[0] + sm[1], sq[0] + sq[1]};
  lds_bar();
  if (w < 2 && l < 32) {
    f32x2 t8[8];
#pragma unroll
    for (int i = 0; i < 8; ++i) t8[i] = tst[(w * 8 + i) * 32 + l];
    const f32x2 t = ((t8[0] + t8[1]) + (t8[2] + t8[3])) + ((t8[4] + t8[5]) + (t8[6] + t8[7]));
    st8<MG>(rw, WS_STAT + (unsigned)(((2 * rb + w) * 8 + cb) * 32 + l) * 8, __builtin_bit_cast(u32x2, t));
  }
  if constexpr (MG) {
    publish(ws, FL_C + ((rb >> 1) * 16 + (rb & 1) * 8 + cb) * 64, c.epoch);
    if (b < 224) prefetch_w1<7>(p, c, 96, 128);
  }
}

constexpr int D_WIN = 6;
template <bool MG> DI void phaseD(const Params& p, const Ctx& c) {
  char* ws = p.ws;
  LAS char* lds = c.lds;
  const int l = c.l, w = c.w, hh = c.hh, b = c.b;
  const int rb = 2 * (b & 7) + ((b >> 3) & 1), cb = b >> 4;
  const int m = w & 3, nh = w >> 2;
  const int rt = 4 * rb + m;
  LAS float* tcs = (LAS float*)(lds + 49152);
  LAS float* tb = tcs + 128;
  if (c.tid < 128) tcs[c.tid] = 0.f;
  const bf16x8 bv = *(const GAS bf16x8*)((const char*)p.b1 + cb * 256 + (l & 15) * 16);
  if constexpr (!MG) { prefetch_w1<8>(p, c, 0, 128); wait_bar<0>(); }
  const int par = m >> 1;
  const int q4 = (l & 15) >> 2, p4 = l & 3, blk = (l >> 4) & 1;
  const int wl = nh * 8192 + (4 * hh + q4) * 128 + (((4 * (m & 1) + 2 * blk + (p4 >> 1)) ^ (((q4 >> 1) & 1) << 2)) * 16) + (p4 & 1) * 8;
  const int wa_e = wl + par * 2048, wa_o = wl + (1 - par) * 2048;
  bf16x8 wf[3][2];
  auto load_w = [&](auto I) {
    constexpr int i = I.value;
    constexpr int o = (int)w1_stage_base(i >> 2) + (i & 2) * 2048;
    const int wa = (i & 1) ? wa_o : wa_e;
    wf[i % 3][0] = cat8(trread(lds + wa + o), trread(lds + wa + o + 1024));
    wf[i % 3][1] = cat8(trread(lds + (wa ^ 64) + o), trread(lds + (wa ^ 64) + o + 1024));
  };
  load_w(IC<0>{}); load_w(IC<1>{});
  if constexpr (MG) await(ws, FL_C + rb * 16 * 64, 16, c.epoch); else lds_bar();
  const char* abase = ws + WS_HF + (size_t)rt * 32 * 1024;
  const unsigned aoff0 = l * 16 + par * 1024, aoff1 = l * 16 + (1 - par) * 1024;
  bf16x8 af[32];
  auto load_a = [&](auto I) { constexpr int i = I.value;
    if constexpr (i & 1) gload16<((i >> 1) & 1) * 2048, MG>(af[i], abase + (i >> 2) * 4096, aoff1);
    else gload16<((i >> 1) & 1) * 2048, MG>(af[i], abase + (i >> 2) * 4096, aoff0); };
  sfor<0, 4>(load_a);
  u32x2 st[4];
  sfor<0, 4>([&](auto I) { constexpr int i = I.value; gload8<i * 256, MG>(st[i], ws + WS_STAT + (size_t)rt * 2048, (l & 31) * 8 + hh * 1024); });
  sfor<4, D_WIN>(load_a);
  f32x16 acc[2] = {};
  f32x4 cs = {};
  const bf16x8 sel = rowsum_sel(l);
  sfor<0, 32>([&](auto I) {
    constexpr int i = I.value;
    if constexpr (i + D_WIN < 32) load_a(IC<i + D_WIN>{});
    constexpr int issued = i + D_WIN < 32 ? i + D_WIN + 1 : 32;
    asm volatile("s_waitcnt vmcnt(%1)" : "+v"(af[i]) : "i"(issued - i - 1 + (i < 4 ? 4 : 0)) : "memory");
    if constexpr (i == 20) asm volatile("s_barrier" ::: "memory");
    acc[0] = MFMA(wf[i % 3][0], af[i], acc[0]);
    acc[1] = MFMA(wf[i % 3][1], af[i], acc[1]);
    if constexpr ((i & 1) == 0) cs = MFMA16(wf[i % 3][0], sel, cs);
    if constexpr (i + 2 < 32) { SBAR(); load_w(IC<i + 2>{}); }
    SBAR();
  });
  if ((l & 15) < 2) {
    LAS float* d = tcs + nh * 64 + (m & 1) * 32 + 16 * (l & 15) + 4 * (l >> 4);
#pragma unroll
    for (int j = 0; j < 4; ++j) __hip_atomic_fetch_add(d + j, cs[j], __ATOMIC_RELAXED, __HIP_MEMORY_SCOPE_WORKGROUP);
  }
  if (w == 0 && l < 16) { const u32x4 u = __builtin_bit_cast(u32x4, bv);
    *(LAS f32x4*)(tb + l * 8) = f32x4{bf_lo(u[0]), bf_hi(u[0]), bf_lo(u[1]), bf_hi(u[1])}; *(LAS f32x4*)(tb + l * 8 + 4) = f32x4{bf_lo(u[2]), bf_hi(u[2]), bf_lo(u[3]), bf_hi(u[3])}; }
  asm volatile("" : "+v"(st[0]), "+v"(st[1]), "+v"(st[2]), "+v"(st[3]));
  const f32x2 ss = __builtin_bit_cast(f32x2, st[0]) + __builtin_bit_cast(f32x2, st[1]) + __builtin_bit_cast(f32x2, st[2]) + __builtin_bit_cast(f32x2, st[3]);
  const float sm = sumx32(ss[0]), sq = sumx32(ss[1]);
  const float mean = sm * (1.f / D), rstd = __builtin_amdgcn_rsqf(fmaxf(sq * (1.f / D) - mean * mean, 0.f) + LN_EPS);
  const float nmr = -mean * rstd;
  lds_bar();
  const __amdgpu_buffer_rsrc_t rw = make_rsrc(ws, WS_USED);
#pragma unroll
  for (int t = 0; t < 2; ++t) {
    const int ct = 2 * nh + ((m & 1) ^ t);
    f32x4 cv[4], bb[4];
#pragma unroll
    for (int g = 0; g < 4; ++g) { cv[g] = *(const LAS f32x4*)(tcs + 32 * ct + 8 * g + 4 * hh); bb[g] = *(const LAS f32x4*)(tb + 32 * ct + 8 * g + 4 * hh); }
#pragma unroll
    for (int s = 0; s < 2; ++s) {
      unsigned pk[4];
#pragma unroll
      for (int j = 0; j < 4; ++j) {
        const int r = 8 * s + 2 * j;
        const f32x2 a = {acc[t][r], acc[t][r + 1]}, c2 = {cv[r >> 2][r & 3], cv[r >> 2][(r & 3) + 1]}, b2 = {bb[r >> 2][r & 3], bb[r >> 2][(r & 3) + 1]};
        const f32x2 y = __builtin_elementwise_fma(a, f32x2{rstd, rstd}, __builtin_elementwise_fma(c2, f32x2{nmr, nmr}, b2));
        const f32x2 z = y * LOG2E;
        const f32x2 u = {__builtin_amdgcn_exp2f(z[0]), __builtin_amdgcn_exp2f(z[1])};
        const f32x2 d = __builtin_elementwise_fma(u, u + 2.f, f32x2{2.f, 2.f});
        const f32x2 r2 = {__builtin_amdgcn_rcpf(d[0]), __builtin_amdgcn_rcpf(d[1])};
        const f32x2 th = __builtin_elementwise_fma(r2, f32x2{-2.f, -2.f}, f32x2{1.f, 1.f});
        const f32x2 o = y * th;
        pk[j] = cvtpk(o[0], o[1]);
      }
      st16<MG>(rw, WS_ACT + (unsigned)((rt * 112 + cb * 8 + ct * 2 + s) * 64 + l) * 16, __builtin_bit_cast(bf16x8, u32x4{pk[0], pk[1], pk[2], pk[3]}));
      if constexpr (MG) {
        const int q = 2 * t + s;
        prefetch_w2<8>(p, c, 24 * q, 24 * q + 24);
      }
    }
  }
  if constexpr (MG) publish(ws, FL_D + (rb * 16 + cb) * 64, c.epoch);
}

constexpr int E_NSLOT = 10, E_NST = 14, E_WIN = 6, E_PRE = 8;
struct ESched {
  int posA[28], posW[E_NST], n_top[E_NST], n_mid[E_NST];
  constexpr ESched() : posA(), posW(), n_top(), n_mid() {
    int n = 0;
    for (int i = 0; i < E_WIN; ++i) posA[i] = n++;
    for (int st = 0; st < E_NST; ++st) {
      n_top[st] = n;
      if (E_PRE + st < E_NST) { n += 2; posW[E_PRE + st] = n - 1; }
      for (int j = 0; j < 2; ++j) if (E_WIN + 2 * st + j < 28) posA[E_WIN + 2 * st + j] = n++;
      n_mid[st] = n;
    }
  }
};
template <bool MG> DI void phaseE(const Params& p, const Ctx& c, u32x4 hkeep) {
  constexpr ESched sch{};
  char* ws = p.ws;
  LAS char* lds = c.lds;
  const int l = c.l, w = c.w, hh = c.hh, b = c.b;
  const int rb = (b & 7) * 4 + ((b >> 3) & 3), cb = b >> 5;
  const int m = w & 1, kq = w >> 1;
  const int rt = 2 * rb + m, kstep = 4 * cb + kq;
  const bool early = MG && b < 224;
  if (early) prefetch_w2<7>(p, c, 16 * 6, 16 * 8); else prefetch_w2<7>(p, c, 0, 16 * E_PRE);
  bf16x8 resv = __builtin_bit_cast(bf16x8, hkeep);
  if constexpr (!MG) resv = *(const GAS bf16x8*)(ws + WS_HF + (size_t)(rt * 32 + kstep) * 1024 + l * 16);
  const int q4 = (l & 15) >> 2, p4 = l & 3, blk = (l >> 4) & 1;
  const int wa0 = kq * 4096 + (4 * hh + q4) * 128 + (((2 * blk + (p4 >> 1)) ^ (((q4 >> 1) & 1) << 2)) * 16) + (p4 & 1) * 8;
  const int wa1 = wa0 ^ 64;
  bf16x8 wf[3][2][2];
  auto load_w = [&](auto ST) {
    constexpr int st = ST.value;
    const LAS char* sl = lds + ((st + 4) % E_NSLOT) * 16384;
#pragma unroll
    for (int i = 0; i < 2; ++i) {
      wf[st % 3][i][0] = cat8(trread(sl + wa0 + i * 2048), trread(sl + wa0 + i * 2048 + 1024));
      wf[st % 3][i][1] = cat8(trread(sl + wa1 + i * 2048), trread(sl + wa1 + i * 2048 + 1024));
    }
  };
  if (!early) wait_bar<0>();
  load_w(IC<0>{}); load_w(IC<1>{});
  if constexpr (MG) await(ws, FL_D + (rb >> 1) * 16 * 64, 14, c.epoch);
  const char* abase = ws + WS_ACT + ((size_t)rt * 112 + kq * 28) * 1024;
  bf16x8 af[28];
  auto load_a = [&](auto I) { constexpr int i = I.value; gload16<(i & 3) * 1024, MG>(af[i], abase + (i >> 2) * 4096, l * 16); };
  sfor<0, E_WIN>(load_a);
  f32x16 acc[2] = {};
  sfor<0, E_NST>([&](auto ST) {
    constexpr int st = ST.value;
    if constexpr (st == 2) asm volatile("s_waitcnt lgkmcnt(0)\n\ts_barrier" ::: "memory");
    if constexpr (st == 6 || st == 8 || st == 10) asm volatile("s_waitcnt vmcnt(%0) lgkmcnt(0)\n\ts_barrier" ::"i"(sch.n_top[st] - sch.posW[st + 3] - 1) : "memory");
    if constexpr (E_PRE + st < E_NST) prefetch_w2<8>(p, c, 16 * (st + E_PRE), 16 * (st + E_PRE + 1));
    if constexpr (E_WIN + 2 * st < 28) load_a(IC<E_WIN + 2 * st>{});
    if constexpr (E_WIN + 2 * st + 1 < 28) load_a(IC<E_WIN + 2 * st + 1>{});
    asm volatile("s_waitcnt vmcnt(%2)" : "+v"(af[2 * st]), "+v"(af[2 * st + 1]) : "i"(sch.n_mid[st] - sch.posA[2 * st + 1] - 1) : "memory");
    acc[0] = MFMA(wf[st % 3][0][0], af[2 * st], acc[0]);
    acc[1] = MFMA(wf[st % 3][0][1], af[2 * st], acc[1]);
    if constexpr (st + 2 < E_NST) { SBAR(); load_w(IC<st + 2>{}); SBAR(); }
    acc[0] = MFMA(wf[st % 3][1][0], af[2 * st + 1], acc[0]);
    acc[1] = MFMA(wf[st % 3][1][1], af[2 * st + 1], acc[1]);
    SBAR();
  });
  { asm volatile("" : "+v"(resv));
    const u32x4 ru = __builtin_bit_cast(u32x4, resv);
#pragma unroll
    for (int k = 0; k < 4; ++k) { const float on = kq == k ? 1.f : 0.f;
#pragma unroll
      for (int j = 0; j < 4; ++j) { acc[k >> 1][8 * (k & 1) + 2 * j] += on * bf_lo(ru[j]); acc[k >> 1][8 * (k & 1) + 2 * j + 1] += on * bf_hi(ru[j]); } } }
  constexpr int PS = 272, PQ = 64 * PS;
  LAS char* xa = lds;
#pragma unroll
  for (int ct = 0; ct < 2; ++ct)
#pragma unroll
    for (int g = 0; g < 4; ++g)
      *(LAS f32x4*)(xa + kq * PQ + (32 * m + (l & 31)) * PS + (32 * ct + 8 * g + 4 * hh) * 4) = f32x4{acc[ct][4 * g], acc[ct][4 * g + 1], acc[ct][4 * g + 2], acc[ct][4 * g + 3]};
  lds_bar();
  { const int row = c.tid >> 3, c8 = c.tid & 7;
    f32x4 t0[4], t1[4];
#pragma unroll
    for (int q = 0; q < 4; ++q) { t0[q] = *(const LAS f32x4*)(xa + q * PQ + row * PS + c8 * 32); t1[q] = *(const LAS f32x4*)(xa + q * PQ + row * PS + c8 * 32 + 16); }
    const f32x4 s0 = (t0[0] + t0[1]) + (t0[2] + t0[3]), s1 = (t1[0] + t1[1]) + (t1[2] + t1[3]);
    const __amdgpu_buffer_rsrc_t ro = make_rsrc(p.out, S * D * 2);
    st16<false>(ro, (unsigned)(rb * 64 + row) * 1024u + cb * 128 + c8 * 16, pack8(s0[0], s0[1], s0[2], s0[3], s1[0], s1[1], s1[2], s1[3])); }
}

DI Ctx make_ctx(char* lds_g) {
  Ctx c;
  c.lds = (LAS char*)lds_g; c.lds0 = (unsigned)(uintptr_t)lds_g;
  c.tid = threadIdx.x; c.l = c.tid & 63; c.w = rfl(c.tid >> 6); c.hh = c.l >> 5; c.b = blockIdx.x; c.epoch = 0;
  return c;
}

__global__ __launch_bounds__(512, 2) void block_mega(Params p) {
  extern __shared__ __attribute__((aligned(16))) char lds_g[];
  Ctx c = make_ctx(lds_g);
  c.epoch = __hip_atomic_load((GAS unsigned*)(p.ws + WS_FLAG) + (threadIdx.x >> 12), __ATOMIC_RELAXED, __HIP_MEMORY_SCOPE_AGENT);
  phaseA<true>(p, c);
  phaseB<true>(p, c);
  u32x4 hkeep;
  phaseC<true>(p, c, hkeep);
  if (c.b < 224) phaseD<true>(p, c);
  phaseE<true>(p, c, hkeep);
  if (c.b == 0 && c.tid == 0) __hip_atomic_store((GAS unsigned*)(p.ws + WS_FLAG), c.epoch, __ATOMIC_RELAXED, __HIP_MEMORY_SCOPE_AGENT);
  asm volatile("mega_code_end:");
}

extern "C" void kernel_launch(void* const* d_in, const int* in_sizes, int n_in, void* d_out, int out_size, void* d_ws, size_t ws_size, hipStream_t stream) {
  Params p;
  p.x = (const u16*)d_in[0]; p.wqkv = (const u16*)d_in[1]; p.wo = (const u16*)d_in[2];
  p.w1 = (const u16*)d_in[4]; p.b1 = (const u16*)d_in[5]; p.w2 = (const u16*)d_in[6];
  p.ws = (char*)d_ws; p.out = (u16*)d_out;
  (void)hipFuncSetAttribute((const void*)block_mega, hipFuncAttributeMaxDynamicSharedMemorySize, LDS_BYTES);
  block_mega<<<256, 512, LDS_BYTES, stream>>>(p);
}
```

```cpp
#include <hip/hip_runtime.h>
#include <hip/hip_bf16.h>
#include <type_traits>

typedef unsigned short u16;
using bf16x8 = __attribute__((ext_vector_type(8))) short;
using s16x4 = __attribute__((ext_vector_type(4))) short;
using f32x16 = __attribute__((ext_vector_type(16))) float;
using f32x4 = __attribute__((ext_vector_type(4))) float;
using f32x2 = __attribute__((ext_vector_type(2))) float;
using u32x4 = __attribute__((ext_vector_type(4))) unsigned;
using u32x2 = __attribute__((ext_vector_type(2))) unsigned;
using bf16x2_t = __attribute__((ext_vector_type(2))) __bf16;
#define DI __device__ __forceinline__
#define LAS __attribute__((address_space(3)))
#define GAS __attribute__((address_space(1)))
#define MFMA(a, b, c) __builtin_amdgcn_mfma_f32_32x32x16_bf16((a), (b), (c), 0, 0, 0)
#define SBAR() __builtin_amdgcn_sched_barrier(0)

constexpr int S = 2048, D = 512, F = 1792;
constexpr float LN_EPS = 1e-5f;
constexpr float LOG2E = 1.4426950408889634f;
constexpr float QSCALE = 0.25f * LOG2E;
constexpr float BOUND_SAFE = 40.f;
constexpr int LDS_BYTES = 163840;

constexpr unsigned WS_FLAG = 0;
constexpr unsigned FL_A = 4096, FL_B = FL_A + 256 * 64, FL_C = FL_B + 256 * 64, FL_D = FL_C + 256 * 64;
constexpr unsigned WS_KMAX = 128u << 10, WS_STAT = 256u << 10;
constexpr unsigned WS_Q = 1u << 20, WS_KV = 3u << 20  , WS_O = 7u << 20, WS_HF = 9u << 20, WS_ACT = 11u << 20;
constexpr unsigned WS_USED = 19u << 20;

struct Params { const u16 *x, *wqkv, *wo, *w1, *b1, *w2; char* ws; u16* out; };

template <int N> using IC = std::integral_constant<int, N>;
template <int B, int E, class Fn> DI void sfor(Fn&& f) { if constexpr (B < E) { f(IC<B>{}); sfor<B + 1, E>(f); } }

DI int rfl(int v) { return __builtin_amdgcn_readfirstlane(v); }
DI unsigned cvtpk(float lo, float hi) { const f32x2 v = {lo, hi}; return __builtin_bit_cast(unsigned, __builtin_convertvector(v, bf16x2_t)); }
DI float bf_lo(unsigned u) { return __uint_as_float(u << 16); }
DI float bf_hi(unsigned u) { return __uint_as_float(u & 0xffff0000u); }
constexpr unsigned ONES2 = 0x3F803F80u;
#define MFMA16(a, b, c) __builtin_amdgcn_mfma_f32_16x16x32_bf16((a), (b), (c), 0, 0, 0)
DI f32x2 unpk(unsigned u) { return f32x2{__uint_as_float(u << 16), __uint_as_float(u & 0xffff0000u)}; }
DI bf16x8 rowsum_sel(int l) { const unsigned on = ((l & 15) == ((l >> 4) & 1)) ? ONES2 : 0u; return __builtin_bit_cast(bf16x8, u32x4{on, on, on, on}); }
DI void dma16(const void* sbase, unsigned voff, unsigned lds) {
  asm volatile("s_mov_b32 m0, %2\n\ts_nop 0\n\tglobal_load_lds_dwordx4 %0, %1" ::"v"(voff), "s"(sbase), "s"(lds) : "memory");
}
DI void dma16_sc1(const void* sbase, unsigned voff, unsigned lds) {
  asm volatile("s_mov_b32 m0, %2\n\ts_nop 0\n\tglobal_load_lds_dwordx4 %0, %1 sc1" ::"v"(voff), "s"(sbase), "s"(lds) : "memory");
}
template <int IMM, bool SC1> DI void gload16(bf16x8& dst, const void* sbase, unsigned voff) {
  if constexpr (SC1) asm volatile("global_load_dwordx4 %0, %1, %2 offset:%3 sc1" : "=v"(dst) : "v"(voff), "s"(sbase), "i"(IMM) : "memory");
  else asm volatile("global_load_dwordx4 %0, %1, %2 offset:%3" : "=v"(dst) : "v"(voff), "s"(sbase), "i"(IMM) : "memory");
}
template <int IMM, bool SC1 = false> DI void gload8(u32x2& dst, const void* sbase, unsigned voff) {
  if constexpr (SC1) asm volatile("global_load_dwordx2 %0, %1, %2 offset:%3 sc1" : "=v"(dst) : "v"(voff), "s"(sbase), "i"(IMM) : "memory");
  else asm volatile("global_load_dwordx2 %0, %1, %2 offset:%3" : "=v"(dst) : "v"(voff), "s"(sbase), "i"(IMM) : "memory");
}
template <bool SC1> DI void gload4(float& dst, const void* sbase, unsigned voff) {
  if constexpr (SC1) asm volatile("global_load_dword %0, %1, %2 sc1" : "=v"(dst) : "v"(voff), "s"(sbase) : "memory");
  else asm volatile("global_load_dword %0, %1, %2" : "=v"(dst) : "v"(voff), "s"(sbase) : "memory");
}
typedef __attribute__((ext_vector_type(4))) unsigned v4u_;
DI __amdgpu_buffer_rsrc_t make_rsrc(void* p, unsigned bytes) { return __builtin_amdgcn_make_buffer_rsrc(p, 0, bytes, 0x00020000); }
template <bool SC1> DI void st16(__amdgpu_buffer_rsrc_t r, unsigned off, bf16x8 v) { __builtin_amdgcn_raw_buffer_store_b128(__builtin_bit_cast(v4u_, v), r, off, 0, SC1 ? 16 : 0); }
template <bool SC1> DI void st8(__amdgpu_buffer_rsrc_t r, unsigned off, u32x2 v) { __builtin_amdgcn_raw_buffer_store_b64(v, r, off, 0, SC1 ? 16 : 0); }
template <bool SC1> DI void st4(__amdgpu_buffer_rsrc_t r, unsigned off, unsigned v) { __builtin_amdgcn_raw_buffer_store_b32(v, r, off, 0, SC1 ? 16 : 0); }

template <int N> DI void wait_bar() { asm volatile("s_waitcnt vmcnt(%0) lgkmcnt(0)\n\ts_barrier" ::"i"(N) : "memory"); }
DI void lds_bar() { asm volatile("s_waitcnt lgkmcnt(0)\n\ts_barrier" ::: "memory"); }
DI s16x4 trread(const LAS char* p) {
  typedef short v4i16_t __attribute__((ext_vector_type(4)));
  return __builtin_bit_cast(s16x4, __builtin_amdgcn_ds_read_tr16_b64_v4i16((LAS v4i16_t*)p));
}
DI bf16x8 cat8(s16x4 a, s16x4 b) { return __builtin_shufflevector(a, b, 0, 1, 2, 3, 4, 5, 6, 7); }
DI bf16x8 pack8(float a0, float a1, float a2, float a3, float a4, float a5, float a6, float a7) {
  u32x4 w = {cvtpk(a0, a1), cvtpk(a2, a3), cvtpk(a4, a5), cvtpk(a6, a7)};
  return __builtin_bit_cast(bf16x8, w);
}
template <int CTRL> DI float dpp(float v) { return __uint_as_float(__builtin_amdgcn_update_dpp(0u, __float_as_uint(v), CTRL, 0xf, 0xf, true)); }
DI float sum8(float v) { v += dpp<0xB1>(v); v += dpp<0x4E>(v); v += dpp<0x141>(v); return v; }
DI float sumx32(float v) { auto r = __builtin_amdgcn_permlane32_swap(__float_as_uint(v), __float_as_uint(v), false, false); return __uint_as_float(r[0]) + __uint_as_float(r[1]); }
DI float maxx32(float v) { auto r = __builtin_amdgcn_permlane32_swap(__float_as_uint(v), __float_as_uint(v), false, false); return fmaxf(__uint_as_float(r[0]), __uint_as_float(r[1])); }
DI float maxx16(float v) { auto r = __builtin_amdgcn_permlane16_swap(__float_as_uint(v), __float_as_uint(v), false, false); return fmaxf(__uint_as_float(r[0]), __uint_as_float(r[1])); }
DI float max16(float v) { v = fmaxf(v, dpp<0xB1>(v)); v = fmaxf(v, dpp<0x4E>(v)); v = fmaxf(v, dpp<0x141>(v)); v = fmaxf(v, dpp<0x140>(v)); return v; }

DI void publish(char* ws, unsigned flag_off, unsigned epoch) {
  asm volatile("s_waitcnt vmcnt(0) lgkmcnt(0)\n\ts_barrier" ::: "memory");
  if (threadIdx.x == 0) __hip_atomic_store((GAS unsigned*)(ws + flag_off), epoch, __ATOMIC_RELAXED, __HIP_MEMORY_SCOPE_AGENT);
}
DI void await(char* ws, unsigned flag_off, int n, unsigned epoch) {
  if (threadIdx.x >= 448) {
    const int l = threadIdx.x & 63;
    GAS unsigned* f = (GAS unsigned*)(ws + flag_off + (l < n ? l : 0) * 64);
    for (unsigned spins = 0; spins < (1u << 20); ++spins) {
      const unsigned v = __hip_atomic_load(f, __ATOMIC_RELAXED, __HIP_MEMORY_SCOPE_AGENT);
      if (__all(v == epoch)) break;
      __builtin_amdgcn_s_sleep(1);
    }
  }
  asm volatile("s_waitcnt lgkmcnt(0)\n\ts_barrier" ::: "memory");
}

struct Ctx { LAS char* lds; unsigned lds0; int tid, l, w, hh, b; unsigned epoch; };

template <bool MG> DI void phaseA(const Params& p, Ctx& c) {
  char* ws = p.ws;
  LAS char* lds = c.lds;
  const int l = c.l, w = c.w, hh = c.hh, b = c.b;
  const int rb = 8 * ((b & 7) >> 1) + ((b >> 3) & 7), hg = 4 * (b & 1) + (b >> 6);
  bf16x8 ft[3] = {};
  if constexpr (MG) {
    unsigned long long pc; unsigned len;
    asm volatile("s_getpc_b64 %0\n.Lmega_here%=:\n\ts_mov_b32 %1, mega_code_end-.Lmega_here%=" : "=s"(pc), "=s"(len));
    const unsigned j = (unsigned)(b >> 3);
    const unsigned oc = j * 1024u + (w & 1) * 32768u + l * 16u;
    ft[2] = *(const GAS bf16x8*)((const char*)pc + (w < 2 && oc + 16 <= len ? oc : 0u));
    ft[0] = *(const GAS bf16x8*)((const char*)p.x + (size_t)b * 8192 + w * 1024 + l * 16);
    ft[1] = *(const GAS bf16x8*)((const char*)p.wqkv + (size_t)b * 6144 + (w < 6 ? w : 5) * 1024 + l * 16);
  }
  const int q4 = (l & 15) >> 2, p4 = l & 3, blk = (l >> 4) & 1;
  LAS float* tmean = (LAS float*)(lds + 0);
  LAS float* trstd = tmean + 64;
  LAS float* tcs = tmean + 128;
  f32x16 acc[3] = {};
  const int m = w & 1, g = (w >> 1) & 1;
  if (w < 4) {
    const bf16x8 sel = rowsum_sel(l);
    const int swz = (l >> 1) & 7;
    const int xrow = (32 * m + (l & 31)) * 128;
    const int wa = 8192 + (8 * hh + q4) * 128 + (((4 * g + 2 * blk + (p4 >> 1)) ^ (((q4 >> 1) & 1) << 2)) * 16) + (p4 & 1) * 8;
    int xo[4], wo_[2];
#pragma unroll
    for (int i = 0; i < 4; ++i) xo[i] = xrow + (((2 * (i ^ m) + hh) ^ swz) * 16);
    wo_[0] = wa + m * 2048; wo_[1] = wa + (1 - m) * 2048;
    bf16x8 xf[2][4], wf[2][4][3];
    f32x4 cs[3] = {};
    auto ld_w = [&](auto SET, const LAS char* sl, int i, int sgm) { constexpr int q = SET.value; const int o = wo_[i & 1] + (i & 2) * 2048 + sgm * 8192;
      wf[q][i][sgm] = cat8(trread(sl + o), trread(sl + o + 512)); };
    lds_bar();
#pragma unroll
    for (int i = 0; i < 4; ++i) { xf[0][i] = *(const LAS bf16x8*)(lds + xo[i]); for (int sgm = 0; sgm < 3; ++sgm) ld_w(IC<0>{}, lds, i, sgm); }
    sfor<0, 8>([&](auto ST) {
      constexpr int st = ST.value, q = st & 1, qn = (st + 1) & 1;
      if constexpr (st + 1 < 8) lds_bar(); else asm volatile("s_waitcnt lgkmcnt(0)" ::: "memory");
      asm volatile("" : "+v"(xf[q][0]), "+v"(xf[q][1]), "+v"(xf[q][2]), "+v"(xf[q][3]), "+v"(wf[q][0][0]), "+v"(wf[q][0][1]), "+v"(wf[q][0][2]), "+v"(wf[q][1][0]), "+v"(wf[q][1][1]), "+v"(wf[q][1][2]),
                   "+v"(wf[q][2][0]), "+v"(wf[q][2][1]), "+v"(wf[q][2][2]), "+v"(wf[q][3][0]), "+v"(wf[q][3][1]), "+v"(wf[q][3][2]));
      const LAS char* sn = lds + ((st + 1) % 5) * 32768;
#pragma unroll
      for (int i = 0; i < 4; ++i) {
        acc[0] = MFMA(wf[q][i][0], xf[q][i], acc[0]);
        SBAR();
        if constexpr (st + 1 < 8) { ld_w(IC<qn>{}, sn, i, 0); xf[qn][i] = *(const LAS bf16x8*)(sn + xo[i]); }
        SBAR();
        acc[1] = MFMA(wf[q][i][1], xf[q][i], acc[1]);
        SBAR();
        if constexpr (st + 1 < 8) ld_w(IC<qn>{}, sn, i, 1);
        SBAR();
        acc[2] = MFMA(xf[q][i], wf[q][i][2], acc[2]);
        SBAR();
        if constexpr (st + 1 < 8) ld_w(IC<qn>{}, sn, i, 2);
        if ((i & 1) == 0) { cs[0] = MFMA16(wf[q][i][0], sel, cs[0]); cs[1] = MFMA16(wf[q][i][1], sel, cs[1]); cs[2] = MFMA16(wf[q][i][2], sel, cs[2]); }
        SBAR();
      }
    });
    if ((l & 15) < 2) {
#pragma unroll
      for (int t = 0; t < 3; ++t) *(LAS f32x4*)(tcs + (w * 3 + t) * 32 + 16 * (l & 15) + 4 * (l >> 4)) = cs[t];
    }
  } else {
    const int h = w - 4;
    const unsigned xo0 = (unsigned)(64 * rb + 16 * h + (l >> 3)) * 1024u + (unsigned)(((l & 7) ^ ((l >> 4) & 7)) * 16);
    const unsigned xo1 = (unsigned)(64 * rb + 16 * h + 8 + (l >> 3)) * 1024u + (unsigned)(((l & 7) ^ ((4 + (l >> 4)) & 7)) * 16);
    const unsigned wof = (unsigned)(16 * h + (l >> 3)) * 3072u + (unsigned)hg * 128u + (unsigned)(((l & 7) ^ (((l >> 4) & 1) << 2)) * 16);
    auto issue = [&](int st) {
      const unsigned dst = c.lds0 + (st % 5) * 32768 + h * 2048;
      const char* wsrc = (const char*)p.wqkv + (size_t)st * 64 * 3072;
      dma16((const char*)p.x + st * 128, xo0, dst);
      dma16((const char*)p.x + st * 128, xo1, dst + 1024);
#pragma unroll
      for (int sgm = 0; sgm < 3; ++sgm) { dma16(wsrc + sgm * 1024, wof, dst + 8192 + sgm * 8192); dma16(wsrc + sgm * 1024 + 8 * 3072, wof, dst + 8192 + sgm * 8192 + 1024); }
    };
    issue(0); issue(1); issue(2); issue(3); issue(4);
    f32x2 rs[2] = {}, rq[2] = {};
    u32x4 sv[2][2];
    wait_bar<32>();
    sv[0][0] = *(const LAS u32x4*)(lds + h * 2048 + l * 16); sv[0][1] = *(const LAS u32x4*)(lds + h * 2048 + 1024 + l * 16);
    sfor<0, 8>([&](auto ST) {
      constexpr int st = ST.value, q = st & 1, qn = (st + 1) & 1;
      if constexpr (st + 1 < 8) {
        constexpr int last = 4 + st > 7 ? 7 : 4 + st;
        wait_bar<8 * (last - (st + 1))>();
        if constexpr (st + 5 < 8) issue(st + 5);
        const LAS char* sn = lds + ((st + 1) % 5) * 32768 + h * 2048 + l * 16;
        sv[qn][0] = *(const LAS u32x4*)sn; sv[qn][1] = *(const LAS u32x4*)(sn + 1024);
      }
#pragma unroll
      for (int r = 0; r < 2; ++r)
#pragma unroll
        for (int i = 0; i < 4; ++i) { const f32x2 v = unpk(sv[q][r][i]); rs[r] += v; rq[r] = __builtin_elementwise_fma(v, v, rq[r]); }
      asm volatile("" : "+v"(rs[0]), "+v"(rq[0]), "+v"(rs[1]), "+v"(rq[1]));
    });
#pragma unroll
    for (int r = 0; r < 2; ++r) {
      const float s1 = sum8(rs[r][0] + rs[r][1]), s2 = sum8(rq[r][0] + rq[r][1]);
      if ((l & 7) == 0) { const float mean = s1 * (1.f / D); const int row = 16 * h + 8 * r + (l >> 3);
        tmean[row] = mean; trstd[row] = __builtin_amdgcn_rsqf(fmaxf(s2 * (1.f / D) - mean * mean, 0.f) + LN_EPS); }
    }
  }
  if constexpr (MG) { asm volatile("" : "+v"(c.epoch), "+v"(ft[0]), "+v"(ft[1]), "+v"(ft[2])); c.epoch += 1u; }
  lds_bar();
  const __amdgpu_buffer_rsrc_t rw = make_rsrc(ws, WS_USED);
  if (w < 4) {
    const int tile = 2 * rb + m;
    const float mean_l = tmean[32 * m + (l & 31)], rstd_l = trstd[32 * m + (l & 31)];
    f32x4 cv[2][4], mm[4], rr[4];
#pragma unroll
    for (int gg = 0; gg < 4; ++gg) {
#pragma unroll
      for (int t = 0; t < 2; ++t) cv[t][gg] = *(const LAS f32x4*)(tcs + (w * 3 + t) * 32 + 8 * gg + 4 * hh) + *(const LAS f32x4*)(tcs + ((w ^ 1) * 3 + t) * 32 + 8 * gg + 4 * hh);
      mm[gg] = *(const LAS f32x4*)(tmean + 32 * m + 8 * gg + 4 * hh); rr[gg] = *(const LAS f32x4*)(trstd + 32 * m + 8 * gg + 4 * hh);
    }
    const int n = l & 31;
    const float ncs = -(tcs[(w * 3 + 2) * 32 + n] + tcs[((w ^ 1) * 3 + 2) * 32 + n]);
#pragma unroll
    for (int s = 0; s < 2; ++s) {
      f32x2 v[4];
#pragma unroll
      for (int j = 0; j < 4; ++j) { const int r = 8 * s + 2 * j; const f32x2 a = {acc[2][r], acc[2][r + 1]}, m2 = {mm[r >> 2][r & 3], mm[r >> 2][(r & 3) + 1]}, r2 = {rr[r >> 2][r & 3], rr[r >> 2][(r & 3) + 1]};
        v[j] = __builtin_elementwise_fma(m2, f32x2{ncs, ncs}, a) * r2; }
      st16<MG>(rw, WS_KV + (unsigned)((4 * hg + 2 * g + (n >> 4)) * 64 + tile) * 2048 + 1024 + s * 512 + hh * 256 + (n & 15) * 16,
               pack8(v[0][0], v[0][1], v[1][0], v[1][1], v[2][0], v[2][1], v[3][0], v[3][1]));
    }
#pragma unroll
    for (int t = 1; t >= 0; --t) {
      const float sc = t == 0 ? rstd_l * QSCALE : rstd_l, nm = -mean_l * sc;
#pragma unroll
      for (int s = 0; s < 2; ++s) {
        f32x2 v[4];
#pragma unroll
        for (int j = 0; j < 4; ++j) { const int r = 8 * s + 2 * j; const f32x2 a = {acc[t][r], acc[t][r + 1]}, cc = {cv[t][r >> 2][r & 3], cv[t][r >> 2][(r & 3) + 1]};
          v[j] = __builtin_elementwise_fma(a, f32x2{sc, sc}, cc * nm); }
        const int head = 4 * hg + 2 * g + s;
        st16<MG>(rw, (t == 0 ? WS_Q + (unsigned)(head * 64 + tile) * 1024 : WS_KV + (unsigned)(head * 64 + tile) * 2048) + l * 16, pack8(v[0][0], v[0][1], v[1][0], v[1][1], v[2][0], v[2][1], v[3][0], v[3][1]));
        if (t == 1) {
          f32x2 n2 = v[0] * v[0]; n2 = __builtin_elementwise_fma(v[1], v[1], n2); n2 = __builtin_elementwise_fma(v[2], v[2], n2); n2 = __builtin_elementwise_fma(v[3], v[3], n2);
          const float mx = maxx16(max16(sumx32(n2[0] + n2[1])));
          if (l == 0) st4<MG>(rw, WS_KMAX + (unsigned)(head * 64 + tile) * 4, __float_as_uint(mx));
        }
      }
    }
  }
  if constexpr (MG) publish(ws, FL_A + (hg * 32 + rb) * 64, c.epoch);
}

DI unsigned wpiece_lane(int l, unsigned row_bytes) { return (unsigned)(l >> 3) * row_bytes + (unsigned)(((l & 7) ^ (((l >> 4) & 1) << 2)) * 16); }
template <int NW, int W0 = 0> DI void prefetch_wo(const Params& p, const Ctx& c) {
  if (c.w < W0 || c.w >= W0 + NW) return;
  const unsigned off = wpiece_lane(c.l, 1024) + (unsigned)(c.b >> 5) * 128u;
  for (int pc = c.w - W0; pc < 64; pc += NW) dma16((const char*)p.wo + (size_t)(8 * pc) * 1024, off, c.lds0 + pc * 1024);
}
DI constexpr unsigned w1_stage_base(int j) { return j < 6 ? 65536 + j * 16384 : (j - 6) * 16384; }
template <int NW, int W0 = 0> DI void prefetch_w1(const Params& p, const Ctx& c, int p0, int p1) {
  if (c.w < W0 || c.w >= W0 + NW) return;
  const unsigned off = wpiece_lane(c.l, F * 2) + (unsigned)(c.b >> 4) * 256u;
  for (int pc = p0 + c.w - W0; pc < p1; pc += NW) {
    const int j = pc >> 4, seg = (pc >> 3) & 1, r8 = pc & 7;
    dma16((const char*)p.w1 + (size_t)(64 * j + 8 * r8) * (F * 2) + seg * 128, off, c.lds0 + w1_stage_base(j) + seg * 8192 + r8 * 1024);
  }
}
template <int NW> DI void prefetch_w2(const Params& p, const Ctx& c, int p0, int p1) {
  if (c.w >= NW) return;
  const unsigned off = wpiece_lane(c.l, 1024) + (unsigned)(c.b >> 5) * 128u;
  for (int pc = p0 + c.w; pc < p1; pc += NW) {
    const int st = pc >> 4, kq = (pc >> 2) & 3, r8 = pc & 3;
    dma16((const char*)p.w2 + (size_t)(448 * kq + 32 * st + 8 * r8) * 1024, off, c.lds0 + ((st + 4) % 10) * 16384 + kq * 4096 + r8 * 1024);
  }
}

DI void warm_first(const Params& p, const Ctx& c) {
  const int h = c.w - 4;
  const unsigned off1 = (unsigned)c.b * 7168u + (unsigned)h * 2048u + (unsigned)c.l * 16u;
  dma16((const char*)p.w1, off1, c.lds0 + 139520);
  dma16((const char*)p.w2, off1, c.lds0 + 139520);
  if (h < 3) { dma16((const char*)p.w1 + 1024, off1, c.lds0 + 139520); dma16((const char*)p.w2 + 1024, off1, c.lds0 + 139520); }
}
DI void warm_w1_late(const Params& p, const Ctx& c) {
  const int h = c.w - 4;
  const unsigned j = (unsigned)(c.b >> 3);
  for (int pc = h; pc < 35; pc += 4)
    dma16((const char*)p.w1, (pc < 21 ? 128u * 3584u + j * 21504u + pc * 1024u : 384u * 3584u + j * 14336u + (pc - 21) * 1024u) + c.l * 16u, c.lds0 + 139520);
}
DI void warm_w2(const Params& p, const Ctx& c) {
  const int h = c.w - 4;
  const unsigned off = (unsigned)(c.b >> 3) * 57344u + (unsigned)h * 14336u + (unsigned)c.l * 16u;
#pragma unroll
  for (int i = 0; i < 14; ++i) dma16((const char*)p.w2 + i * 1024, off, c.lds0 + 139520);
}

constexpr int B_ONES = 131072;
template <bool MG> DI void phaseB(const Params& p, const Ctx& c) {
  char* ws = p.ws;
  LAS char* lds = c.lds;
  const int l = c.l, w = c.w, hh = c.hh, b = c.b, tid = c.tid;
  const int head = 4 * (b & 7) + ((b >> 3) & 3), qb = b >> 5;
  for (int i = tid; i < (4096 + 512) / 16; i += 512) { const unsigned v = (i & 31) == 0 ? ONES2 : 0u; ((LAS u32x4*)(lds + B_ONES))[i] = u32x4{v, v, v, v}; }
  if constexpr (MG) await(ws, FL_A + (head >> 2) * 32 * 64, 32, c.epoch);
  const unsigned l16 = l * 16;
  if (w >= 4) {
    const char* kvg = ws + WS_KV + (size_t)head * 131072;
    const int h = w - 4;
    auto piece = [&](int pc) { if constexpr (MG) dma16_sc1(kvg + pc * 1024, l16, c.lds0 + pc * 1024); else dma16(kvg + pc * 1024, l16, c.lds0 + pc * 1024); };
#pragma unroll
    for (int i = 0; i < 4; ++i) piece(4 * h + i);
#pragma unroll
    for (int i = 0; i < 12; ++i) piece(16 + 4 * i + h);
    wait_bar<12>();
#pragma unroll
    for (int i = 0; i < 16; ++i) piece(64 + 4 * i + h);
    wait_bar<24>();
    wait_bar<16>();
    if constexpr (MG) warm_first(p, c);
    wait_bar<0>();
    if constexpr (MG) { if (b < 224) prefetch_w1<4, 4>(p, c, 80, 96); }
    lds_bar();
    if constexpr (MG) { prefetch_wo<4, 4>(p, c); warm_w1_late(p, c); }
    lds_bar();
    if constexpr (MG) { if (b < 224) prefetch_w1<4, 4>(p, c, 0, 32); warm_w2(p, c); }
  } else {
    bf16x8 qa, qb_;
    float kmx;
    gload4<MG>(kmx, ws + WS_KMAX + head * 256, l * 4);
    gload16<0, MG>(qa, ws + WS_Q + (size_t)(head * 64 + qb * 8 + w) * 1024, l * 16);
    gload16<0, MG>(qb_, ws + WS_Q + (size_t)(head * 64 + qb * 8 + w + 4) * 1024, l * 16);
    asm volatile("s_waitcnt vmcnt(0)" : "+v"(qa), "+v"(qb_), "+v"(kmx)::"memory");
    kmx = maxx32(maxx16(max16(kmx)));
    auto qbound = [&](bf16x8 q) { const u32x4 u = __builtin_bit_cast(u32x4, q); f32x2 t = {0.f, 0.f}; for (int i = 0; i < 4; ++i) { const f32x2 v = unpk(u[i]); t = __builtin_elementwise_fma(v, v, t); }
      return __builtin_amdgcn_sqrtf(sumx32(t[0] + t[1]) * kmx) * 1.01f + 1e-6f; };
    const float bna = qbound(qa), bnb = qbound(qb_);
    const bool safe = __all(bna <= BOUND_SAFE && bnb <= BOUND_SAFE);
    f32x16 oa = {}, ob = {};
    const int lo5 = l & 31;
    const bool isv = lo5 < 16;
    const int va0 = isv ? 1024 + hh * 256 + lo5 * 16 : B_ONES + (lo5 == 16 ? 0 : (lo5 >= 20 && lo5 < 28) ? 64 : 16);
    lds_bar();
    if (safe) {
      f32x16 nga, ngb; for (int i = 0; i < 16; ++i) { nga[i] = -bna; ngb[i] = -bnb; }
      f32x16 s0, s1;
      bf16x8 pa, pb = {};
      bf16x8 kf, v0, v1 = {};
      unsigned ka = c.lds0 + l * 16, va = c.lds0 + va0;
      const unsigned vstep = isv ? 4096u : 0u;
      asm volatile("ds_read_b128 %1, %3\n\tds_read_b128 %2, %4\n\ts_waitcnt lgkmcnt(0)\n\t"
                   "v_mfma_f32_32x32x16_bf16 %0, %1, %5, %6\n\ts_nop 7\n\ts_nop 3"
                   : "=&{v[192:207]}"(s0), "=&v"(kf), "=&v"(v0) : "v"(ka), "v"(va), "v"(qa), "v"(nga) : "memory");
#define E_(b, i) "v_exp_f32 v[" #b "+" #i "], v[" #b "+" #i "]\n\t"
#define C_(d, i, b, j, k) "v_cvt_pk_bf16_f32 v[" #d "+" #i "], v[" #b "+" #j "], v[" #b "+" #k "]\n\t"
#define MID_(SC) E_(SC, 4) E_(SC, 5) E_(SC, 6) E_(SC, 7) C_(224, 0, SC, 0, 1) C_(224, 1, SC, 2, 3) E_(SC, 8) E_(SC, 9) C_(224, 2, SC, 4, 5) C_(224, 3, SC, 6, 7) E_(SC, 10) E_(SC, 11)
#define END_(SC) E_(SC, 12) E_(SC, 13) E_(SC, 14) E_(SC, 15) C_(228, 0, SC, 8, 9) C_(228, 1, SC, 10, 11) C_(228, 2, SC, 12, 13) C_(228, 3, SC, 14, 15)
#define UNIT_A(KOFF, V1OFF) \
      "v_mfma_f32_32x32x16_bf16 v[208:223], %[kf], %[qb], %[ngb]\n\t" \
      "ds_read_b128 %[kf], %[ka] offset:" #KOFF "\n\t" \
      E_(192, 0) E_(192, 1) E_(192, 2) E_(192, 3) \
      "v_mfma_f32_32x32x16_bf16 %[ob], %[v1], v[228:231], %[ob]\n\t" \
      "ds_read_b128 %[v1], %[va] offset:" #V1OFF "\n\t" \
      MID_(192) \
      "s_waitcnt lgkmcnt(2)\n\t" \
      "v_mfma_f32_32x32x16_bf16 %[oa], %[v0], v[224:227], %[oa]\n\t" \
      END_(192)
#define UNIT_B(V0OFF) \
      "s_waitcnt lgkmcnt(0)\n\t" \
      "v_mfma_f32_32x32x16_bf16 v[192:207], %[kf], %[qa], %[nga]\n\t" \
      E_(208, 0) E_(208, 1) E_(208, 2) E_(208, 3) \
      "v_mfma_f32_32x32x16_bf16 %[oa], %[v1], v[228:231], %[oa]\n\t" \
      MID_(208) \
      "v_mfma_f32_32x32x16_bf16 %[ob], %[v0], v[224:227], %[ob]\n\t" \
      "ds_read_b128 %[v0], %[va] offset:" #V0OFF "\n\t" \
      END_(208)
#define RUN_GROUPS(NG) do { int cnt_ = (NG); \
      asm volatile("1:\n\t" UNIT_A(2048, 512) UNIT_B(2048) UNIT_A(4096, 2560) UNIT_B(4096) \
        "v_add_u32 %[ka], 0x1000, %[ka]\n\tv_add_u32 %[va], %[va], %[vs]\n\t" \
        "s_sub_u32 %[cnt], %[cnt], 1\n\ts_cmp_lg_u32 %[cnt], 0\n\ts_cbranch_scc1 1b\n\ts_waitcnt lgkmcnt(0)\n\ts_nop 7\n\ts_nop 3" \
        : "+{v[192:207]}"(s0), "=&{v[208:223]}"(s1), "=&{v[224:227]}"(pa), "+{v[228:231]}"(pb), [oa] "+v"(oa), [ob] "+v"(ob), [kf] "+v"(kf), [v0] "+v"(v0), [v1] "+v"(v1), \
          [ka] "+v"(ka), [va] "+v"(va), [cnt] "+s"(cnt_) \
        : [qa] "v"(qa), [qb] "v"(qb_), [nga] "v"(nga), [ngb] "v"(ngb), [vs] "v"(vstep) : "memory", "scc"); } while (0)
      RUN_GROUPS(3);
      lds_bar();
      RUN_GROUPS(4);
      lds_bar();
      RUN_GROUPS(8);
      lds_bar();
      RUN_GROUPS(1);
      lds_bar();
      RUN_GROUPS(8);
      lds_bar();
      RUN_GROUPS(8);
      ob = MFMA(v1, pb, ob);
    } else {
      float ca = -3.0e38f, cb = -3.0e38f;
      const f32x16 zero = {};
      auto online = [&](const bf16x8& kf, const bf16x8& v0, const bf16x8& v1, const bf16x8& q, f32x16& o, float& mrun) {
        f32x16 s = MFMA(kf, q, zero);
        float tm = s[0];
#pragma unroll
        for (int i = 1; i < 16; ++i) tm = fmaxf(tm, s[i]);
        tm = maxx32(tm);
        const float mn = fmaxf(mrun, tm), f = __builtin_amdgcn_exp2f(mrun - mn);
        mrun = mn;
#pragma unroll
        for (int i = 0; i < 16; ++i) { o[i] *= f; s[i] = __builtin_amdgcn_exp2f(s[i] - mn); }
        o = MFMA(v0, pack8(s[0], s[1], s[2], s[3], s[4], s[5], s[6], s[7]), o);
        o = MFMA(v1, pack8(s[8], s[9], s[10], s[11], s[12], s[13], s[14], s[15]), o);
      };
      for (int t = 0; t < 64; ++t) {
        if (t == 6 || t == 14 || t == 30 || t == 32 || t == 48) lds_bar();
        const bf16x8 kf = *(const LAS bf16x8*)(lds + l * 16 + t * 2048);
        const int vo = isv ? va0 + t * 2048 : va0;
        const bf16x8 v0 = *(const LAS bf16x8*)(lds + vo), v1 = *(const LAS bf16x8*)(lds + vo + 512);
        online(kf, v0, v1, qa, oa, ca);
        online(kf, v0, v1, qb_, ob, cb);
      }
    }
    const float ia = __builtin_amdgcn_rcpf(sumx32(oa[8])), ib = __builtin_amdgcn_rcpf(sumx32(ob[8]));
    const __amdgpu_buffer_rsrc_t rw = make_rsrc(ws, WS_USED);
    st16<MG>(rw, WS_O + (unsigned)(((qb * 8 + w) * 32 + head) * 64 + l) * 16, pack8(oa[0] * ia, oa[1] * ia, oa[2] * ia, oa[3] * ia, oa[4] * ia, oa[5] * ia, oa[6] * ia, oa[7] * ia));
    st16<MG>(rw, WS_O + (unsigned)(((qb * 8 + w + 4) * 32 + head) * 64 + l) * 16, pack8(ob[0] * ib, ob[1] * ib, ob[2] * ib, ob[3] * ib, ob[4] * ib, ob[5] * ib, ob[6] * ib, ob[7] * ib));
  }
  if constexpr (MG) {
    publish(ws, FL_B + (qb * 32 + head) * 64, c.epoch);
    if (b < 224) prefetch_w1<7>(p, c, 32, 80);
  }
}

template <int KQ, int HGAP> DI void quarter_reduce_t(int m, int l, LAS f32x4* xch, const f32x16 (&acc)[2], float (&val)[8]) {
#pragma unroll
  for (int d = 0; d < 4; ++d) {
    if (d == KQ) continue;
    xch[(d >> 1) * HGAP + (((d * 4 + KQ) * 2 + m) * 2 + 0) * 64 + l] = f32x4{acc[d >> 1][8 * (d & 1)], acc[d >> 1][8 * (d & 1) + 1], acc[d >> 1][8 * (d & 1) + 2], acc[d >> 1][8 * (d & 1) + 3]};
    xch[(d >> 1) * HGAP + (((d * 4 + KQ) * 2 + m) * 2 + 1) * 64 + l] = f32x4{acc[d >> 1][8 * (d & 1) + 4], acc[d >> 1][8 * (d & 1) + 5], acc[d >> 1][8 * (d & 1) + 6], acc[d >> 1][8 * (d & 1) + 7]};
  }
  lds_bar();
  f32x4 t[4][2];
#pragma unroll
  for (int s = 0; s < 4; ++s) { if (s == KQ) continue; t[s][0] = xch[(KQ >> 1) * HGAP + (((KQ * 4 + s) * 2 + m) * 2 + 0) * 64 + l]; t[s][1] = xch[(KQ >> 1) * HGAP + (((KQ * 4 + s) * 2 + m) * 2 + 1) * 64 + l]; }
#pragma unroll
  for (int j = 0; j < 8; ++j) val[j] = acc[KQ >> 1][8 * (KQ & 1) + j];
#pragma unroll
  for (int s = 0; s < 4; ++s) { if (s == KQ) continue; for (int j = 0; j < 4; ++j) { val[j] += t[s][0][j]; val[4 + j] += t[s][1][j]; } }
}
template <int HGAP> DI void quarter_reduce(const Ctx& c, LAS f32x4* xch, const f32x16 (&acc)[2], float (&val)[8]) {
  const int m = c.w & 1, kq = c.w >> 1;
  if (kq == 0) quarter_reduce_t<0, HGAP>(m, c.l, xch, acc, val);
  else if (kq == 1) quarter_reduce_t<1, HGAP>(m, c.l, xch, acc, val);
  else if (kq == 2) quarter_reduce_t<2, HGAP>(m, c.l, xch, acc, val);
  else quarter_reduce_t<3, HGAP>(m, c.l, xch, acc, val);
}

template <bool MG> DI void phaseC(const Params& p, const Ctx& c, u32x4& hkeep) {
  char* ws = p.ws;
  LAS char* lds = c.lds;
  const int l = c.l, w = c.w, hh = c.hh, b = c.b;
  const int rb = (b & 7) * 4 + ((b >> 3) & 3), cb = b >> 5;
  const int m = w & 1, kq = w >> 1;
  const int rt = 2 * rb + m, kstep = 4 * cb + kq;
  const GAS u32x2* xp = (const GAS u32x2*)((const char*)p.x + (size_t)rt * 32 * 1024 + kstep * 32 + (l & 31) * 1024 + hh * 8);
  const u32x2 xr0 = xp[0], xr1 = xp[2];
  const int q4 = (l & 15) >> 2, p4 = l & 3, blk = (l >> 4) & 1;
  const int wa0 = (128 * kq + 4 * hh + q4) * 128 + (((2 * blk + (p4 >> 1)) ^ (((q4 >> 1) & 1) << 2)) * 16) + (p4 & 1) * 8;
  const int wa1 = wa0 ^ 64;
  if constexpr (!MG) { prefetch_wo<8>(p, c); wait_bar<0>(); }
  bf16x8 wf[8][2];
#pragma unroll
  for (int i = 0; i < 8; ++i) {
    wf[i][0] = cat8(trread(lds + wa0 + i * 2048), trread(lds + wa0 + i * 2048 + 1024));
    wf[i][1] = cat8(trread(lds + wa1 + i * 2048), trread(lds + wa1 + i * 2048 + 1024));
  }
  if constexpr (MG) await(ws, FL_B + (rb >> 2) * 32 * 64, 32, c.epoch); else lds_bar();
  const char* abase = ws + WS_O + ((size_t)rt * 32 + kq * 8) * 1024;
  bf16x8 af[8];
  sfor<0, 8>([&](auto I) { constexpr int i = I.value; gload16<(i & 3) * 1024, MG>(af[i], abase + (i >> 2) * 4096, l * 16); });
  f32x16 acc[2] = {};
  sfor<0, 8>([&](auto I) {
    constexpr int i = I.value;
    asm volatile("s_waitcnt vmcnt(%1)" : "+v"(af[i]) : "i"(7 - i) : "memory");
    acc[0] = MFMA(wf[i][0], af[i], acc[0]);
    acc[1] = MFMA(wf[i][1], af[i], acc[1]);
    SBAR();
  });
  float val[8];
  quarter_reduce<0>(c, (LAS f32x4*)lds, acc, val);
  const u32x4 o = {cvtpk(val[0] + bf_lo(xr0[0]), val[1] + bf_hi(xr0[0])), cvtpk(val[2] + bf_lo(xr0[1]), val[3] + bf_hi(xr0[1])),
                   cvtpk(val[4] + bf_lo(xr1[0]), val[5] + bf_hi(xr1[0])), cvtpk(val[6] + bf_lo(xr1[1]), val[7] + bf_hi(xr1[1]))};
  hkeep = o;
  const __amdgpu_buffer_rsrc_t rw = make_rsrc(ws, WS_USED);
  st16<MG>(rw, WS_HF + (unsigned)((rt * 32 + kstep) * 64 + l) * 16, __builtin_bit_cast(bf16x8, o));
  f32x2 sm = {0.f, 0.f}, sq = {0.f, 0.f};
#pragma unroll
  for (int i = 0; i < 4; ++i) { const f32x2 v = unpk(o[i]); sm += v; sq = __builtin_elementwise_fma(v, v, sq); }
  LAS f32x2* tst = (LAS f32x2*)(lds + 0);
  tst[((m * 4 + kq) * 2 + hh) * 32 + (l & 31)] = f32x2{sm[0] + sm[1], sq[0] + sq[1]};
  lds_bar();
  if (w < 2 && l < 32) {
    f32x2 t8[8];
#pragma unroll
    for (int i = 0; i < 8; ++i) t8[i] = tst[(w * 8 + i) * 32 + l];
    const f32x2 t = ((t8[0] + t8[1]) + (t8[2] + t8[3])) + ((t8[4] + t8[5]) + (t8[6] + t8[7]));
    st8<MG>(rw, WS_STAT + (unsigned)(((2 * rb + w) * 8 + cb) * 32 + l) * 8, __builtin_bit_cast(u32x2, t));
  }
  if constexpr (MG) {
    publish(ws, FL_C + ((rb >> 1) * 16 + (rb & 1) * 8 + cb) * 64, c.epoch);
    if (b < 224) prefetch_w1<7>(p, c, 96, 128);
  }
}

constexpr int D_WIN = 6;
template <bool MG> DI void phaseD(const Params& p, const Ctx& c) {
  char* ws = p.ws;
  LAS char* lds = c.lds;
  const int l = c.l, w = c.w, hh = c.hh, b = c.b;
  const int rb = 2 * (b & 7) + ((b >> 3) & 1), cb = b >> 4;
  const int m = w & 3, nh = w >> 2;
  const int rt = 4 * rb + m;
  LAS float* tcs = (LAS float*)(lds + 49152);
  LAS float* tb = tcs + 128;
  if (c.tid < 128) tcs[c.tid] = 0.f;
  const bf16x8 bv = *(const GAS bf16x8*)((const char*)p.b1 + cb * 256 + (l & 15) * 16);
  if constexpr (!MG) { prefetch_w1<8>(p, c, 0, 128); wait_bar<0>(); }
  const int par = m >> 1;
  const int q4 = (l & 15) >> 2, p4 = l & 3, blk = (l >> 4) & 1;
  const int wl = nh * 8192 + (4 * hh + q4) * 128 + (((4 * (m & 1) + 2 * blk + (p4 >> 1)) ^ (((q4 >> 1) & 1) << 2)) * 16) + (p4 & 1) * 8;
  const int wa_e = wl + par * 2048, wa_o = wl + (1 - par) * 2048;
  bf16x8 wf[3][2];
  auto load_w = [&](auto I) {
    constexpr int i = I.value;
    constexpr int o = (int)w1_stage_base(i >> 2) + (i & 2) * 2048;
    const int wa = (i & 1) ? wa_o : wa_e;
    wf[i % 3][0] = cat8(trread(lds + wa + o), trread(lds + wa + o + 1024));
    wf[i % 3][1] = cat8(trread(lds + (wa ^ 64) + o), trread(lds + (wa ^ 64) + o + 1024));
  };
  load_w(IC<0>{}); load_w(IC<1>{});
  if constexpr (MG) await(ws, FL_C + rb * 16 * 64, 16, c.epoch); else lds_bar();
  const char* abase = ws + WS_HF + (size_t)rt * 32 * 1024;
  const unsigned aoff0 = l * 16 + par * 1024, aoff1 = l * 16 + (1 - par) * 1024;
  bf16x8 af[32];
  auto load_a = [&](auto I) { constexpr int i = I.value;
    if constexpr (i & 1) gload16<((i >> 1) & 1) * 2048, MG>(af[i], abase + (i >> 2) * 4096, aoff1);
    else gload16<((i >> 1) & 1) * 2048, MG>(af[i], abase + (i >> 2) * 4096, aoff0); };
  sfor<0, 4>(load_a);
  u32x2 st[4];
  sfor<0, 4>([&](auto I) { constexpr int i = I.value; gload8<i * 256, MG>(st[i], ws + WS_STAT + (size_t)rt * 2048, (l & 31) * 8 + hh * 1024); });
  sfor<4, D_WIN>(load_a);
  f32x16 acc[2] = {};
  f32x4 cs = {};
  const bf16x8 sel = rowsum_sel(l);
  sfor<0, 32>([&](auto I) {
    constexpr int i = I.value;
    if constexpr (i + D_WIN < 32) load_a(IC<i + D_WIN>{});
    constexpr int issued = i + D_WIN < 32 ? i + D_WIN + 1 : 32;
    asm volatile("s_waitcnt vmcnt(%1)" : "+v"(af[i]) : "i"(issued - i - 1 + (i < 4 ? 4 : 0)) : "memory");
    if constexpr (i == 20) asm volatile("s_barrier" ::: "memory");
    acc[0] = MFMA(wf[i % 3][0], af[i], acc[0]);
    acc[1] = MFMA(wf[i % 3][1], af[i], acc[1]);
    if constexpr ((i & 1) == 0) cs = MFMA16(wf[i % 3][0], sel, cs);
    if constexpr (i + 2 < 32) { SBAR(); load_w(IC<i + 2>{}); }
    SBAR();
  });
  if ((l & 15) < 2) {
    LAS float* d = tcs + nh * 64 + (m & 1) * 32 + 16 * (l & 15) + 4 * (l >> 4);
#pragma unroll
    for (int j = 0; j < 4; ++j) __hip_atomic_fetch_add(d + j, cs[j], __ATOMIC_RELAXED, __HIP_MEMORY_SCOPE_WORKGROUP);
  }
  if (w == 0 && l < 16) { const u32x4 u = __builtin_bit_cast(u32x4, bv);
    *(LAS f32x4*)(tb + l * 8) = f32x4{bf_lo(u[0]), bf_hi(u[0]), bf_lo(u[1]), bf_hi(u[1])}; *(LAS f32x4*)(tb + l * 8 + 4) = f32x4{bf_lo(u[2]), bf_hi(u[2]), bf_lo(u[3]), bf_hi(u[3])}; }
  asm volatile("" : "+v"(st[0]), "+v"(st[1]), "+v"(st[2]), "+v"(st[3]));
  const f32x2 ss = __builtin_bit_cast(f32x2, st[0]) + __builtin_bit_cast(f32x2, st[1]) + __builtin_bit_cast(f32x2, st[2]) + __builtin_bit_cast(f32x2, st[3]);
  const float sm = sumx32(ss[0]), sq = sumx32(ss[1]);
  const float mean = sm * (1.f / D), rstd = __builtin_amdgcn_rsqf(fmaxf(sq * (1.f / D) - mean * mean, 0.f) + LN_EPS);
  const float nmr = -mean * rstd;
  lds_bar();
  const __amdgpu_buffer_rsrc_t rw = make_rsrc(ws, WS_USED);
#pragma unroll
  for (int t = 0; t < 2; ++t) {
    const int ct = 2 * nh + ((m & 1) ^ t);
    f32x4 cv[4], bb[4];
#pragma unroll
    for (int g = 0; g < 4; ++g) { cv[g] = *(const LAS f32x4*)(tcs + 32 * ct + 8 * g + 4 * hh); bb[g] = *(const LAS f32x4*)(tb + 32 * ct + 8 * g + 4 * hh); }
#pragma unroll
    for (int s = 0; s < 2; ++s) {
      unsigned pk[4];
#pragma unroll
      for (int j = 0; j < 4; ++j) {
        const int r = 8 * s + 2 * j;
        const f32x2 a = {acc[t][r], acc[t][r + 1]}, c2 = {cv[r >> 2][r & 3], cv[r >> 2][(r & 3) + 1]}, b2 = {bb[r >> 2][r & 3], bb[r >> 2][(r & 3) + 1]};
        const f32x2 y = __builtin_elementwise_fma(a, f32x2{rstd, rstd}, __builtin_elementwise_fma(c2, f32x2{nmr, nmr}, b2));
        const f32x2 z = y * LOG2E;
        const f32x2 u = {__builtin_amdgcn_exp2f(z[0]), __builtin_amdgcn_exp2f(z[1])};
        const f32x2 d = __builtin_elementwise_fma(u, u + 2.f, f32x2{2.f, 2.f});
        const f32x2 r2 = {__builtin_amdgcn_rcpf(d[0]), __builtin_amdgcn_rcpf(d[1])};
        const f32x2 th = __builtin_elementwise_fma(r2, f32x2{-2.f, -2.f}, f32x2{1.f, 1.f});
        const f32x2 o = y * th;
        pk[j] = cvtpk(o[0], o[1]);
      }
      st16<MG>(rw, WS_ACT + (unsigned)((rt * 112 + cb * 8 + ct * 2 + s) * 64 + l) * 16, __builtin_bit_cast(bf16x8, u32x4{pk[0], pk[1], pk[2], pk[3]}));
      if constexpr (MG) {
        const int q = 2 * t + s;
        prefetch_w2<8>(p, c, 24 * q, 24 * q + 24);
      }
    }
  }
  if constexpr (MG) publish(ws, FL_D + (rb * 16 + cb) * 64, c.epoch);
}

constexpr int E_NSLOT = 10, E_NST = 14, E_WIN = 6, E_PRE = 8;
struct ESched {
  int posA[28], posW[E_NST], n_top[E_NST], n_mid[E_NST];
  constexpr ESched() : posA(), posW(), n_top(), n_mid() {
    int n = 0;
    for (int i = 0; i < E_WIN; ++i) posA[i] = n++;
    for (int st = 0; st < E_NST; ++st) {
      n_top[st] = n;
      if (E_PRE + st < E_NST) { n += 2; posW[E_PRE + st] = n - 1; }
      for (int j = 0; j < 2; ++j) if (E_WIN + 2 * st + j < 28) posA[E_WIN + 2 * st + j] = n++;
      n_mid[st] = n;
    }
  }
};
template <bool MG> DI void phaseE(const Params& p, const Ctx& c, u32x4 hkeep) {
  constexpr ESched sch{};
  char* ws = p.ws;
  LAS char* lds = c.lds;
  const int l = c.l, w = c.w, hh = c.hh, b = c.b;
  const int rb = (b & 7) * 4 + ((b >> 3) & 3), cb = b >> 5;
  const int m = w & 1, kq = w >> 1;
  const int rt = 2 * rb + m, kstep = 4 * cb + kq;
  const bool early = MG && b < 224;
  if (early) prefetch_w2<7>(p, c, 16 * 6, 16 * 8); else prefetch_w2<7>(p, c, 0, 16 * E_PRE);
  bf16x8 resv = __builtin_bit_cast(bf16x8, hkeep);
  if constexpr (!MG) resv = *(const GAS bf16x8*)(ws + WS_HF + (size_t)(rt * 32 + kstep) * 1024 + l * 16);
  const int q4 = (l & 15) >> 2, p4 = l & 3, blk = (l >> 4) & 1;
  const int wa0 = kq * 4096 + (4 * hh + q4) * 128 + (((2 * blk + (p4 >> 1)) ^ (((q4 >> 1) & 1) << 2)) * 16) + (p4 & 1) * 8;
  const int wa1 = wa0 ^ 64;
  bf16x8 wf[3][2][2];
  auto load_w = [&](auto ST) {
    constexpr int st = ST.value;
    const LAS char* sl = lds + ((st + 4) % E_NSLOT) * 16384;
#pragma unroll
    for (int i = 0; i < 2; ++i) {
      wf[st % 3][i][0] = cat8(trread(sl + wa0 + i * 2048), trread(sl + wa0 + i * 2048 + 1024));
      wf[st % 3][i][1] = cat8(trread(sl + wa1 + i * 2048), trread(sl + wa1 + i * 2048 + 1024));
    }
  };
  if (!early) wait_bar<0>();
  load_w(IC<0>{}); load_w(IC<1>{});
  if constexpr (MG) await(ws, FL_D + (rb >> 1) * 16 * 64, 14, c.epoch);
  const char* abase = ws + WS_ACT + ((size_t)rt * 112 + kq * 28) * 1024;
  bf16x8 af[28];
  auto load_a = [&](auto I) { constexpr int i = I.value; gload16<(i & 3) * 1024, MG>(af[i], abase + (i >> 2) * 4096, l * 16); };
  sfor<0, E_WIN>(load_a);
  f32x16 acc[2] = {};
  sfor<0, E_NST>([&](auto ST) {
    constexpr int st = ST.value;
    if constexpr (st == 2) asm volatile("s_waitcnt lgkmcnt(0)\n\ts_barrier" ::: "memory");
    if constexpr (st == 6 || st == 8 || st == 10) asm volatile("s_waitcnt vmcnt(%0) lgkmcnt(0)\n\ts_barrier" ::"i"(sch.n_top[st] - sch.posW[st + 3] - 1) : "memory");
    if constexpr (E_PRE + st < E_NST) prefetch_w2<8>(p, c, 16 * (st + E_PRE), 16 * (st + E_PRE + 1));
    if constexpr (E_WIN + 2 * st < 28) load_a(IC<E_WIN + 2 * st>{});
    if constexpr (E_WIN + 2 * st + 1 < 28) load_a(IC<E_WIN + 2 * st + 1>{});
    asm volatile("s_waitcnt vmcnt(%2)" : "+v"(af[2 * st]), "+v"(af[2 * st + 1]) : "i"(sch.n_mid[st] - sch.posA[2 * st + 1] - 1) : "memory");
    acc[0] = MFMA(wf[st % 3][0][0], af[2 * st], acc[0]);
    acc[1] = MFMA(wf[st % 3][0][1], af[2 * st], acc[1]);
    if constexpr (st + 2 < E_NST) { SBAR(); load_w(IC<st + 2>{}); SBAR(); }
    acc[0] = MFMA(wf[st % 3][1][0], af[2 * st + 1], acc[0]);
    acc[1] = MFMA(wf[st % 3][1][1], af[2 * st + 1], acc[1]);
    SBAR();
  });
  { asm volatile("" : "+v"(resv));
    const u32x4 ru = __builtin_bit_cast(u32x4, resv);
#pragma unroll
    for (int k = 0; k < 4; ++k) { const float on = kq == k ? 1.f : 0.f;
#pragma unroll
      for (int j = 0; j < 4; ++j) { acc[k >> 1][8 * (k & 1) + 2 * j] += on * bf_lo(ru[j]); acc[k >> 1][8 * (k & 1) + 2 * j + 1] += on * bf_hi(ru[j]); } } }
  constexpr int PS = 272, PQ = 64 * PS;
  LAS char* xa = lds;
#pragma unroll
  for (int ct = 0; ct < 2; ++ct)
#pragma unroll
    for (int g = 0; g < 4; ++g)
      *(LAS f32x4*)(xa + kq * PQ + (32 * m + (l & 31)) * PS + (32 * ct + 8 * g + 4 * hh) * 4) = f32x4{acc[ct][4 * g], acc[ct][4 * g + 1], acc[ct][4 * g + 2], acc[ct][4 * g + 3]};
  lds_bar();
  { const int row = c.tid >> 3, c8 = c.tid & 7;
    f32x4 t0[4], t1[4];
#pragma unroll
    for (int q = 0; q < 4; ++q) { t0[q] = *(const LAS f32x4*)(xa + q * PQ + row * PS + c8 * 32); t1[q] = *(const LAS f32x4*)(xa + q * PQ + row * PS + c8 * 32 + 16); }
    const f32x4 s0 = (t0[0] + t0[1]) + (t0[2] + t0[3]), s1 = (t1[0] + t1[1]) + (t1[2] + t1[3]);
    const __amdgpu_buffer_rsrc_t ro = make_rsrc(p.out, S * D * 2);
    st16<false>(ro, (unsigned)(rb * 64 + row) * 1024u + cb * 128 + c8 * 16, pack8(s0[0], s0[1], s0[2], s0[3], s1[0], s1[1], s1[2], s1[3])); }
}

DI Ctx make_ctx(char* lds_g) {
  Ctx c;
  c.lds = (LAS char*)lds_g; c.lds0 = (unsigned)(uintptr_t)lds_g;
  c.tid = threadIdx.x; c.l = c.tid & 63; c.w = rfl(c.tid >> 6); c.hh = c.l >> 5; c.b = blockIdx.x; c.epoch = 0;
  return c;
}

__global__ __launch_bounds__(512, 2) void block_mega(Params p) {
  extern __shared__ __attribute__((aligned(16))) char lds_g[];
  Ctx c = make_ctx(lds_g);
  c.epoch = __hip_atomic_load((GAS unsigned*)(p.ws + WS_FLAG) + (threadIdx.x >> 12), __ATOMIC_RELAXED, __HIP_MEMORY_SCOPE_AGENT);
  phaseA<true>(p, c);
  phaseB<true>(p, c);
  u32x4 hkeep;
  phaseC<true>(p, c, hkeep);
  if (c.b < 224) phaseD<true>(p, c);
  phaseE<true>(p, c, hkeep);
  if (c.b == 0 && c.tid == 0) __hip_atomic_store((GAS unsigned*)(p.ws + WS_FLAG), c.epoch, __ATOMIC_RELAXED, __HIP_MEMORY_SCOPE_AGENT);
  asm volatile("mega_code_end:");
}

extern "C" void kernel_launch(void* const* d_in, const int* in_sizes, int n_in, void* d_out, int out_size, void* d_ws, size_t ws_size, hipStream_t stream) {
  Params p;
  p.x = (const u16*)d_in[0]; p.wqkv = (const u16*)d_in[1]; p.wo = (const u16*)d_in[2];
  p.w1 = (const u16*)d_in[4]; p.b1 = (const u16*)d_in[5]; p.w2 = (const u16*)d_in[6];
  p.ws = (char*)d_ws; p.out = (u16*)d_out;
  (void)hipFuncSetAttribute((const void*)block_mega, hipFuncAttributeMaxDynamicSharedMemorySize, LDS_BYTES);
  block_mega<<<256, 512, LDS_BYTES, stream>>>(p);
}
```

```cpp
#include <hip/hip_runtime.h>
#include <hip/hip_bf16.h>
#include <type_traits>

typedef unsigned short u16;
using bf16x8 = __attribute__((ext_vector_type(8))) short;
using s16x4 = __attribute__((ext_vector_type(4))) short;
using f32x16 = __attribute__((ext_vector_type(16))) float;
using f32x4 = __attribute__((ext_vector_type(4))) float;
using f32x2 = __attribute__((ext_vector_type(2))) float;
using u32x4 = __attribute__((ext_vector_type(4))) unsigned;
using u32x2 = __attribute__((ext_vector_type(2))) unsigned;
using bf16x2_t = __attribute__((ext_vector_type(2))) __bf16;
#define DI __device__ __forceinline__
#define LAS __attribute__((address_space(3)))
#define GAS __attribute__((address_space(1)))
#define MFMA(a, b, c) __builtin_amdgcn_mfma_f32_32x32x16_bf16((a), (b), (c), 0, 0, 0)
#define SBAR() __builtin_amdgcn_sched_barrier(0)

constexpr int S = 2048, D = 512, F = 1792;
constexpr float LN_EPS = 1e-5f;
constexpr float LOG2E = 1.4426950408889634f;
constexpr float QSCALE = 0.25f * LOG2E;
constexpr float BOUND_SAFE = 40.f;
constexpr int LDS_BYTES = 163840;

constexpr unsigned WS_FLAG = 0;
constexpr unsigned FL_A = 4096, FL_B = FL_A + 256 * 64, FL_C = FL_B + 256 * 64, FL_D = FL_C + 256 * 64;
constexpr unsigned WS_KMAX = 128u << 10, WS_STAT = 256u << 10;
constexpr unsigned WS_Q = 1u << 20, WS_KV = 3u << 20  , WS_O = 7u << 20, WS_HF = 9u << 20, WS_ACT = 11u << 20;
constexpr unsigned WS_USED = 19u << 20;

struct Params { const u16 *x, *wqkv, *wo, *w1, *b1, *w2; char* ws; u16* out; };

template <int N> using IC = std::integral_constant<int, N>;
template <int B, int E, class Fn> DI void sfor(Fn&& f) { if constexpr (B < E) { f(IC<B>{}); sfor<B + 1, E>(f); } }

DI int rfl(int v) { return __builtin_amdgcn_readfirstlane(v); }
DI unsigned cvtpk(float lo, float hi) { const f32x2 v = {lo, hi}; return __builtin_bit_cast(unsigned, __builtin_convertvector(v, bf16x2_t)); }
DI float bf_lo(unsigned u) { return __uint_as_float(u << 16); }
DI float bf_hi(unsigned u) { return __uint_as_float(u & 0xffff0000u); }
constexpr unsigned ONES2 = 0x3F803F80u;
#define MFMA16(a, b, c) __builtin_amdgcn_mfma_f32_16x16x32_bf16((a), (b), (c), 0, 0, 0)
DI f32x2 unpk(unsigned u) { return f32x2{__uint_as_float(u << 16), __uint_as_float(u & 0xffff0000u)}; }
DI bf16x8 rowsum_sel(int l) { const unsigned on = ((l & 15) == ((l >> 4) & 1)) ? ONES2 : 0u; return __builtin_bit_cast(bf16x8, u32x4{on, on, on, on}); }
DI void dma16(const void* sbase, unsigned voff, unsigned lds) {
  asm volatile("s_mov_b32 m0, %2\n\ts_nop 0\n\tglobal_load_lds_dwordx4 %0, %1" ::"v"(voff), "s"(sbase), "s"(lds) : "memory");
}
DI void dma16_nt(const void* sbase, unsigned voff, unsigned lds) {
  asm volatile("s_mov_b32 m0, %2\n\ts_nop 0\n\tglobal_load_lds_dwordx4 %0, %1 nt" ::"v"(voff), "s"(sbase), "s"(lds) : "memory");
}
DI void dma16_sc1(const void* sbase, unsigned voff, unsigned lds) {
  asm volatile("s_mov_b32 m0, %2\n\ts_nop 0\n\tglobal_load_lds_dwordx4 %0, %1 sc1" ::"v"(voff), "s"(sbase), "s"(lds) : "memory");
}
template <int IMM, bool SC1> DI void gload16(bf16x8& dst, const void* sbase, unsigned voff) {
  if constexpr (SC1) asm volatile("global_load_dwordx4 %0, %1, %2 offset:%3 sc1" : "=v"(dst) : "v"(voff), "s"(sbase), "i"(IMM) : "memory");
  else asm volatile("global_load_dwordx4 %0, %1, %2 offset:%3" : "=v"(dst) : "v"(voff), "s"(sbase), "i"(IMM) : "memory");
}
template <int IMM, bool SC1 = false> DI void gload8(u32x2& dst, const void* sbase, unsigned voff) {
  if constexpr (SC1) asm volatile("global_load_dwordx2 %0, %1, %2 offset:%3 sc1" : "=v"(dst) : "v"(voff), "s"(sbase), "i"(IMM) : "memory");
  else asm volatile("global_load_dwordx2 %0, %1, %2 offset:%3" : "=v"(dst) : "v"(voff), "s"(sbase), "i"(IMM) : "memory");
}
template <bool SC1> DI void gload4(float& dst, const void* sbase, unsigned voff) {
  if constexpr (SC1) asm volatile("global_load_dword %0, %1, %2 sc1" : "=v"(dst) : "v"(voff), "s"(sbase) : "memory");
  else asm volatile("global_load_dword %0, %1, %2" : "=v"(dst) : "v"(voff), "s"(sbase) : "memory");
}
typedef __attribute__((ext_vector_type(4))) unsigned v4u_;
DI __amdgpu_buffer_rsrc_t make_rsrc(void* p, unsigned bytes) { return __builtin_amdgcn_make_buffer_rsrc(p, 0, bytes, 0x00020000); }
template <bool SC1> DI void st16(__amdgpu_buffer_rsrc_t r, unsigned off, bf16x8 v) { __builtin_amdgcn_raw_buffer_store_b128(__builtin_bit_cast(v4u_, v), r, off, 0, SC1 ? 16 : 0); }
template <bool SC1> DI void st8(__amdgpu_buffer_rsrc_t r, unsigned off, u32x2 v) { __builtin_amdgcn_raw_buffer_store_b64(v, r, off, 0, SC1 ? 16 : 0); }
template <bool SC1> DI void st4(__amdgpu_buffer_rsrc_t r, unsigned off, unsigned v) { __builtin_amdgcn_raw_buffer_store_b32(v, r, off, 0, SC1 ? 16 : 0); }

template <int N> DI void wait_bar() { asm volatile("s_waitcnt vmcnt(%0) lgkmcnt(0)\n\ts_barrier" ::"i"(N) : "memory"); }
DI void lds_bar() { asm volatile("s_waitcnt lgkmcnt(0)\n\ts_barrier" ::: "memory"); }
DI s16x4 trread(const LAS char* p) {
  typedef short v4i16_t __attribute__((ext_vector_type(4)));
  return __builtin_bit_cast(s16x4, __builtin_amdgcn_ds_read_tr16_b64_v4i16((LAS v4i16_t*)p));
}
DI bf16x8 cat8(s16x4 a, s16x4 b) { return __builtin_shufflevector(a, b, 0, 1, 2, 3, 4, 5, 6, 7); }
DI bf16x8 pack8(float a0, float a1, float a2, float a3, float a4, float a5, float a6, float a7) {
  u32x4 w = {cvtpk(a0, a1), cvtpk(a2, a3), cvtpk(a4, a5), cvtpk(a6, a7)};
  return __builtin_bit_cast(bf16x8, w);
}
template <int CTRL> DI float dpp(float v) { return __uint_as_float(__builtin_amdgcn_update_dpp(0u, __float_as_uint(v), CTRL, 0xf, 0xf, true)); }
DI float sum8(float v) { v += dpp<0xB1>(v); v += dpp<0x4E>(v); v += dpp<0x141>(v); return v; }
DI float sumx32(float v) { auto r = __builtin_amdgcn_permlane32_swap(__float_as_uint(v), __float_as_uint(v), false, false); return __uint_as_float(r[0]) + __uint_as_float(r[1]); }
DI float maxx32(float v) { auto r = __builtin_amdgcn_permlane32_swap(__float_as_uint(v), __float_as_uint(v), false, false); return fmaxf(__uint_as_float(r[0]), __uint_as_float(r[1])); }
DI float maxx16(float v) { auto r = __builtin_amdgcn_permlane16_swap(__float_as_uint(v), __float_as_uint(v), false, false); return fmaxf(__uint_as_float(r[0]), __uint_as_float(r[1])); }
DI float max16(float v) { v = fmaxf(v, dpp<0xB1>(v)); v = fmaxf(v, dpp<0x4E>(v)); v = fmaxf(v, dpp<0x141>(v)); v = fmaxf(v, dpp<0x140>(v)); return v; }

DI void publish(char* ws, unsigned flag_off, unsigned epoch) {
  asm volatile("s_waitcnt vmcnt(0) lgkmcnt(0)\n\ts_barrier" ::: "memory");
  if (threadIdx.x == 0) __hip_atomic_store((GAS unsigned*)(ws + flag_off), epoch, __ATOMIC_RELAXED, __HIP_MEMORY_SCOPE_AGENT);
}
DI void await(char* ws, unsigned flag_off, int n, unsigned epoch) {
  if (threadIdx.x >= 448) {
    const int l = threadIdx.x & 63;
    GAS unsigned* f = (GAS unsigned*)(ws + flag_off + (l < n ? l : 0) * 64);
    for (unsigned spins = 0; spins < (1u << 20); ++spins) {
      const unsigned v = __hip_atomic_load(f, __ATOMIC_RELAXED, __HIP_MEMORY_SCOPE_AGENT);
      if (__all(v == epoch)) break;
      __builtin_amdgcn_s_sleep(1);
    }
  }
  asm volatile("s_waitcnt lgkmcnt(0)\n\ts_barrier" ::: "memory");
}

struct Ctx { LAS char* lds; unsigned lds0; int tid, l, w, hh, b; unsigned epoch; };

template <bool MG> DI void phaseA(const Params& p, Ctx& c) {
  char* ws = p.ws;
  LAS char* lds = c.lds;
  const int l = c.l, w = c.w, hh = c.hh, b = c.b;
  const int rb = 8 * ((b & 7) >> 1) + ((b >> 3) & 7), hg = 4 * (b & 1) + (b >> 6);
  bf16x8 ft[3] = {};
  if constexpr (MG) {
    unsigned long long pc; unsigned len;
    asm volatile("s_getpc_b64 %0\n.Lmega_here%=:\n\ts_mov_b32 %1, mega_code_end-.Lmega_here%=" : "=s"(pc), "=s"(len));
    const unsigned j = (unsigned)(b >> 3);
    const unsigned oc = j * 1024u + (w & 1) * 32768u + l * 16u;
    ft[2] = *(const GAS bf16x8*)((const char*)pc + (w < 2 && oc + 16 <= len ? oc : 0u));
    ft[0] = *(const GAS bf16x8*)((const char*)p.x + (size_t)b * 8192 + w * 1024 + l * 16);
    ft[1] = *(const GAS bf16x8*)((const char*)p.wqkv + (size_t)b * 6144 + (w < 6 ? w : 5) * 1024 + l * 16);
  }
  const int q4 = (l & 15) >> 2, p4 = l & 3, blk = (l >> 4) & 1;
  LAS float* tmean = (LAS float*)(lds + 0);
  LAS float* trstd = tmean + 64;
  LAS float* tcs = tmean + 128;
  f32x16 acc[3] = {};
  const int m = w & 1, g = (w >> 1) & 1;
  if (w < 4) {
    const bf16x8 sel = rowsum_sel(l);
    const int swz = (l >> 1) & 7;
    const int xrow = (32 * m + (l & 31)) * 128;
    const int wa = 8192 + (8 * hh + q4) * 128 + (((4 * g + 2 * blk + (p4 >> 1)) ^ (((q4 >> 1) & 1) << 2)) * 16) + (p4 & 1) * 8;
    int xo[4], wo_[2];
#pragma unroll
    for (int i = 0; i < 4; ++i) xo[i] = xrow + (((2 * (i ^ m) + hh) ^ swz) * 16);
    wo_[0] = wa + m * 2048; wo_[1] = wa + (1 - m) * 2048;
    bf16x8 xf[2][4], wf[2][4][3];
    f32x4 cs[3] = {};
    auto ld_w = [&](auto SET, const LAS char* sl, int i, int sgm) { constexpr int q = SET.value; const int o = wo_[i & 1] + (i & 2) * 2048 + sgm * 8192;
      wf[q][i][sgm] = cat8(trread(sl + o), trread(sl + o + 512)); };
    lds_bar();
#pragma unroll
    for (int i = 0; i < 4; ++i) { xf[0][i] = *(const LAS bf16x8*)(lds + xo[i]); for (int sgm = 0; sgm < 3; ++sgm) ld_w(IC<0>{}, lds, i, sgm); }
    sfor<0, 8>([&](auto ST) {
      constexpr int st = ST.value, q = st & 1, qn = (st + 1) & 1;
      if constexpr (st + 1 < 8) lds_bar(); else asm volatile("s_waitcnt lgkmcnt(0)" ::: "memory");
      asm volatile("" : "+v"(xf[q][0]), "+v"(xf[q][1]), "+v"(xf[q][2]), "+v"(xf[q][3]), "+v"(wf[q][0][0]), "+v"(wf[q][0][1]), "+v"(wf[q][0][2]), "+v"(wf[q][1][0]), "+v"(wf[q][1][1]), "+v"(wf[q][1][2]),
                   "+v"(wf[q][2][0]), "+v"(wf[q][2][1]), "+v"(wf[q][2][2]), "+v"(wf[q][3][0]), "+v"(wf[q][3][1]), "+v"(wf[q][3][2]));
      const LAS char* sn = lds + ((st + 1) % 5) * 32768;
#pragma unroll
      for (int i = 0; i < 4; ++i) {
        acc[0] = MFMA(wf[q][i][0], xf[q][i], acc[0]);
        SBAR();
        if constexpr (st + 1 < 8) { ld_w(IC<qn>{}, sn, i, 0); xf[qn][i] = *(const LAS bf16x8*)(sn + xo[i]); }
        SBAR();
        acc[1] = MFMA(wf[q][i][1], xf[q][i], acc[1]);
        SBAR();
        if constexpr (st + 1 < 8) ld_w(IC<qn>{}, sn, i, 1);
        SBAR();
        acc[2] = MFMA(xf[q][i], wf[q][i][2], acc[2]);
        SBAR();
        if constexpr (st + 1 < 8) ld_w(IC<qn>{}, sn, i, 2);
        if ((i & 1) == 0) { cs[0] = MFMA16(wf[q][i][0], sel, cs[0]); cs[1] = MFMA16(wf[q][i][1], sel, cs[1]); cs[2] = MFMA16(wf[q][i][2], sel, cs[2]); }
        SBAR();
      }
    });
    if ((l & 15) < 2) {
#pragma unroll
      for (int t = 0; t < 3; ++t) *(LAS f32x4*)(tcs + (w * 3 + t) * 32 + 16 * (l & 15) + 4 * (l >> 4)) = cs[t];
    }
  } else {
    const int h = w - 4;
    const unsigned xo0 = (unsigned)(64 * rb + 16 * h + (l >> 3)) * 1024u + (unsigned)(((l & 7) ^ ((l >> 4) & 7)) * 16);
    const unsigned xo1 = (unsigned)(64 * rb + 16 * h + 8 + (l >> 3)) * 1024u + (unsigned)(((l & 7) ^ ((4 + (l >> 4)) & 7)) * 16);
    const unsigned wof = (unsigned)(16 * h + (l >> 3)) * 3072u + (unsigned)hg * 128u + (unsigned)(((l & 7) ^ (((l >> 4) & 1) << 2)) * 16);
    auto issue = [&](int st) {
      const unsigned dst = c.lds0 + (st % 5) * 32768 + h * 2048;
      const char* wsrc = (const char*)p.wqkv + (size_t)st * 64 * 3072;
      dma16_nt((const char*)p.x + st * 128, xo0, dst);
      dma16_nt((const char*)p.x + st * 128, xo1, dst + 1024);
#pragma unroll
      for (int sgm = 0; sgm < 3; ++sgm) { dma16_nt(wsrc + sgm * 1024, wof, dst + 8192 + sgm * 8192); dma16_nt(wsrc + sgm * 1024 + 8 * 3072, wof, dst + 8192 + sgm * 8192 + 1024); }
    };
    issue(0); issue(1); issue(2); issue(3); issue(4);
    f32x2 rs[2] = {}, rq[2] = {};
    u32x4 sv[2][2];
    wait_bar<32>();
    sv[0][0] = *(const LAS u32x4*)(lds + h * 2048 + l * 16); sv[0][1] = *(const LAS u32x4*)(lds + h * 2048 + 1024 + l * 16);
    sfor<0, 8>([&](auto ST) {
      constexpr int st = ST.value, q = st & 1, qn = (st + 1) & 1;
      if constexpr (st + 1 < 8) {
        constexpr int last = 4 + st > 7 ? 7 : 4 + st;
        wait_bar<8 * (last - (st + 1))>();
        if constexpr (st + 5 < 8) issue(st + 5);
        const LAS char* sn = lds + ((st + 1) % 5) * 32768 + h * 2048 + l * 16;
        sv[qn][0] = *(const LAS u32x4*)sn; sv[qn][1] = *(const LAS u32x4*)(sn + 1024);
      }
#pragma unroll
      for (int r = 0; r < 2; ++r)
#pragma unroll
        for (int i = 0; i < 4; ++i) { const f32x2 v = unpk(sv[q][r][i]); rs[r] += v; rq[r] = __builtin_elementwise_fma(v, v, rq[r]); }
      asm volatile("" : "+v"(rs[0]), "+v"(rq[0]), "+v"(rs[1]), "+v"(rq[1]));
    });
#pragma unroll
    for (int r = 0; r < 2; ++r) {
      const float s1 = sum8(rs[r][0] + rs[r][1]), s2 = sum8(rq[r][0] + rq[r][1]);
      if ((l & 7) == 0) { const float mean = s1 * (1.f / D); const int row = 16 * h + 8 * r + (l >> 3);
        tmean[row] = mean; trstd[row] = __builtin_amdgcn_rsqf(fmaxf(s2 * (1.f / D) - mean * mean, 0.f) + LN_EPS); }
    }
  }
  if constexpr (MG) { asm volatile("" : "+v"(c.epoch), "+v"(ft[0]), "+v"(ft[1]), "+v"(ft[2])); c.epoch += 1u; }
  lds_bar();
  const __amdgpu_buffer_rsrc_t rw = make_rsrc(ws, WS_USED);
  if (w < 4) {
    const int tile = 2 * rb + m;
    const float mean_l = tmean[32 * m + (l & 31)], rstd_l = trstd[32 * m + (l & 31)];
    f32x4 cv[2][4], mm[4], rr[4];
#pragma unroll
    for (int gg = 0; gg < 4; ++gg) {
#pragma unroll
      for (int t = 0; t < 2; ++t) cv[t][gg] = *(const LAS f32x4*)(tcs + (w * 3 + t) * 32 + 8 * gg + 4 * hh) + *(const LAS f32x4*)(tcs + ((w ^ 1) * 3 + t) * 32 + 8 * gg + 4 * hh);
      mm[gg] = *(const LAS f32x4*)(tmean + 32 * m + 8 * gg + 4 * hh); rr[gg] = *(const LAS f32x4*)(trstd + 32 * m + 8 * gg + 4 * hh);
    }
    const int n = l & 31;
    const float ncs = -(tcs[(w * 3 + 2) * 32 + n] + tcs[((w ^ 1) * 3 + 2) * 32 + n]);
#pragma unroll
    for (int s = 0; s < 2; ++s) {
      f32x2 v[4];
#pragma unroll
      for (int j = 0; j < 4; ++j) { const int r = 8 * s + 2 * j; const f32x2 a = {acc[2][r], acc[2][r + 1]}, m2 = {mm[r >> 2][r & 3], mm[r >> 2][(r & 3) + 1]}, r2 = {rr[r >> 2][r & 3], rr[r >> 2][(r & 3) + 1]};
        v[j] = __builtin_elementwise_fma(m2, f32x2{ncs, ncs}, a) * r2; }
      st16<MG>(rw, WS_KV + (unsigned)((4 * hg + 2 * g + (n >> 4)) * 64 + tile) * 2048 + 1024 + s * 512 + hh * 256 + (n & 15) * 16,
               pack8(v[0][0], v[0][1], v[1][0], v[1][1], v[2][0], v[2][1], v[3][0], v[3][1]));
    }
#pragma unroll
    for (int t = 1; t >= 0; --t) {
      const float sc = t == 0 ? rstd_l * QSCALE : rstd_l, nm = -mean_l * sc;
#pragma unroll
      for (int s = 0; s < 2; ++s) {
        f32x2 v[4];
#pragma unroll
        for (int j = 0; j < 4; ++j) { const int r = 8 * s + 2 * j; const f32x2 a = {acc[t][r], acc[t][r + 1]}, cc = {cv[t][r >> 2][r & 3], cv[t][r >> 2][(r & 3) + 1]};
          v[j] = __builtin_elementwise_fma(a, f32x2{sc, sc}, cc * nm); }
        const int head = 4 * hg + 2 * g + s;
        st16<MG>(rw, (t == 0 ? WS_Q + (unsigned)(head * 64 + tile) * 1024 : WS_KV + (unsigned)(head * 64 + tile) * 2048) + l * 16, pack8(v[0][0], v[0][1], v[1][0], v[1][1], v[2][0], v[2][1], v[3][0], v[3][1]));
        if (t == 1) {
          f32x2 n2 = v[0] * v[0]; n2 = __builtin_elementwise_fma(v[1], v[1], n2); n2 = __builtin_elementwise_fma(v[2], v[2], n2); n2 = __builtin_elementwise_fma(v[3], v[3], n2);
          const float mx = maxx16(max16(sumx32(n2[0] + n2[1])));
          if (l == 0) st4<MG>(rw, WS_KMAX + (unsigned)(head * 64 + tile) * 4, __float_as_uint(mx));
        }
      }
    }
  }
  if constexpr (MG) publish(ws, FL_A + (hg * 32 + rb) * 64, c.epoch);
}

DI unsigned wpiece_lane(int l, unsigned row_bytes) { return (unsigned)(l >> 3) * row_bytes + (unsigned)(((l & 7) ^ (((l >> 4) & 1) << 2)) * 16); }
template <int NW, int W0 = 0> DI void prefetch_wo(const Params& p, const Ctx& c) {
  if (c.w < W0 || c.w >= W0 + NW) return;
  const unsigned off = wpiece_lane(c.l, 1024) + (unsigned)(c.b >> 5) * 128u;
  for (int pc = c.w - W0; pc < 64; pc += NW) dma16((const char*)p.wo + (size_t)(8 * pc) * 1024, off, c.lds0 + pc * 1024);
}
DI constexpr unsigned w1_stage_base(int j) { return j < 6 ? 65536 + j * 16384 : (j - 6) * 16384; }
template <int NW, int W0 = 0> DI void prefetch_w1(const Params& p, const Ctx& c, int p0, int p1) {
  if (c.w < W0 || c.w >= W0 + NW) return;
  const unsigned off = wpiece_lane(c.l, F * 2) + (unsigned)(c.b >> 4) * 256u;
  for (int pc = p0 + c.w - W0; pc < p1; pc += NW) {
    const int j = pc >> 4, seg = (pc >> 3) & 1, r8 = pc & 7;
    dma16((const char*)p.w1 + (size_t)(64 * j + 8 * r8) * (F * 2) + seg * 128, off, c.lds0 + w1_stage_base(j) + seg * 8192 + r8 * 1024);
  }
}
template <int NW> DI void prefetch_w2(const Params& p, const Ctx& c, int p0, int p1) {
  if (c.w >= NW) return;
  const unsigned off = wpiece_lane(c.l, 1024) + (unsigned)(c.b >> 5) * 128u;
  for (int pc = p0 + c.w; pc < p1; pc += NW) {
    const int st = pc >> 4, kq = (pc >> 2) & 3, r8 = pc & 3;
    dma16((const char*)p.w2 + (size_t)(448 * kq + 32 * st + 8 * r8) * 1024, off, c.lds0 + ((st + 4) % 10) * 16384 + kq * 4096 + r8 * 1024);
  }
}

DI void warm_first(const Params& p, const Ctx& c) {
  const int h = c.w - 4;
  const unsigned off1 = (unsigned)c.b * 7168u + (unsigned)h * 2048u + (unsigned)c.l * 16u;
  dma16((const char*)p.w1, off1, c.lds0 + 139520);
  dma16((const char*)p.w2, off1, c.lds0 + 139520);
  if (h < 3) { dma16((const char*)p.w1 + 1024, off1, c.lds0 + 139520); dma16((const char*)p.w2 + 1024, off1, c.lds0 + 139520); }
}
DI void warm_w1_late(const Params& p, const Ctx& c) {
  const int h = c.w - 4;
  const unsigned j = (unsigned)(c.b >> 3);
  for (int pc = h; pc < 35; pc += 4)
    dma16((const char*)p.w1, (pc < 21 ? 128u * 3584u + j * 21504u + pc * 1024u : 384u * 3584u + j * 14336u + (pc - 21) * 1024u) + c.l * 16u, c.lds0 + 139520);
}
DI void warm_w2(const Params& p, const Ctx& c) {
  const int h = c.w - 4;
  const unsigned off = (unsigned)(c.b >> 3) * 57344u + (unsigned)h * 14336u + (unsigned)c.l * 16u;
#pragma unroll
  for (int i = 0; i < 14; ++i) dma16((const char*)p.w2 + i * 1024, off, c.lds0 + 139520);
}

constexpr int B_ONES = 131072;
template <bool MG> DI void phaseB(const Params& p, const Ctx& c) {
  char* ws = p.ws;
  LAS char* lds = c.lds;
  const int l = c.l, w = c.w, hh = c.hh, b = c.b, tid = c.tid;
  const int head = 4 * (b & 7) + ((b >> 3) & 3), qb = b >> 5;
  for (int i = tid; i < (4096 + 512) / 16; i += 512) { const unsigned v = (i & 31) == 0 ? ONES2 : 0u; ((LAS u32x4*)(lds + B_ONES))[i] = u32x4{v, v, v, v}; }
  if constexpr (MG) await(ws, FL_A + (head >> 2) * 32 * 64, 32, c.epoch);
  const unsigned l16 = l * 16;
  if (w >= 4) {
    const char* kvg = ws + WS_KV + (size_t)head * 131072;
    const int h = w - 4;
    auto piece = [&](int pc) { if constexpr (MG) dma16_sc1(kvg + pc * 1024, l16, c.lds0 + pc * 1024); else dma16(kvg + pc * 1024, l16, c.lds0 + pc * 1024); };
#pragma unroll
    for (int i = 0; i < 4; ++i) piece(4 * h + i);
#pragma unroll
    for (int i = 0; i < 12; ++i) piece(16 + 4 * i + h);
    wait_bar<12>();
#pragma unroll
    for (int i = 0; i < 16; ++i) piece(64 + 4 * i + h);
    wait_bar<24>();
    wait_bar<16>();
    if constexpr (MG) warm_first(p, c);
    wait_bar<0>();
    if constexpr (MG) { if (b < 224) prefetch_w1<4, 4>(p, c, 80, 96); }
    lds_bar();
    if constexpr (MG) { prefetch_wo<4, 4>(p, c); warm_w1_late(p, c); }
    lds_bar();
    if constexpr (MG) { if (b < 224) prefetch_w1<4, 4>(p, c, 0, 32); warm_w2(p, c); }
  } else {
    bf16x8 qa, qb_;
    float kmx;
    gload4<MG>(kmx, ws + WS_KMAX + head * 256, l * 4);
    gload16<0, MG>(qa, ws + WS_Q + (size_t)(head * 64 + qb * 8 + w) * 1024, l * 16);
    gload16<0, MG>(qb_, ws + WS_Q + (size_t)(head * 64 + qb * 8 + w + 4) * 1024, l * 16);
    asm volatile("s_waitcnt vmcnt(0)" : "+v"(qa), "+v"(qb_), "+v"(kmx)::"memory");
    kmx = maxx32(maxx16(max16(kmx)));
    auto qbound = [&](bf16x8 q) { const u32x4 u = __builtin_bit_cast(u32x4, q); f32x2 t = {0.f, 0.f}; for (int i = 0; i < 4; ++i) { const f32x2 v = unpk(u[i]); t = __builtin_elementwise_fma(v, v, t); }
      return __builtin_amdgcn_sqrtf(sumx32(t[0] + t[1]) * kmx) * 1.01f + 1e-6f; };
    const float bna = qbound(qa), bnb = qbound(qb_);
    const bool safe = __all(bna <= BOUND_SAFE && bnb <= BOUND_SAFE);
    f32x16 oa = {}, ob = {};
    const int lo5 = l & 31;
    const bool isv = lo5 < 16;
    const int va0 = isv ? 1024 + hh * 256 + lo5 * 16 : B_ONES + (lo5 == 16 ? 0 : (lo5 >= 20 && lo5 < 28) ? 64 : 16);
    lds_bar();
    if (safe) {
      f32x16 nga, ngb; for (int i = 0; i < 16; ++i) { nga[i] = -bna; ngb[i] = -bnb; }
      f32x16 s0, s1;
      bf16x8 pa, pb = {};
      bf16x8 kf, v0, v1 = {};
      unsigned ka = c.lds0 + l * 16, va = c.lds0 + va0;
      const unsigned vstep = isv ? 4096u : 0u;
      asm volatile("ds_read_b128 %1, %3\n\tds_read_b128 %2, %4\n\ts_waitcnt lgkmcnt(0)\n\t"
                   "v_mfma_f32_32x32x16_bf16 %0, %1, %5, %6\n\ts_nop 7\n\ts_nop 3"
                   : "=&{v[192:207]}"(s0), "=&v"(kf), "=&v"(v0) : "v"(ka), "v"(va), "v"(qa), "v"(nga) : "memory");
#define E_(b, i) "v_exp_f32 v[" #b "+" #i "], v[" #b "+" #i "]\n\t"
#define C_(d, i, b, j, k) "v_cvt_pk_bf16_f32 v[" #d "+" #i "], v[" #b "+" #j "], v[" #b "+" #k "]\n\t"
#define MID_(SC) E_(SC, 4) E_(SC, 5) E_(SC, 6) E_(SC, 7) C_(224, 0, SC, 0, 1) C_(224, 1, SC, 2, 3) E_(SC, 8) E_(SC, 9) C_(224, 2, SC, 4, 5) C_(224, 3, SC, 6, 7) E_(SC, 10) E_(SC, 11)
#define END_(SC) E_(SC, 12) E_(SC, 13) E_(SC, 14) E_(SC, 15) C_(228, 0, SC, 8, 9) C_(228, 1, SC, 10, 11) C_(228, 2, SC, 12, 13) C_(228, 3, SC, 14, 15)
#define UNIT_A(KOFF, V1OFF) \
      "v_mfma_f32_32x32x16_bf16 v[208:223], %[kf], %[qb], %[ngb]\n\t" \
      "ds_read_b128 %[kf], %[ka] offset:" #KOFF "\n\t" \
      E_(192, 0) E_(192, 1) E_(192, 2) E_(192, 3) \
      "v_mfma_f32_32x32x16_bf16 %[ob], %[v1], v[228:231], %[ob]\n\t" \
      "ds_read_b128 %[v1], %[va] offset:" #V1OFF "\n\t" \
      MID_(192) \
      "s_waitcnt lgkmcnt(2)\n\t" \
      "v_mfma_f32_32x32x16_bf16 %[oa], %[v0], v[224:227], %[oa]\n\t" \
      END_(192)
#define UNIT_B(V0OFF) \
      "s_waitcnt lgkmcnt(0)\n\t" \
      "v_mfma_f32_32x32x16_bf16 v[192:207], %[kf], %[qa], %[nga]\n\t" \
      E_(208, 0) E_(208, 1) E_(208, 2) E_(208, 3) \
      "v_mfma_f32_32x32x16_bf16 %[oa], %[v1], v[228:231], %[oa]\n\t" \
      MID_(208) \
      "v_mfma_f32_32x32x16_bf16 %[ob], %[v0], v[224:227], %[ob]\n\t" \
      "ds_read_b128 %[v0], %[va] offset:" #V0OFF "\n\t" \
      END_(208)
#define RUN_GROUPS(NG) do { int cnt_ = (NG); \
      asm volatile("1:\n\t" UNIT_A(2048, 512) UNIT_B(2048) UNIT_A(4096, 2560) UNIT_B(4096) \
        "v_add_u32 %[ka], 0x1000, %[ka]\n\tv_add_u32 %[va], %[va], %[vs]\n\t" \
        "s_sub_u32 %[cnt], %[cnt], 1\n\ts_cmp_lg_u32 %[cnt], 0\n\ts_cbranch_scc1 1b\n\ts_waitcnt lgkmcnt(0)\n\ts_nop 7\n\ts_nop 3" \
        : "+{v[192:207]}"(s0), "=&{v[208:223]}"(s1), "=&{v[224:227]}"(pa), "+{v[228:231]}"(pb), [oa] "+v"(oa), [ob] "+v"(ob), [kf] "+v"(kf), [v0] "+v"(v0), [v1] "+v"(v1), \
          [ka] "+v"(ka), [va] "+v"(va), [cnt] "+s"(cnt_) \
        : [qa] "v"(qa), [qb] "v"(qb_), [nga] "v"(nga), [ngb] "v"(ngb), [vs] "v"(vstep) : "memory", "scc"); } while (0)
      RUN_GROUPS(3);
      lds_bar();
      RUN_GROUPS(4);
      lds_bar();
      RUN_GROUPS(8);
      lds_bar();
      RUN_GROUPS(1);
      lds_bar();
      RUN_GROUPS(8);
      lds_bar();
      RUN_GROUPS(8);
      ob = MFMA(v1, pb, ob);
    } else {
      float ca = -3.0e38f, cb = -3.0e38f;
      const f32x16 zero = {};
      auto online = [&](const bf16x8& kf, const bf16x8& v0, const bf16x8& v1, const bf16x8& q, f32x16& o, float& mrun) {
        f32x16 s = MFMA(kf, q, zero);
        float tm = s[0];
#pragma unroll
        for (int i = 1; i < 16; ++i) tm = fmaxf(tm, s[i]);
        tm = maxx32(tm);
        const float mn = fmaxf(mrun, tm), f = __builtin_amdgcn_exp2f(mrun - mn);
        mrun = mn;
#pragma unroll
        for (int i = 0; i < 16; ++i) { o[i] *= f; s[i] = __builtin_amdgcn_exp2f(s[i] - mn); }
        o = MFMA(v0, pack8(s[0], s[1], s[2], s[3], s[4], s[5], s[6], s[7]), o);
        o = MFMA(v1, pack8(s[8], s[9], s[10], s[11], s[12], s[13], s[14], s[15]), o);
      };
      for (int t = 0; t < 64; ++t) {
        if (t == 6 || t == 14 || t == 30 || t == 32 || t == 48) lds_bar();
        const bf16x8 kf = *(const LAS bf16x8*)(lds + l * 16 + t * 2048);
        const int vo = isv ? va0 + t * 2048 : va0;
        const bf16x8 v0 = *(const LAS bf16x8*)(lds + vo), v1 = *(const LAS bf16x8*)(lds + vo + 512);
        online(kf, v0, v1, qa, oa, ca);
        online(kf, v0, v1, qb_, ob, cb);
      }
    }
    const float ia = __builtin_amdgcn_rcpf(sumx32(oa[8])), ib = __builtin_amdgcn_rcpf(sumx32(ob[8]));
    const __amdgpu_buffer_rsrc_t rw = make_rsrc(ws, WS_USED);
    st16<MG>(rw, WS_O + (unsigned)(((qb * 8 + w) * 32 + head) * 64 + l) * 16, pack8(oa[0] * ia, oa[1] * ia, oa[2] * ia, oa[3] * ia, oa[4] * ia, oa[5] * ia, oa[6] * ia, oa[7] * ia));
    st16<MG>(rw, WS_O + (unsigned)(((qb * 8 + w + 4) * 32 + head) * 64 + l) * 16, pack8(ob[0] * ib, ob[1] * ib, ob[2] * ib, ob[3] * ib, ob[4] * ib, ob[5] * ib, ob[6] * ib, ob[7] * ib));
  }
  if constexpr (MG) {
    publish(ws, FL_B + (qb * 32 + head) * 64, c.epoch);
    if (b < 224) prefetch_w1<7>(p, c, 32, 80);
  }
}

template <int KQ, int HGAP> DI void quarter_reduce_t(int m, int l, LAS f32x4* xch, const f32x16 (&acc)[2], float (&val)[8]) {
#pragma unroll
  for (int d = 0; d < 4; ++d) {
    if (d == KQ) continue;
    xch[(d >> 1) * HGAP + (((d * 4 + KQ) * 2 + m) * 2 + 0) * 64 + l] = f32x4{acc[d >> 1][8 * (d & 1)], acc[d >> 1][8 * (d & 1) + 1], acc[d >> 1][8 * (d & 1) + 2], acc[d >> 1][8 * (d & 1) + 3]};
    xch[(d >> 1) * HGAP + (((d * 4 + KQ) * 2 + m) * 2 + 1) * 64 + l] = f32x4{acc[d >> 1][8 * (d & 1) + 4], acc[d >> 1][8 * (d & 1) + 5], acc[d >> 1][8 * (d & 1) + 6], acc[d >> 1][8 * (d & 1) + 7]};
  }
  lds_bar();
  f32x4 t[4][2];
#pragma unroll
  for (int s = 0; s < 4; ++s) { if (s == KQ) continue; t[s][0] = xch[(KQ >> 1) * HGAP + (((KQ * 4 + s) * 2 + m) * 2 + 0) * 64 + l]; t[s][1] = xch[(KQ >> 1) * HGAP + (((KQ * 4 + s) * 2 + m) * 2 + 1) * 64 + l]; }
#pragma unroll
  for (int j = 0; j < 8; ++j) val[j] = acc[KQ >> 1][8 * (KQ & 1) + j];
#pragma unroll
  for (int s = 0; s < 4; ++s) { if (s == KQ) continue; for (int j = 0; j < 4; ++j) { val[j] += t[s][0][j]; val[4 + j] += t[s][1][j]; } }
}
template <int HGAP> DI void quarter_reduce(const Ctx& c, LAS f32x4* xch, const f32x16 (&acc)[2], float (&val)[8]) {
  const int m = c.w & 1, kq = c.w >> 1;
  if (kq == 0) quarter_reduce_t<0, HGAP>(m, c.l, xch, acc, val);
  else if (kq == 1) quarter_reduce_t<1, HGAP>(m, c.l, xch, acc, val);
  else if (kq == 2) quarter_reduce_t<2, HGAP>(m, c.l, xch, acc, val);
  else quarter_reduce_t<3, HGAP>(m, c.l, xch, acc, val);
}

template <bool MG> DI void phaseC(const Params& p, const Ctx& c, u32x4& hkeep) {
  char* ws = p.ws;
  LAS char* lds = c.lds;
  const int l = c.l, w = c.w, hh = c.hh, b = c.b;
  const int rb = (b & 7) * 4 + ((b >> 3) & 3), cb = b >> 5;
  const int m = w & 1, kq = w >> 1;
  const int rt = 2 * rb + m, kstep = 4 * cb + kq;
  const GAS u32x2* xp = (const GAS u32x2*)((const char*)p.x + (size_t)rt * 32 * 1024 + kstep * 32 + (l & 31) * 1024 + hh * 8);
  const u32x2 xr0 = xp[0], xr1 = xp[2];
  const int q4 = (l & 15) >> 2, p4 = l & 3, blk = (l >> 4) & 1;
  const int wa0 = (128 * kq + 4 * hh + q4) * 128 + (((2 * blk + (p4 >> 1)) ^ (((q4 >> 1) & 1) << 2)) * 16) + (p4 & 1) * 8;
  const int wa1 = wa0 ^ 64;
  if constexpr (!MG) { prefetch_wo<8>(p, c); wait_bar<0>(); }
  bf16x8 wf[8][2];
#pragma unroll
  for (int i = 0; i < 8; ++i) {
    wf[i][0] = cat8(trread(lds + wa0 + i * 2048), trread(lds + wa0 + i * 2048 + 1024));
    wf[i][1] = cat8(trread(lds + wa1 + i * 2048), trread(lds + wa1 + i * 2048 + 1024));
  }
  if constexpr (MG) await(ws, FL_B + (rb >> 2) * 32 * 64, 32, c.epoch); else lds_bar();
  const char* abase = ws + WS_O + ((size_t)rt * 32 + kq * 8) * 1024;
  bf16x8 af[8];
  sfor<0, 8>([&](auto I) { constexpr int i = I.value; gload16<(i & 3) * 1024, MG>(af[i], abase + (i >> 2) * 4096, l * 16); });
  f32x16 acc[2] = {};
  sfor<0, 8>([&](auto I) {
    constexpr int i = I.value;
    asm volatile("s_waitcnt vmcnt(%1)" : "+v"(af[i]) : "i"(7 - i) : "memory");
    acc[0] = MFMA(wf[i][0], af[i], acc[0]);
    acc[1] = MFMA(wf[i][1], af[i], acc[1]);
    SBAR();
  });
  float val[8];
  quarter_reduce<0>(c, (LAS f32x4*)lds, acc, val);
  const u32x4 o = {cvtpk(val[0] + bf_lo(xr0[0]), val[1] + bf_hi(xr0[0])), cvtpk(val[2] + bf_lo(xr0[1]), val[3] + bf_hi(xr0[1])),
                   cvtpk(val[4] + bf_lo(xr1[0]), val[5] + bf_hi(xr1[0])), cvtpk(val[6] + bf_lo(xr1[1]), val[7] + bf_hi(xr1[1]))};
  hkeep = o;
  const __amdgpu_buffer_rsrc_t rw = make_rsrc(ws, WS_USED);
  st16<MG>(rw, WS_HF + (unsigned)((rt * 32 + kstep) * 64 + l) * 16, __builtin_bit_cast(bf16x8, o));
  f32x2 sm = {0.f, 0.f}, sq = {0.f, 0.f};
#pragma unroll
  for (int i = 0; i < 4; ++i) { const f32x2 v = unpk(o[i]); sm += v; sq = __builtin_elementwise_fma(v, v, sq); }
  LAS f32x2* tst = (LAS f32x2*)(lds + 0);
  tst[((m * 4 + kq) * 2 + hh) * 32 + (l & 31)] = f32x2{sm[0] + sm[1], sq[0] + sq[1]};
  lds_bar();
  if (w < 2 && l < 32) {
    f32x2 t8[8];
#pragma unroll
    for (int i = 0; i < 8; ++i) t8[i] = tst[(w * 8 + i) * 32 + l];
    const f32x2 t = ((t8[0] + t8[1]) + (t8[2] + t8[3])) + ((t8[4] + t8[5]) + (t8[6] + t8[7]));
    st8<MG>(rw, WS_STAT + (unsigned)(((2 * rb + w) * 8 + cb) * 32 + l) * 8, __builtin_bit_cast(u32x2, t));
  }
  if constexpr (MG) {
    publish(ws, FL_C + ((rb >> 1) * 16 + (rb & 1) * 8 + cb) * 64, c.epoch);
    if (b < 224) prefetch_w1<7>(p, c, 96, 128);
  }
}

constexpr int D_WIN = 6;
template <bool MG> DI void phaseD(const Params& p, const Ctx& c) {
  char* ws = p.ws;
  LAS char* lds = c.lds;
  const int l = c.l, w = c.w, hh = c.hh, b = c.b;
  const int rb = 2 * (b & 7) + ((b >> 3) & 1), cb = b >> 4;
  const int m = w & 3, nh = w >> 2;
  const int rt = 4 * rb + m;
  LAS float* tcs = (LAS float*)(lds + 49152);
  LAS float* tb = tcs + 128;
  if (c.tid < 128) tcs[c.tid] = 0.f;
  const bf16x8 bv = *(const GAS bf16x8*)((const char*)p.b1 + cb * 256 + (l & 15) * 16);
  if constexpr (!MG) { prefetch_w1<8>(p, c, 0, 128); wait_bar<0>(); }
  const int par = m >> 1;
  const int q4 = (l & 15) >> 2, p4 = l & 3, blk = (l >> 4) & 1;
  const int wl = nh * 8192 + (4 * hh + q4) * 128 + (((4 * (m & 1) + 2 * blk + (p4 >> 1)) ^ (((q4 >> 1) & 1) << 2)) * 16) + (p4 & 1) * 8;
  const int wa_e = wl + par * 2048, wa_o = wl + (1 - par) * 2048;
  bf16x8 wf[3][2];
  auto load_w = [&](auto I) {
    constexpr int i = I.value;
    constexpr int o = (int)w1_stage_base(i >> 2) + (i & 2) * 2048;
    const int wa = (i & 1) ? wa_o : wa_e;
    wf[i % 3][0] = cat8(trread(lds + wa + o), trread(lds + wa + o + 1024));
    wf[i % 3][1] = cat8(trread(lds + (wa ^ 64) + o), trread(lds + (wa ^ 64) + o + 1024));
  };
  load_w(IC<0>{}); load_w(IC<1>{});
  if constexpr (MG) await(ws, FL_C + rb * 16 * 64, 16, c.epoch); else lds_bar();
  const char* abase = ws + WS_HF + (size_t)rt * 32 * 1024;
  const unsigned aoff0 = l * 16 + par * 1024, aoff1 = l * 16 + (1 - par) * 1024;
  bf16x8 af[32];
  auto load_a = [&](auto I) { constexpr int i = I.value;
    if constexpr (i & 1) gload16<((i >> 1) & 1) * 2048, MG>(af[i], abase + (i >> 2) * 4096, aoff1);
    else gload16<((i >> 1) & 1) * 2048, MG>(af[i], abase + (i >> 2) * 4096, aoff0); };
  sfor<0, 4>(load_a);
  u32x2 st[4];
  sfor<0, 4>([&](auto I) { constexpr int i = I.value; gload8<i * 256, MG>(st[i], ws + WS_STAT + (size_t)rt * 2048, (l & 31) * 8 + hh * 1024); });
  sfor<4, D_WIN>(load_a);
  f32x16 acc[2] = {};
  f32x4 cs = {};
  const bf16x8 sel = rowsum_sel(l);
  sfor<0, 32>([&](auto I) {
    constexpr int i = I.value;
    if constexpr (i + D_WIN < 32) load_a(IC<i + D_WIN>{});
    constexpr int issued = i + D_WIN < 32 ? i + D_WIN + 1 : 32;
    asm volatile("s_waitcnt vmcnt(%1)" : "+v"(af[i]) : "i"(issued - i - 1 + (i < 4 ? 4 : 0)) : "memory");
    if constexpr (i == 20) asm volatile("s_barrier" ::: "memory");
    acc[0] = MFMA(wf[i % 3][0], af[i], acc[0]);
    acc[1] = MFMA(wf[i % 3][1], af[i], acc[1]);
    if constexpr ((i & 1) == 0) cs = MFMA16(wf[i % 3][0], sel, cs);
    if constexpr (i + 2 < 32) { SBAR(); load_w(IC<i + 2>{}); }
    SBAR();
  });
  if ((l & 15) < 2) {
    LAS float* d = tcs + nh * 64 + (m & 1) * 32 + 16 * (l & 15) + 4 * (l >> 4);
#pragma unroll
    for (int j = 0; j < 4; ++j) __hip_atomic_fetch_add(d + j, cs[j], __ATOMIC_RELAXED, __HIP_MEMORY_SCOPE_WORKGROUP);
  }
  if (w == 0 && l < 16) { const u32x4 u = __builtin_bit_cast(u32x4, bv);
    *(LAS f32x4*)(tb + l * 8) = f32x4{bf_lo(u[0]), bf_hi(u[0]), bf_lo(u[1]), bf_hi(u[1])}; *(LAS f32x4*)(tb + l * 8 + 4) = f32x4{bf_lo(u[2]), bf_hi(u[2]), bf_lo(u[3]), bf_hi(u[3])}; }
  asm volatile("" : "+v"(st[0]), "+v"(st[1]), "+v"(st[2]), "+v"(st[3]));
  const f32x2 ss = __builtin_bit_cast(f32x2, st[0]) + __builtin_bit_cast(f32x2, st[1]) + __builtin_bit_cast(f32x2, st[2]) + __builtin_bit_cast(f32x2, st[3]);
  const float sm = sumx32(ss[0]), sq = sumx32(ss[1]);
  const float mean = sm * (1.f / D), rstd = __builtin_amdgcn_rsqf(fmaxf(sq * (1.f / D) - mean * mean, 0.f) + LN_EPS);
  const float nmr = -mean * rstd;
  lds_bar();
  const __amdgpu_buffer_rsrc_t rw = make_rsrc(ws, WS_USED);
#pragma unroll
  for (int t = 0; t < 2; ++t) {
    const int ct = 2 * nh + ((m & 1) ^ t);
    f32x4 cv[4], bb[4];
#pragma unroll
    for (int g = 0; g < 4; ++g) { cv[g] = *(const LAS f32x4*)(tcs + 32 * ct + 8 * g + 4 * hh); bb[g] = *(const LAS f32x4*)(tb + 32 * ct + 8 * g + 4 * hh); }
#pragma unroll
    for (int s = 0; s < 2; ++s) {
      unsigned pk[4];
#pragma unroll
      for (int j = 0; j < 4; ++j) {
        const int r = 8 * s + 2 * j;
        const f32x2 a = {acc[t][r], acc[t][r + 1]}, c2 = {cv[r >> 2][r & 3], cv[r >> 2][(r & 3) + 1]}, b2 = {bb[r >> 2][r & 3], bb[r >> 2][(r & 3) + 1]};
        const f32x2 y = __builtin_elementwise_fma(a, f32x2{rstd, rstd}, __builtin_elementwise_fma(c2, f32x2{nmr, nmr}, b2));
        const f32x2 z = y * LOG2E;
        const f32x2 u = {__builtin_amdgcn_exp2f(z[0]), __builtin_amdgcn_exp2f(z[1])};
        const f32x2 d = __builtin_elementwise_fma(u, u + 2.f, f32x2{2.f, 2.f});
        const f32x2 r2 = {__builtin_amdgcn_rcpf(d[0]), __builtin_amdgcn_rcpf(d[1])};
        const f32x2 th = __builtin_elementwise_fma(r2, f32x2{-2.f, -2.f}, f32x2{1.f, 1.f});
        const f32x2 o = y * th;
        pk[j] = cvtpk(o[0], o[1]);
      }
      st16<MG>(rw, WS_ACT + (unsigned)((rt * 112 + cb * 8 + ct * 2 + s) * 64 + l) * 16, __builtin_bit_cast(bf16x8, u32x4{pk[0], pk[1], pk[2], pk[3]}));
      if constexpr (MG) {
        const int q = 2 * t + s;
        prefetch_w2<8>(p, c, 24 * q, 24 * q + 24);
      }
    }
  }
  if constexpr (MG) publish(ws, FL_D + (rb * 16 + cb) * 64, c.epoch);
}

constexpr int E_NSLOT = 10, E_NST = 14, E_WIN = 6, E_PRE = 8;
struct ESched {
  int posA[28], posW[E_NST], n_top[E_NST], n_mid[E_NST];
  constexpr ESched() : posA(), posW(), n_top(), n_mid() {
    int n = 0;
    for (int i = 0; i < E_WIN; ++i) posA[i] = n++;
    for (int st = 0; st < E_NST; ++st) {
      n_top[st] = n;
      if (E_PRE + st < E_NST) { n += 2; posW[E_PRE + st] = n - 1; }
      for (int j = 0; j < 2; ++j) if (E_WIN + 2 * st + j < 28) posA[E_WIN + 2 * st + j] = n++;
      n_mid[st] = n;
    }
  }
};
template <bool MG> DI void phaseE(const Params& p, const Ctx& c, u32x4 hkeep) {
  constexpr ESched sch{};
  char* ws = p.ws;
  LAS char* lds = c.lds;
  const int l = c.l, w = c.w, hh = c.hh, b = c.b;
  const int rb = (b & 7) * 4 + ((b >> 3) & 3), cb = b >> 5;
  const int m = w & 1, kq = w >> 1;
  const int rt = 2 * rb + m, kstep = 4 * cb + kq;
  const bool early = MG && b < 224;
  if (early) prefetch_w2<7>(p, c, 16 * 6, 16 * 8); else prefetch_w2<7>(p, c, 0, 16 * E_PRE);
  bf16x8 resv = __builtin_bit_cast(bf16x8, hkeep);
  if constexpr (!MG) resv = *(const GAS bf16x8*)(ws + WS_HF + (size_t)(rt * 32 + kstep) * 1024 + l * 16);
  const int q4 = (l & 15) >> 2, p4 = l & 3, blk = (l >> 4) & 1;
  const int wa0 = kq * 4096 + (4 * hh + q4) * 128 + (((2 * blk + (p4 >> 1)) ^ (((q4 >> 1) & 1) << 2)) * 16) + (p4 & 1) * 8;
  const int wa1 = wa0 ^ 64;
  bf16x8 wf[3][2][2];
  auto load_w = [&](auto ST) {
    constexpr int st = ST.value;
    const LAS char* sl = lds + ((st + 4) % E_NSLOT) * 16384;
#pragma unroll
    for (int i = 0; i < 2; ++i) {
      wf[st % 3][i][0] = cat8(trread(sl + wa0 + i * 2048), trread(sl + wa0 + i * 2048 + 1024));
      wf[st % 3][i][1] = cat8(trread(sl + wa1 + i * 2048), trread(sl + wa1 + i * 2048 + 1024));
    }
  };
  if (!early) wait_bar<0>();
  load_w(IC<0>{}); load_w(IC<1>{});
  if constexpr (MG) await(ws, FL_D + (rb >> 1) * 16 * 64, 14, c.epoch);
  const char* abase = ws + WS_ACT + ((size_t)rt * 112 + kq * 28) * 1024;
  bf16x8 af[28];
  auto load_a = [&](auto I) { constexpr int i = I.value; gload16<(i & 3) * 1024, MG>(af[i], abase + (i >> 2) * 4096, l * 16); };
  sfor<0, E_WIN>(load_a);
  f32x16 acc[2] = {};
  sfor<0, E_NST>([&](auto ST) {
    constexpr int st = ST.value;
    if constexpr (st == 2) asm volatile("s_waitcnt lgkmcnt(0)\n\ts_barrier" ::: "memory");
    if constexpr (st == 6 || st == 8 || st == 10) asm volatile("s_waitcnt vmcnt(%0) lgkmcnt(0)\n\ts_barrier" ::"i"(sch.n_top[st] - sch.posW[st + 3] - 1) : "memory");
    if constexpr (E_PRE + st < E_NST) prefetch_w2<8>(p, c, 16 * (st + E_PRE), 16 * (st + E_PRE + 1));
    if constexpr (E_WIN + 2 * st < 28) load_a(IC<E_WIN + 2 * st>{});
    if constexpr (E_WIN + 2 * st + 1 < 28) load_a(IC<E_WIN + 2 * st + 1>{});
    asm volatile("s_waitcnt vmcnt(%2)" : "+v"(af[2 * st]), "+v"(af[2 * st + 1]) : "i"(sch.n_mid[st] - sch.posA[2 * st + 1] - 1) : "memory");
    acc[0] = MFMA(wf[st % 3][0][0], af[2 * st], acc[0]);
    acc[1] = MFMA(wf[st % 3][0][1], af[2 * st], acc[1]);
    if constexpr (st + 2 < E_NST) { SBAR(); load_w(IC<st + 2>{}); SBAR(); }
    acc[0] = MFMA(wf[st % 3][1][0], af[2 * st + 1], acc[0]);
    acc[1] = MFMA(wf[st % 3][1][1], af[2 * st + 1], acc[1]);
    SBAR();
  });
  { asm volatile("" : "+v"(resv));
    const u32x4 ru = __builtin_bit_cast(u32x4, resv);
#pragma unroll
    for (int k = 0; k < 4; ++k) { const float on = kq == k ? 1.f : 0.f;
#pragma unroll
      for (int j = 0; j < 4; ++j) { acc[k >> 1][8 * (k & 1) + 2 * j] += on * bf_lo(ru[j]); acc[k >> 1][8 * (k & 1) + 2 * j + 1] += on * bf_hi(ru[j]); } } }
  constexpr int PS = 272, PQ = 64 * PS;
  LAS char* xa = lds;
#pragma unroll
  for (int ct = 0; ct < 2; ++ct)
#pragma unroll
    for (int g = 0; g < 4; ++g)
      *(LAS f32x4*)(xa + kq * PQ + (32 * m + (l & 31)) * PS + (32 * ct + 8 * g + 4 * hh) * 4) = f32x4{acc[ct][4 * g], acc[ct][4 * g + 1], acc[ct][4 * g + 2], acc[ct][4 * g + 3]};
  lds_bar();
  { const int row = c.tid >> 3, c8 = c.tid & 7;
    f32x4 t0[4], t1[4];
#pragma unroll
    for (int q = 0; q < 4; ++q) { t0[q] = *(const LAS f32x4*)(xa + q * PQ + row * PS + c8 * 32); t1[q] = *(const LAS f32x4*)(xa + q * PQ + row * PS + c8 * 32 + 16); }
    const f32x4 s0 = (t0[0] + t0[1]) + (t0[2] + t0[3]), s1 = (t1[0] + t1[1]) + (t1[2] + t1[3]);
    const __amdgpu_buffer_rsrc_t ro = make_rsrc(p.out, S * D * 2);
    st16<false>(ro, (unsigned)(rb * 64 + row) * 1024u + cb * 128 + c8 * 16, pack8(s0[0], s0[1], s0[2], s0[3], s1[0], s1[1], s1[2], s1[3])); }
}

DI Ctx make_ctx(char* lds_g) {
  Ctx c;
  c.lds = (LAS char*)lds_g; c.lds0 = (unsigned)(uintptr_t)lds_g;
  c.tid = threadIdx.x; c.l = c.tid & 63; c.w = rfl(c.tid >> 6); c.hh = c.l >> 5; c.b = blockIdx.x; c.epoch = 0;
  return c;
}

__global__ __launch_bounds__(512, 2) void block_mega(Params p) {
  extern __shared__ __attribute__((aligned(16))) char lds_g[];
  Ctx c = make_ctx(lds_g);
  c.epoch = __hip_atomic_load((GAS unsigned*)(p.ws + WS_FLAG) + (threadIdx.x >> 12), __ATOMIC_RELAXED, __HIP_MEMORY_SCOPE_AGENT);
  phaseA<true>(p, c);
  phaseB<true>(p, c);
  u32x4 hkeep;
  phaseC<true>(p, c, hkeep);
  if (c.b < 224) phaseD<true>(p, c);
  phaseE<true>(p, c, hkeep);
  if (c.b == 0 && c.tid == 0) __hip_atomic_store((GAS unsigned*)(p.ws + WS_FLAG), c.epoch, __ATOMIC_RELAXED, __HIP_MEMORY_SCOPE_AGENT);
  asm volatile("mega_code_end:");
}

extern "C" void kernel_launch(void* const* d_in, const int* in_sizes, int n_in, void* d_out, int out_size, void* d_ws, size_t ws_size, hipStream_t stream) {
  Params p;
  p.x = (const u16*)d_in[0]; p.wqkv = (const u16*)d_in[1]; p.wo = (const u16*)d_in[2];
  p.w1 = (const u16*)d_in[4]; p.b1 = (const u16*)d_in[5]; p.w2 = (const u16*)d_in[6];
  p.ws = (char*)d_ws; p.out = (u16*)d_out;
  (void)hipFuncSetAttribute((const void*)block_mega, hipFuncAttributeMaxDynamicSharedMemorySize, LDS_BYTES);
  block_mega<<<256, 512, LDS_BYTES, stream>>>(p);
}
```

```cpp
#include <hip/hip_runtime.h>
#include <hip/hip_bf16.h>
#include <type_traits>

typedef unsigned short u16;
using bf16x8 = __attribute__((ext_vector_type(8))) short;
using s16x4 = __attribute__((ext_vector_type(4))) short;
using f32x16 = __attribute__((ext_vector_type(16))) float;
using f32x4 = __attribute__((ext_vector_type(4))) float;
using f32x2 = __attribute__((ext_vector_type(2))) float;
using u32x4 = __attribute__((ext_vector_type(4))) unsigned;
using u32x2 = __attribute__((ext_vector_type(2))) unsigned;
using bf16x2_t = __attribute__((ext_vector_type(2))) __bf16;
#define DI __device__ __forceinline__
#define LAS __attribute__((address_space(3)))
#define GAS __attribute__((address_space(1)))
#define MFMA(a, b, c) __builtin_amdgcn_mfma_f32_32x32x16_bf16((a), (b), (c), 0, 0, 0)
#define SBAR() __builtin_amdgcn_sched_barrier(0)

constexpr int S = 2048, D = 512, F = 1792;
constexpr float LN_EPS = 1e-5f;
constexpr float LOG2E = 1.4426950408889634f;
constexpr float QSCALE = 0.25f * LOG2E;
constexpr float BOUND_SAFE = 40.f;
constexpr int LDS_BYTES = 163840;

constexpr unsigned WS_FLAG = 0;
constexpr unsigned FL_A = 4096, FL_B = FL_A + 256 * 64, FL_C = FL_B + 256 * 64, FL_D = FL_C + 256 * 64;
constexpr unsigned WS_KMAX = 128u << 10, WS_STAT = 256u << 10;
constexpr unsigned WS_Q = 1u << 20, WS_KV = 3u << 20  , WS_O = 7u << 20, WS_HF = 9u << 20, WS_ACT = 11u << 20;
constexpr unsigned WS_USED = 19u << 20;

struct Params { const u16 *x, *wqkv, *wo, *w1, *b1, *w2; char* ws; u16* out; };

template <int N> using IC = std::integral_constant<int, N>;
template <int B, int E, class Fn> DI void sfor(Fn&& f) { if constexpr (B < E) { f(IC<B>{}); sfor<B + 1, E>(f); } }

DI int rfl(int v) { return __builtin_amdgcn_readfirstlane(v); }
DI unsigned cvtpk(float lo, float hi) { const f32x2 v = {lo, hi}; return __builtin_bit_cast(unsigned, __builtin_convertvector(v, bf16x2_t)); }
DI float bf_lo(unsigned u) { return __uint_as_float(u << 16); }
DI float bf_hi(unsigned u) { return __uint_as_float(u & 0xffff0000u); }
constexpr unsigned ONES2 = 0x3F803F80u;
#define MFMA16(a, b, c) __builtin_amdgcn_mfma_f32_16x16x32_bf16((a), (b), (c), 0, 0, 0)
DI f32x2 unpk(unsigned u) { return f32x2{__uint_as_float(u << 16), __uint_as_float(u & 0xffff0000u)}; }
DI bf16x8 rowsum_sel(int l) { const unsigned on = ((l & 15) == ((l >> 4) & 1)) ? ONES2 : 0u; return __builtin_bit_cast(bf16x8, u32x4{on, on, on, on}); }
DI void dma16(const void* sbase, unsigned voff, unsigned lds) {
  asm volatile("s_mov_b32 m0, %2\n\ts_nop 0\n\tglobal_load_lds_dwordx4 %0, %1" ::"v"(voff), "s"(sbase), "s"(lds) : "memory");
}
DI void dma16_sc1(const void* sbase, unsigned voff, unsigned lds) {
  asm volatile("s_mov_b32 m0, %2\n\ts_nop 0\n\tglobal_load_lds_dwordx4 %0, %1 sc1" ::"v"(voff), "s"(sbase), "s"(lds) : "memory");
}
template <int IMM, bool SC1> DI void gload16(bf16x8& dst, const void* sbase, unsigned voff) {
  if constexpr (SC1) asm volatile("global_load_dwordx4 %0, %1, %2 offset:%3 sc1" : "=v"(dst) : "v"(voff), "s"(sbase), "i"(IMM) : "memory");
  else asm volatile("global_load_dwordx4 %0, %1, %2 offset:%3" : "=v"(dst) : "v"(voff), "s"(sbase), "i"(IMM) : "memory");
}
template <int IMM, bool SC1 = false> DI void gload8(u32x2& dst, const void* sbase, unsigned voff) {
  if constexpr (SC1) asm volatile("global_load_dwordx2 %0, %1, %2 offset:%3 sc1" : "=v"(dst) : "v"(voff), "s"(sbase), "i"(IMM) : "memory");
  else asm volatile("global_load_dwordx2 %0, %1, %2 offset:%3" : "=v"(dst) : "v"(voff), "s"(sbase), "i"(IMM) : "memory");
}
template <bool SC1> DI void gload4(float& dst, const void* sbase, unsigned voff) {
  if constexpr (SC1) asm volatile("global_load_dword %0, %1, %2 sc1" : "=v"(dst) : "v"(voff), "s"(sbase) : "memory");
  else asm volatile("global_load_dword %0, %1, %2" : "=v"(dst) : "v"(voff), "s"(sbase) : "memory");
}
typedef __attribute__((ext_vector_type(4))) unsigned v4u_;
DI __amdgpu_buffer_rsrc_t make_rsrc(void* p, unsigned bytes) { return __builtin_amdgcn_make_buffer_rsrc(p, 0, bytes, 0x00020000); }
template <bool SC1> DI void st16(__amdgpu_buffer_rsrc_t r, unsigned off, bf16x8 v) { __builtin_amdgcn_raw_buffer_store_b128(__builtin_bit_cast(v4u_, v), r, off, 0, SC1 ? 16 : 0); }
template <bool SC1> DI void st8(__amdgpu_buffer_rsrc_t r, unsigned off, u32x2 v) { __builtin_amdgcn_raw_buffer_store_b64(v, r, off, 0, SC1 ? 16 : 0); }
template <bool SC1> DI void st4(__amdgpu_buffer_rsrc_t r, unsigned off, unsigned v) { __builtin_amdgcn_raw_buffer_store_b32(v, r, off, 0, SC1 ? 16 : 0); }

template <int N> DI void wait_bar() { asm volatile("s_waitcnt vmcnt(%0) lgkmcnt(0)\n\ts_barrier" ::"i"(N) : "memory"); }
DI void lds_bar() { asm volatile("s_waitcnt lgkmcnt(0)\n\ts_barrier" ::: "memory"); }
DI s16x4 trread(const LAS char* p) {
  typedef short v4i16_t __attribute__((ext_vector_type(4)));
  return __builtin_bit_cast(s16x4, __builtin_amdgcn_ds_read_tr16_b64_v4i16((LAS v4i16_t*)p));
}
DI bf16x8 cat8(s16x4 a, s16x4 b) { return __builtin_shufflevector(a, b, 0, 1, 2, 3, 4, 5, 6, 7); }
DI bf16x8 pack8(float a0, float a1, float a2, float a3, float a4, float a5, float a6, float a7) {
  u32x4 w = {cvtpk(a0, a1), cvtpk(a2, a3), cvtpk(a4, a5), cvtpk(a6, a7)};
  return __builtin_bit_cast(bf16x8, w);
}
template <int CTRL> DI float dpp(float v) { return __uint_as_float(__builtin_amdgcn_update_dpp(0u, __float_as_uint(v), CTRL, 0xf, 0xf, true)); }
DI float sum8(float v) { v += dpp<0xB1>(v); v += dpp<0x4E>(v); v += dpp<0x141>(v); return v; }
DI float sumx32(float v) { auto r = __builtin_amdgcn_permlane32_swap(__float_as_uint(v), __float_as_uint(v), false, false); return __uint_as_float(r[0]) + __uint_as_float(r[1]); }
DI float maxx32(float v) { auto r = __builtin_amdgcn_permlane32_swap(__float_as_uint(v), __float_as_uint(v), false, false); return fmaxf(__uint_as_float(r[0]), __uint_as_float(r[1])); }
DI float maxx16(float v) { auto r = __builtin_amdgcn_permlane16_swap(__float_as_uint(v), __float_as_uint(v), false, false); return fmaxf(__uint_as_float(r[0]), __uint_as_float(r[1])); }
DI float max16(float v) { v = fmaxf(v, dpp<0xB1>(v)); v = fmaxf(v, dpp<0x4E>(v)); v = fmaxf(v, dpp<0x141>(v)); v = fmaxf(v, dpp<0x140>(v)); return v; }

DI void publish(char* ws, unsigned flag_off, unsigned epoch) {
  asm volatile("s_waitcnt vmcnt(0) lgkmcnt(0)\n\ts_barrier" ::: "memory");
  if (threadIdx.x == 0) __hip_atomic_store((GAS unsigned*)(ws + flag_off), epoch, __ATOMIC_RELAXED, __HIP_MEMORY_SCOPE_AGENT);
}
DI void await(char* ws, unsigned flag_off, int n, unsigned epoch) {
  if (threadIdx.x >= 448) {
    const int l = threadIdx.x & 63;
    GAS unsigned* f = (GAS unsigned*)(ws + flag_off + (l < n ? l : 0) * 64);
    for (unsigned spins = 0; spins < (1u << 20); ++spins) {
      const unsigned v = __hip_atomic_load(f, __ATOMIC_RELAXED, __HIP_MEMORY_SCOPE_AGENT);
      if (__all(v == epoch)) break;
      __builtin_amdgcn_s_sleep(1);
    }
  }
  asm volatile("s_waitcnt lgkmcnt(0)\n\ts_barrier" ::: "memory");
}

struct Ctx { LAS char* lds; unsigned lds0; int tid, l, w, hh, b; unsigned epoch; };

DI void phaseA(const Params& p, Ctx& c) {
  char* ws = p.ws;
  LAS char* lds = c.lds;
  const int l = c.l, w = c.w, hh = c.hh, b = c.b;
  const int rb = 8 * ((b & 7) >> 1) + ((b >> 3) & 7), hg = 4 * (b & 1) + (b >> 6);
  bf16x8 ft[3] = {};
  {
    unsigned long long pc; unsigned len;
    asm volatile("s_getpc_b64 %0\n.Lmega_here%=:\n\ts_mov_b32 %1, mega_code_end-.Lmega_here%=" : "=s"(pc), "=s"(len));
    const unsigned j = (unsigned)(b >> 3);
    const unsigned oc = j * 1024u + (w & 1) * 32768u + l * 16u;
    ft[2] = *(const GAS bf16x8*)((const char*)pc + (w < 2 && oc + 16 <= len ? oc : 0u));
    ft[0] = *(const GAS bf16x8*)((const char*)p.x + (size_t)(512 * ((b & 7) >> 1) + 256 * (b & 1) + 8 * j + w) * 1024 + l * 16);
    { const unsigned pair = 2u * (w < 6 ? w : 5) + (l >> 5);
      ft[1] = *(const GAS bf16x8*)((const char*)p.wqkv + (size_t)(128 * ((b & 7) >> 1) + 4 * j + pair / 3) * 3072 + (pair % 3) * 1024 + (b & 1) * 512 + (l & 31) * 16); }
  }
  const int q4 = (l & 15) >> 2, p4 = l & 3, blk = (l >> 4) & 1;
  LAS float* tmean = (LAS float*)(lds + 0);
  LAS float* trstd = tmean + 64;
  LAS float* tcs = tmean + 128;
  f32x16 acc[3] = {};
  const int m = w & 1, g = (w >> 1) & 1;
  if (w < 4) {
    const bf16x8 sel = rowsum_sel(l);
    const int swz = (l >> 1) & 7;
    const int xrow = (32 * m + (l & 31)) * 128;
    const int wa = 8192 + (8 * hh + q4) * 128 + (((4 * g + 2 * blk + (p4 >> 1)) ^ (((q4 >> 1) & 1) << 2)) * 16) + (p4 & 1) * 8;
    int xo[4], wo_[2];
#pragma unroll
    for (int i = 0; i < 4; ++i) xo[i] = xrow + (((2 * (i ^ m) + hh) ^ swz) * 16);
    wo_[0] = wa + m * 2048; wo_[1] = wa + (1 - m) * 2048;
    bf16x8 xf[2][4], wf[2][4][3];
    f32x4 cs[3] = {};
    auto ld_w = [&](auto SET, const LAS char* sl, int i, int sgm) { constexpr int q = SET.value; const int o = wo_[i & 1] + (i & 2) * 2048 + sgm * 8192;
      wf[q][i][sgm] = cat8(trread(sl + o), trread(sl + o + 512)); };
    lds_bar();
#pragma unroll
    for (int i = 0; i < 4; ++i) { xf[0][i] = *(const LAS bf16x8*)(lds + xo[i]); for (int sgm = 0; sgm < 3; ++sgm) ld_w(IC<0>{}, lds, i, sgm); }
    sfor<0, 8>([&](auto ST) {
      constexpr int st = ST.value, q = st & 1, qn = (st + 1) & 1;
      if constexpr (st + 1 < 8) lds_bar(); else asm volatile("s_waitcnt lgkmcnt(0)" ::: "memory");
      asm volatile("" : "+v"(xf[q][0]), "+v"(xf[q][1]), "+v"(xf[q][2]), "+v"(xf[q][3]), "+v"(wf[q][0][0]), "+v"(wf[q][0][1]), "+v"(wf[q][0][2]), "+v"(wf[q][1][0]), "+v"(wf[q][1][1]), "+v"(wf[q][1][2]),
                   "+v"(wf[q][2][0]), "+v"(wf[q][2][1]), "+v"(wf[q][2][2]), "+v"(wf[q][3][0]), "+v"(wf[q][3][1]), "+v"(wf[q][3][2]));
      const LAS char* sn = lds + ((st + 1) % 5) * 32768;
#pragma unroll
      for (int i = 0; i < 4; ++i) {
        acc[0] = MFMA(wf[q][i][0], xf[q][i], acc[0]);
        SBAR();
        if constexpr (st + 1 < 8) { ld_w(IC<qn>{}, sn, i, 0); xf[qn][i] = *(const LAS bf16x8*)(sn + xo[i]); }
        SBAR();
        acc[1] = MFMA(wf[q][i][1], xf[q][i], acc[1]);
        SBAR();
        if constexpr (st + 1 < 8) ld_w(IC<qn>{}, sn, i, 1);
        SBAR();
        acc[2] = MFMA(xf[q][i], wf[q][i][2], acc[2]);
        SBAR();
        if constexpr (st + 1 < 8) ld_w(IC<qn>{}, sn, i, 2);
        if ((i & 1) == 0) { cs[0] = MFMA16(wf[q][i][0], sel, cs[0]); cs[1] = MFMA16(wf[q][i][1], sel, cs[1]); cs[2] = MFMA16(wf[q][i][2], sel, cs[2]); }
        SBAR();
      }
    });
    if ((l & 15) < 2) {
#pragma unroll
      for (int t = 0; t < 3; ++t) *(LAS f32x4*)(tcs + (w * 3 + t) * 32 + 16 * (l & 15) + 4 * (l >> 4)) = cs[t];
    }
  } else {
    const int h = w - 4;
    const unsigned xo0 = (unsigned)(64 * rb + 16 * h + (l >> 3)) * 1024u + (unsigned)(((l & 7) ^ ((l >> 4) & 7)) * 16);
    const unsigned xo1 = (unsigned)(64 * rb + 16 * h + 8 + (l >> 3)) * 1024u + (unsigned)(((l & 7) ^ ((4 + (l >> 4)) & 7)) * 16);
    const unsigned wof = (unsigned)(16 * h + (l >> 3)) * 3072u + (unsigned)hg * 128u + (unsigned)(((l & 7) ^ (((l >> 4) & 1) << 2)) * 16);
    auto issue = [&](int st) {
      const unsigned dst = c.lds0 + (st % 5) * 32768 + h * 2048;
      const char* wsrc = (const char*)p.wqkv + (size_t)st * 64 * 3072;
      dma16((const char*)p.x + st * 128, xo0, dst);
      dma16((const char*)p.x + st * 128, xo1, dst + 1024);
#pragma unroll
      for (int sgm = 0; sgm < 3; ++sgm) { dma16(wsrc + sgm * 1024, wof, dst + 8192 + sgm * 8192); dma16(wsrc + sgm * 1024 + 8 * 3072, wof, dst + 8192 + sgm * 8192 + 1024); }
    };
    issue(0); issue(1); issue(2); issue(3); issue(4);
    f32x2 rs[2] = {}, rq[2] = {};
    u32x4 sv[2][2];
    wait_bar<32>();
    sv[0][0] = *(const LAS u32x4*)(lds + h * 2048 + l * 16); sv[0][1] = *(const LAS u32x4*)(lds + h * 2048 + 1024 + l * 16);
    sfor<0, 8>([&](auto ST) {
      constexpr int st = ST.value, q = st & 1, qn = (st + 1) & 1;
      if constexpr (st + 1 < 8) {
        constexpr int last = 4 + st > 7 ? 7 : 4 + st;
        wait_bar<8 * (last - (st + 1))>();
        if constexpr (st + 5 < 8) issue(st + 5);
        const LAS char* sn = lds + ((st + 1) % 5) * 32768 + h * 2048 + l * 16;
        sv[qn][0] = *(const LAS u32x4*)sn; sv[qn][1] = *(const LAS u32x4*)(sn + 1024);
      }
#pragma unroll
      for (int r = 0; r < 2; ++r)
#pragma unroll
        for (int i = 0; i < 4; ++i) { const f32x2 v = unpk(sv[q][r][i]); rs[r] += v; rq[r] = __builtin_elementwise_fma(v, v, rq[r]); }
      asm volatile("" : "+v"(rs[0]), "+v"(rq[0]), "+v"(rs[1]), "+v"(rq[1]));
    });
#pragma unroll
    for (int r = 0; r < 2; ++r) {
      const float s1 = sum8(rs[r][0] + rs[r][1]), s2 = sum8(rq[r][0] + rq[r][1]);
      if ((l & 7) == 0) { const float mean = s1 * (1.f / D); const int row = 16 * h + 8 * r + (l >> 3);
        tmean[row] = mean; trstd[row] = __builtin_amdgcn_rsqf(fmaxf(s2 * (1.f / D) - mean * mean, 0.f) + LN_EPS); }
    }
  }
  { asm volatile("" : "+v"(c.epoch), "+v"(ft[0]), "+v"(ft[1]), "+v"(ft[2])); c.epoch += 1u; }
  lds_bar();
  const __amdgpu_buffer_rsrc_t rw = make_rsrc(ws, WS_USED);
  if (w < 4) {
    const int tile = 2 * rb + m;
    const float mean_l = tmean[32 * m + (l & 31)], rstd_l = trstd[32 * m + (l & 31)];
    f32x4 cv[2][4], mm[4], rr[4];
#pragma unroll
    for (int gg = 0; gg < 4; ++gg) {
#pragma unroll
      for (int t = 0; t < 2; ++t) cv[t][gg] = *(const LAS f32x4*)(tcs + (w * 3 + t) * 32 + 8 * gg + 4 * hh) + *(const LAS f32x4*)(tcs + ((w ^ 1) * 3 + t) * 32 + 8 * gg + 4 * hh);
      mm[gg] = *(const LAS f32x4*)(tmean + 32 * m + 8 * gg + 4 * hh); rr[gg] = *(const LAS f32x4*)(trstd + 32 * m + 8 * gg + 4 * hh);
    }
    const int n = l & 31;
    const float ncs = -(tcs[(w * 3 + 2) * 32 + n] + tcs[((w ^ 1) * 3 + 2) * 32 + n]);
#pragma unroll
    for (int s = 0; s < 2; ++s) {
      f32x2 v[4];
#pragma unroll
      for (int j = 0; j < 4; ++j) { const int r = 8 * s + 2 * j; const f32x2 a = {acc[2][r], acc[2][r + 1]}, m2 = {mm[r >> 2][r & 3], mm[r >> 2][(r & 3) + 1]}, r2 = {rr[r >> 2][r & 3], rr[r >> 2][(r & 3) + 1]};
        v[j] = __builtin_elementwise_fma(m2, f32x2{ncs, ncs}, a) * r2; }
      st16<true>(rw, WS_KV + (unsigned)((4 * hg + 2 * g + (n >> 4)) * 64 + tile) * 2048 + 1024 + s * 512 + hh * 256 + (n & 15) * 16,
               pack8(v[0][0], v[0][1], v[1][0], v[1][1], v[2][0], v[2][1], v[3][0], v[3][1]));
    }
#pragma unroll
    for (int t = 1; t >= 0; --t) {
      const float sc = t == 0 ? rstd_l * QSCALE : rstd_l, nm = -mean_l * sc;
#pragma unroll
      for (int s = 0; s < 2; ++s) {
        f32x2 v[4];
#pragma unroll
        for (int j = 0; j < 4; ++j) { const int r = 8 * s + 2 * j; const f32x2 a = {acc[t][r], acc[t][r + 1]}, cc = {cv[t][r >> 2][r & 3], cv[t][r >> 2][(r & 3) + 1]};
          v[j] = __builtin_elementwise_fma(a, f32x2{sc, sc}, cc * nm); }
        const int head = 4 * hg + 2 * g + s;
        st16<true>(rw, (t == 0 ? WS_Q + (unsigned)(head * 64 + tile) * 1024 : WS_KV + (unsigned)(head * 64 + tile) * 2048) + l * 16, pack8(v[0][0], v[0][1], v[1][0], v[1][1], v[2][0], v[2][1], v[3][0], v[3][1]));
        if (t == 1) {
          f32x2 n2 = v[0] * v[0]; n2 = __builtin_elementwise_fma(v[1], v[1], n2); n2 = __builtin_elementwise_fma(v[2], v[2], n2); n2 = __builtin_elementwise_fma(v[3], v[3], n2);
          const float mx = maxx16(max16(sumx32(n2[0] + n2[1])));
          if (l == 0) st4<true>(rw, WS_KMAX + (unsigned)(head * 64 + tile) * 4, __float_as_uint(mx));
        }
      }
    }
  }
  publish(ws, FL_A + (hg * 32 + rb) * 64, c.epoch);
}

DI unsigned wpiece_lane(int l, unsigned row_bytes) { return (unsigned)(l >> 3) * row_bytes + (unsigned)(((l & 7) ^ (((l >> 4) & 1) << 2)) * 16); }
template <int NW, int W0 = 0> DI void prefetch_wo(const Params& p, const Ctx& c) {
  if (c.w < W0 || c.w >= W0 + NW) return;
  const unsigned off = wpiece_lane(c.l, 1024) + (unsigned)(c.b >> 5) * 128u;
  for (int pc = c.w - W0; pc < 64; pc += NW) dma16((const char*)p.wo + (size_t)(8 * pc) * 1024, off, c.lds0 + pc * 1024);
}
DI constexpr unsigned w1_stage_base(int j) { return j < 6 ? 65536 + j * 16384 : (j - 6) * 16384; }
template <int NW, int W0 = 0> DI void prefetch_w1(const Params& p, const Ctx& c, int p0, int p1) {
  if (c.w < W0 || c.w >= W0 + NW) return;
  const unsigned off = wpiece_lane(c.l, F * 2) + (unsigned)(c.b >> 4) * 256u;
  for (int pc = p0 + c.w - W0; pc < p1; pc += NW) {
    const int j = pc >> 4, seg = (pc >> 3) & 1, r8 = pc & 7;
    dma16((const char*)p.w1 + (size_t)(64 * j + 8 * r8) * (F * 2) + seg * 128, off, c.lds0 + w1_stage_base(j) + seg * 8192 + r8 * 1024);
  }
}
template <int NW> DI void prefetch_w2(const Params& p, const Ctx& c, int p0, int p1) {
  if (c.w >= NW) return;
  const unsigned off = wpiece_lane(c.l, 1024) + (unsigned)(c.b >> 5) * 128u;
  for (int pc = p0 + c.w; pc < p1; pc += NW) {
    const int st = pc >> 4, kq = (pc >> 2) & 3, r8 = pc & 3;
    dma16((const char*)p.w2 + (size_t)(448 * kq + 32 * st + 8 * r8) * 1024, off, c.lds0 + ((st + 4) % 10) * 16384 + kq * 4096 + r8 * 1024);
  }
}

DI void warm_first(const Params& p, const Ctx& c) {
  const int h = c.w - 4;
  const unsigned off1 = (unsigned)c.b * 7168u + (unsigned)h * 2048u + (unsigned)c.l * 16u;
  dma16((const char*)p.w1, off1, c.lds0 + 139520);
  dma16((const char*)p.w2, off1, c.lds0 + 139520);
  if (h < 3) { dma16((const char*)p.w1 + 1024, off1, c.lds0 + 139520); dma16((const char*)p.w2 + 1024, off1, c.lds0 + 139520); }
}
DI void warm_w1_late(const Params& p, const Ctx& c) {
  const int h = c.w - 4;
  const unsigned j = (unsigned)(c.b >> 3);
  for (int pc = h; pc < 35; pc += 4)
    dma16((const char*)p.w1, (pc < 21 ? 128u * 3584u + j * 21504u + pc * 1024u : 384u * 3584u + j * 14336u + (pc - 21) * 1024u) + c.l * 16u, c.lds0 + 139520);
}
DI void warm_w2(const Params& p, const Ctx& c) {
  const int h = c.w - 4;
  const unsigned off = (unsigned)(c.b >> 3) * 57344u + (unsigned)h * 14336u + (unsigned)c.l * 16u;
#pragma unroll
  for (int i = 0; i < 14; ++i) dma16((const char*)p.w2 + i * 1024, off, c.lds0 + 139520);
}

constexpr int B_ONES = 131072;
DI void phaseB(const Params& p, const Ctx& c) {
  char* ws = p.ws;
  LAS char* lds = c.lds;
  const int l = c.l, w = c.w, hh = c.hh, b = c.b, tid = c.tid;
  const int head = 4 * (b & 7) + ((b >> 3) & 3), qb = b >> 5;
  for (int i = tid; i < (4096 + 512) / 16; i += 512) { const unsigned v = (i & 31) == 0 ? ONES2 : 0u; ((LAS u32x4*)(lds + B_ONES))[i] = u32x4{v, v, v, v}; }
  await(ws, FL_A + (head >> 2) * 32 * 64, 32, c.epoch);
  const unsigned l16 = l * 16;
  if (w >= 4) {
    const char* kvg = ws + WS_KV + (size_t)head * 131072;
    const int h = w - 4;
    auto piece = [&](int pc) { dma16_sc1(kvg + pc * 1024, l16, c.lds0 + pc * 1024); };
#pragma unroll
    for (int i = 0; i < 4; ++i) piece(4 * h + i);
#pragma unroll
    for (int i = 0; i < 12; ++i) piece(16 + 4 * i + h);
    wait_bar<12>();
#pragma unroll
    for (int i = 0; i < 16; ++i) piece(64 + 4 * i + h);
    wait_bar<24>();
    wait_bar<16>();
    warm_first(p, c);
    wait_bar<0>();
    if (b < 224) prefetch_w1<4, 4>(p, c, 80, 96);
    lds_bar();
    prefetch_wo<4, 4>(p, c); warm_w1_late(p, c);
    lds_bar();
    if (b < 224) prefetch_w1<4, 4>(p, c, 0, 32);
    warm_w2(p, c);
  } else {
    bf16x8 qa, qb_;
    float kmx;
    gload4<true>(kmx, ws + WS_KMAX + head * 256, l * 4);
    gload16<0, true>(qa, ws + WS_Q + (size_t)(head * 64 + qb * 8 + w) * 1024, l * 16);
    gload16<0, true>(qb_, ws + WS_Q + (size_t)(head * 64 + qb * 8 + w + 4) * 1024, l * 16);
    asm volatile("s_waitcnt vmcnt(0)" : "+v"(qa), "+v"(qb_), "+v"(kmx)::"memory");
    kmx = maxx32(maxx16(max16(kmx)));
    auto qbound = [&](bf16x8 q) { const u32x4 u = __builtin_bit_cast(u32x4, q); f32x2 t = {0.f, 0.f}; for (int i = 0; i < 4; ++i) { const f32x2 v = unpk(u[i]); t = __builtin_elementwise_fma(v, v, t); }
      return __builtin_amdgcn_sqrtf(sumx32(t[0] + t[1]) * kmx) * 1.01f + 1e-6f; };
    const float bna = qbound(qa), bnb = qbound(qb_);
    const bool safe = __all(bna <= BOUND_SAFE && bnb <= BOUND_SAFE);
    f32x16 oa = {}, ob = {};
    const int lo5 = l & 31;
    const bool isv = lo5 < 16;
    const int va0 = isv ? 1024 + hh * 256 + lo5 * 16 : B_ONES + (lo5 == 16 ? 0 : (lo5 >= 20 && lo5 < 28) ? 64 : 16);
    lds_bar();
    if (safe) {
      f32x16 nga, ngb; for (int i = 0; i < 16; ++i) { nga[i] = -bna; ngb[i] = -bnb; }
      f32x16 s0, s1;
      bf16x8 pa, pb = {};
      bf16x8 kf, v0, v1 = {};
      unsigned ka = c.lds0 + l * 16, va = c.lds0 + va0;
      const unsigned vstep = isv ? 4096u : 0u;
      asm volatile("ds_read_b128 %1, %3\n\tds_read_b128 %2, %4\n\ts_waitcnt lgkmcnt(0)\n\t"
                   "v_mfma_f32_32x32x16_bf16 %0, %1, %5, %6\n\ts_nop 7\n\ts_nop 3"
                   : "=&{v[192:207]}"(s0), "=&v"(kf), "=&v"(v0) : "v"(ka), "v"(va), "v"(qa), "v"(nga) : "memory");
#define E_(b, i) "v_exp_f32 v[" #b "+" #i "], v[" #b "+" #i "]\n\t"
#define C_(d, i, b, j, k) "v_cvt_pk_bf16_f32 v[" #d "+" #i "], v[" #b "+" #j "], v[" #b "+" #k "]\n\t"
#define MID_(SC) E_(SC, 4) E_(SC, 5) E_(SC, 6) E_(SC, 7) C_(224, 0, SC, 0, 1) C_(224, 1, SC, 2, 3) E_(SC, 8) E_(SC, 9) C_(224, 2, SC, 4, 5) C_(224, 3, SC, 6, 7) E_(SC, 10) E_(SC, 11)
#define END_(SC) E_(SC, 12) E_(SC, 13) E_(SC, 14) E_(SC, 15) C_(228, 0, SC, 8, 9) C_(228, 1, SC, 10, 11) C_(228, 2, SC, 12, 13) C_(228, 3, SC, 14, 15)
#define UNIT_A(KOFF, V1OFF) \
      "v_mfma_f32_32x32x16_bf16 v[208:223], %[kf], %[qb], %[ngb]\n\t" \
      "ds_read_b128 %[kf], %[ka] offset:" #KOFF "\n\t" \
      E_(192, 0) E_(192, 1) E_(192, 2) E_(192, 3) \
      "v_mfma_f32_32x32x16_bf16 %[ob], %[v1], v[228:231], %[ob]\n\t" \
      "ds_read_b128 %[v1], %[va] offset:" #V1OFF "\n\t" \
      MID_(192) \
      "s_waitcnt lgkmcnt(2)\n\t" \
      "v_mfma_f32_32x32x16_bf16 %[oa], %[v0], v[224:227], %[oa]\n\t" \
      END_(192)
#define UNIT_B(V0OFF) \
      "s_waitcnt lgkmcnt(0)\n\t" \
      "v_mfma_f32_32x32x16_bf16 v[192:207], %[kf], %[qa], %[nga]\n\t" \
      E_(208, 0) E_(208, 1) E_(208, 2) E_(208, 3) \
      "v_mfma_f32_32x32x16_bf16 %[oa], %[v1], v[228:231], %[oa]\n\t" \
      MID_(208) \
      "v_mfma_f32_32x32x16_bf16 %[ob], %[v0], v[224:227], %[ob]\n\t" \
      "ds_read_b128 %[v0], %[va] offset:" #V0OFF "\n\t" \
      END_(208)
#define RUN_GROUPS(NG) do { int cnt_ = (NG); \
      asm volatile("1:\n\t" UNIT_A(2048, 512) UNIT_B(2048) UNIT_A(4096, 2560) UNIT_B(4096) \
        "v_add_u32 %[ka], 0x1000, %[ka]\n\tv_add_u32 %[va], %[va], %[vs]\n\t" \
        "s_sub_u32 %[cnt], %[cnt], 1\n\ts_cmp_lg_u32 %[cnt], 0\n\ts_cbranch_scc1 1b\n\ts_waitcnt lgkmcnt(0)\n\ts_nop 7\n\ts_nop 3" \
        : "+{v[192:207]}"(s0), "=&{v[208:223]}"(s1), "=&{v[224:227]}"(pa), "+{v[228:231]}"(pb), [oa] "+v"(oa), [ob] "+v"(ob), [kf] "+v"(kf), [v0] "+v"(v0), [v1] "+v"(v1), \
          [ka] "+v"(ka), [va] "+v"(va), [cnt] "+s"(cnt_) \
        : [qa] "v"(qa), [qb] "v"(qb_), [nga] "v"(nga), [ngb] "v"(ngb), [vs] "v"(vstep) : "memory", "scc"); } while (0)
      RUN_GROUPS(3);
      lds_bar();
      RUN_GROUPS(4);
      lds_bar();
      RUN_GROUPS(8);
      lds_bar();
      RUN_GROUPS(1);
      lds_bar();
      RUN_GROUPS(8);
      lds_bar();
      RUN_GROUPS(8);
      ob = MFMA(v1, pb, ob);
    } else {
      float ca = -3.0e38f, cb = -3.0e38f;
      const f32x16 zero = {};
      auto online = [&](const bf16x8& kf, const bf16x8& v0, const bf16x8& v1, const bf16x8& q, f32x16& o, float& mrun) {
        f32x16 s = MFMA(kf, q, zero);
        float tm = s[0];
#pragma unroll
        for (int i = 1; i < 16; ++i) tm = fmaxf(tm, s[i]);
        tm = maxx32(tm);
        const float mn = fmaxf(mrun, tm), f = __builtin_amdgcn_exp2f(mrun - mn);
        mrun = mn;
#pragma unroll
        for (int i = 0; i < 16; ++i) { o[i] *= f; s[i] = __builtin_amdgcn_exp2f(s[i] - mn); }
        o = MFMA(v0, pack8(s[0], s[1], s[2], s[3], s[4], s[5], s[6], s[7]), o);
        o = MFMA(v1, pack8(s[8], s[9], s[10], s[11], s[12], s[13], s[14], s[15]), o);
      };
      for (int t = 0; t < 64; ++t) {
        if (t == 6 || t == 14 || t == 30 || t == 32 || t == 48) lds_bar();
        const bf16x8 kf = *(const LAS bf16x8*)(lds + l * 16 + t * 2048);
        const int vo = isv ? va0 + t * 2048 : va0;
        const bf16x8 v0 = *(const LAS bf16x8*)(lds + vo), v1 = *(const LAS bf16x8*)(lds + vo + 512);
        online(kf, v0, v1, qa, oa, ca);
        online(kf, v0, v1, qb_, ob, cb);
      }
    }
    const float ia = __builtin_amdgcn_rcpf(sumx32(oa[8])), ib = __builtin_amdgcn_rcpf(sumx32(ob[8]));
    const __amdgpu_buffer_rsrc_t rw = make_rsrc(ws, WS_USED);
    st16<true>(rw, WS_O + (unsigned)(((qb * 8 + w) * 32 + head) * 64 + l) * 16, pack8(oa[0] * ia, oa[1] * ia, oa[2] * ia, oa[3] * ia, oa[4] * ia, oa[5] * ia, oa[6] * ia, oa[7] * ia));
    st16<true>(rw, WS_O + (unsigned)(((qb * 8 + w + 4) * 32 + head) * 64 + l) * 16, pack8(ob[0] * ib, ob[1] * ib, ob[2] * ib, ob[3] * ib, ob[4] * ib, ob[5] * ib, ob[6] * ib, ob[7] * ib));
  }
  {
    publish(ws, FL_B + (qb * 32 + head) * 64, c.epoch);
    if (b < 224) prefetch_w1<7>(p, c, 32, 80);
  }
}

template <int KQ, int HGAP> DI void quarter_reduce_t(int m, int l, LAS f32x4* xch, const f32x16 (&acc)[2], float (&val)[8]) {
#pragma unroll
  for (int d = 0; d < 4; ++d) {
    if (d == KQ) continue;
    xch[(d >> 1) * HGAP + (((d * 4 + KQ) * 2 + m) * 2 + 0) * 64 + l] = f32x4{acc[d >> 1][8 * (d & 1)], acc[d >> 1][8 * (d & 1) + 1], acc[d >> 1][8 * (d & 1) + 2], acc[d >> 1][8 * (d & 1) + 3]};
    xch[(d >> 1) * HGAP + (((d * 4 + KQ) * 2 + m) * 2 + 1) * 64 + l] = f32x4{acc[d >> 1][8 * (d & 1) + 4], acc[d >> 1][8 * (d & 1) + 5], acc[d >> 1][8 * (d & 1) + 6], acc[d >> 1][8 * (d & 1) + 7]};
  }
  lds_bar();
  f32x4 t[4][2];
#pragma unroll
  for (int s = 0; s < 4; ++s) { if (s == KQ) continue; t[s][0] = xch[(KQ >> 1) * HGAP + (((KQ * 4 + s) * 2 + m) * 2 + 0) * 64 + l]; t[s][1] = xch[(KQ >> 1) * HGAP + (((KQ * 4 + s) * 2 + m) * 2 + 1) * 64 + l]; }
#pragma unroll
  for (int j = 0; j < 8; ++j) val[j] = acc[KQ >> 1][8 * (KQ & 1) + j];
#pragma unroll
  for (int s = 0; s < 4; ++s) { if (s == KQ) continue; for (int j = 0; j < 4; ++j) { val[j] += t[s][0][j]; val[4 + j] += t[s][1][j]; } }
}
template <int HGAP> DI void quarter_reduce(const Ctx& c, LAS f32x4* xch, const f32x16 (&acc)[2], float (&val)[8]) {
  const int m = c.w & 1, kq = c.w >> 1;
  if (kq == 0) quarter_reduce_t<0, HGAP>(m, c.l, xch, acc, val);
  else if (kq == 1) quarter_reduce_t<1, HGAP>(m, c.l, xch, acc, val);
  else if (kq == 2) quarter_reduce_t<2, HGAP>(m, c.l, xch, acc, val);
  else quarter_reduce_t<3, HGAP>(m, c.l, xch, acc, val);
}

DI void phaseC(const Params& p, const Ctx& c, u32x4& hkeep) {
  char* ws = p.ws;
  LAS char* lds = c.lds;
  const int l = c.l, w = c.w, hh = c.hh, b = c.b;
  const int rb = (b & 7) * 4 + ((b >> 3) & 3), cb = b >> 5;
  const int m = w & 1, kq = w >> 1;
  const int rt = 2 * rb + m, kstep = 4 * cb + kq;
  const GAS u32x2* xp = (const GAS u32x2*)((const char*)p.x + (size_t)rt * 32 * 1024 + kstep * 32 + (l & 31) * 1024 + hh * 8);
  const u32x2 xr0 = xp[0], xr1 = xp[2];
  const int q4 = (l & 15) >> 2, p4 = l & 3, blk = (l >> 4) & 1;
  const int wa0 = (128 * kq + 4 * hh + q4) * 128 + (((2 * blk + (p4 >> 1)) ^ (((q4 >> 1) & 1) << 2)) * 16) + (p4 & 1) * 8;
  const int wa1 = wa0 ^ 64;
  bf16x8 wf[8][2];
#pragma unroll
  for (int i = 0; i < 8; ++i) {
    wf[i][0] = cat8(trread(lds + wa0 + i * 2048), trread(lds + wa0 + i * 2048 + 1024));
    wf[i][1] = cat8(trread(lds + wa1 + i * 2048), trread(lds + wa1 + i * 2048 + 1024));
  }
  await(ws, FL_B + (rb >> 2) * 32 * 64, 32, c.epoch);
  const char* abase = ws + WS_O + ((size_t)rt * 32 + kq * 8) * 1024;
  bf16x8 af[8];
  sfor<0, 8>([&](auto I) { constexpr int i = I.value; gload16<(i & 3) * 1024, true>(af[i], abase + (i >> 2) * 4096, l * 16); });
  f32x16 acc[2] = {};
  sfor<0, 8>([&](auto I) {
    constexpr int i = I.value;
    asm volatile("s_waitcnt vmcnt(%1)" : "+v"(af[i]) : "i"(7 - i) : "memory");
    acc[0] = MFMA(wf[i][0], af[i], acc[0]);
    acc[1] = MFMA(wf[i][1], af[i], acc[1]);
    SBAR();
  });
  float val[8];
  quarter_reduce<0>(c, (LAS f32x4*)lds, acc, val);
  const u32x4 o = {cvtpk(val[0] + bf_lo(xr0[0]), val[1] + bf_hi(xr0[0])), cvtpk(val[2] + bf_lo(xr0[1]), val[3] + bf_hi(xr0[1])),
                   cvtpk(val[4] + bf_lo(xr1[0]), val[5] + bf_hi(xr1[0])), cvtpk(val[6] + bf_lo(xr1[1]), val[7] + bf_hi(xr1[1]))};
  hkeep = o;
  const __amdgpu_buffer_rsrc_t rw = make_rsrc(ws, WS_USED);
  st16<true>(rw, WS_HF + (unsigned)((rt * 32 + kstep) * 64 + l) * 16, __builtin_bit_cast(bf16x8, o));
  f32x2 sm = {0.f, 0.f}, sq = {0.f, 0.f};
#pragma unroll
  for (int i = 0; i < 4; ++i) { const f32x2 v = unpk(o[i]); sm += v; sq = __builtin_elementwise_fma(v, v, sq); }
  LAS f32x2* tst = (LAS f32x2*)(lds + 0);
  tst[((m * 4 + kq) * 2 + hh) * 32 + (l & 31)] = f32x2{sm[0] + sm[1], sq[0] + sq[1]};
  lds_bar();
  if (w < 2 && l < 32) {
    f32x2 t8[8];
#pragma unroll
    for (int i = 0; i < 8; ++i) t8[i] = tst[(w * 8 + i) * 32 + l];
    const f32x2 t = ((t8[0] + t8[1]) + (t8[2] + t8[3])) + ((t8[4] + t8[5]) + (t8[6] + t8[7]));
    st8<true>(rw, WS_STAT + (unsigned)(((2 * rb + w) * 8 + cb) * 32 + l) * 8, __builtin_bit_cast(u32x2, t));
  }
  publish(ws, FL_C + ((rb >> 1) * 16 + (rb & 1) * 8 + cb) * 64, c.epoch);
  if (b < 224) prefetch_w1<7>(p, c, 96, 128);
}

constexpr int D_WIN = 6;
DI void phaseD(const Params& p, const Ctx& c) {
  char* ws = p.ws;
  LAS char* lds = c.lds;
  const int l = c.l, w = c.w, hh = c.hh, b = c.b;
  const int rb = 2 * (b & 7) + ((b >> 3) & 1), cb = b >> 4;
  const int m = w & 3, nh = w >> 2;
  const int rt = 4 * rb + m;
  LAS float* tcs = (LAS float*)(lds + 49152);
  LAS float* tb = tcs + 128;
  if (c.tid < 128) tcs[c.tid] = 0.f;
  const bf16x8 bv = *(const GAS bf16x8*)((const char*)p.b1 + cb * 256 + (l & 15) * 16);
  const int par = m >> 1;
  const int q4 = (l & 15) >> 2, p4 = l & 3, blk = (l >> 4) & 1;
  const int wl = nh * 8192 + (4 * hh + q4) * 128 + (((4 * (m & 1) + 2 * blk + (p4 >> 1)) ^ (((q4 >> 1) & 1) << 2)) * 16) + (p4 & 1) * 8;
  const int wa_e = wl + par * 2048, wa_o = wl + (1 - par) * 2048;
  bf16x8 wf[3][2];
  auto load_w = [&](auto I) {
    constexpr int i = I.value;
    constexpr int o = (int)w1_stage_base(i >> 2) + (i & 2) * 2048;
    const int wa = (i & 1) ? wa_o : wa_e;
    wf[i % 3][0] = cat8(trread(lds + wa + o), trread(lds + wa + o + 1024));
    wf[i % 3][1] = cat8(trread(lds + (wa ^ 64) + o), trread(lds + (wa ^ 64) + o + 1024));
  };
  load_w(IC<0>{}); load_w(IC<1>{});
  await(ws, FL_C + rb * 16 * 64, 16, c.epoch);
  const char* abase = ws + WS_HF + (size_t)rt * 32 * 1024;
  const unsigned aoff0 = l * 16 + par * 1024, aoff1 = l * 16 + (1 - par) * 1024;
  bf16x8 af[32];
  auto load_a = [&](auto I) { constexpr int i = I.value;
    if constexpr (i & 1) gload16<((i >> 1) & 1) * 2048, true>(af[i], abase + (i >> 2) * 4096, aoff1);
    else gload16<((i >> 1) & 1) * 2048, true>(af[i], abase + (i >> 2) * 4096, aoff0); };
  sfor<0, 4>(load_a);
  u32x2 st[4];
  sfor<0, 4>([&](auto I) { constexpr int i = I.value; gload8<i * 256, true>(st[i], ws + WS_STAT + (size_t)rt * 2048, (l & 31) * 8 + hh * 1024); });
  sfor<4, D_WIN>(load_a);
  f32x16 acc[2] = {};
  f32x4 cs = {};
  const bf16x8 sel = rowsum_sel(l);
  sfor<0, 32>([&](auto I) {
    constexpr int i = I.value;
    if constexpr (i + D_WIN < 32) load_a(IC<i + D_WIN>{});
    constexpr int issued = i + D_WIN < 32 ? i + D_WIN + 1 : 32;
    asm volatile("s_waitcnt vmcnt(%1)" : "+v"(af[i]) : "i"(issued - i - 1 + (i < 4 ? 4 : 0)) : "memory");
    if constexpr (i == 20) asm volatile("s_barrier" ::: "memory");
    acc[0] = MFMA(wf[i % 3][0], af[i], acc[0]);
    acc[1] = MFMA(wf[i % 3][1], af[i], acc[1]);
    if constexpr ((i & 1) == 0) cs = MFMA16(wf[i % 3][0], sel, cs);
    if constexpr (i + 2 < 32) { SBAR(); load_w(IC<i + 2>{}); }
    SBAR();
  });
  if ((l & 15) < 2) {
    LAS float* d = tcs + nh * 64 + (m & 1) * 32 + 16 * (l & 15) + 4 * (l >> 4);
#pragma unroll
    for (int j = 0; j < 4; ++j) __hip_atomic_fetch_add(d + j, cs[j], __ATOMIC_RELAXED, __HIP_MEMORY_SCOPE_WORKGROUP);
  }
  if (w == 0 && l < 16) { const u32x4 u = __builtin_bit_cast(u32x4, bv);
    *(LAS f32x4*)(tb + l * 8) = f32x4{bf_lo(u[0]), bf_hi(u[0]), bf_lo(u[1]), bf_hi(u[1])}; *(LAS f32x4*)(tb + l * 8 + 4) = f32x4{bf_lo(u[2]), bf_hi(u[2]), bf_lo(u[3]), bf_hi(u[3])}; }
  asm volatile("" : "+v"(st[0]), "+v"(st[1]), "+v"(st[2]), "+v"(st[3]));
  const f32x2 ss = __builtin_bit_cast(f32x2, st[0]) + __builtin_bit_cast(f32x2, st[1]) + __builtin_bit_cast(f32x2, st[2]) + __builtin_bit_cast(f32x2, st[3]);
  const float sm = sumx32(ss[0]), sq = sumx32(ss[1]);
  const float mean = sm * (1.f / D), rstd = __builtin_amdgcn_rsqf(fmaxf(sq * (1.f / D) - mean * mean, 0.f) + LN_EPS);
  const float nmr = -mean * rstd;
  lds_bar();
  const __amdgpu_buffer_rsrc_t rw = make_rsrc(ws, WS_USED);
#pragma unroll
  for (int t = 0; t < 2; ++t) {
    const int ct = 2 * nh + ((m & 1) ^ t);
    f32x4 cv[4], bb[4];
#pragma unroll
    for (int g = 0; g < 4; ++g) { cv[g] = *(const LAS f32x4*)(tcs + 32 * ct + 8 * g + 4 * hh); bb[g] = *(const LAS f32x4*)(tb + 32 * ct + 8 * g + 4 * hh); }
#pragma unroll
    for (int s = 0; s < 2; ++s) {
      unsigned pk[4];
#pragma unroll
      for (int j = 0; j < 4; ++j) {
        const int r = 8 * s + 2 * j;
        const f32x2 a = {acc[t][r], acc[t][r + 1]}, c2 = {cv[r >> 2][r & 3], cv[r >> 2][(r & 3) + 1]}, b2 = {bb[r >> 2][r & 3], bb[r >> 2][(r & 3) + 1]};
        const f32x2 y = __builtin_elementwise_fma(a, f32x2{rstd, rstd}, __builtin_elementwise_fma(c2, f32x2{nmr, nmr}, b2));
        const f32x2 z = y * LOG2E;
        const f32x2 u = {__builtin_amdgcn_exp2f(z[0]), __builtin_amdgcn_exp2f(z[1])};
        const f32x2 d = __builtin_elementwise_fma(u, u + 2.f, f32x2{2.f, 2.f});
        const f32x2 r2 = {__builtin_amdgcn_rcpf(d[0]), __builtin_amdgcn_rcpf(d[1])};
        const f32x2 th = __builtin_elementwise_fma(r2, f32x2{-2.f, -2.f}, f32x2{1.f, 1.f});
        const f32x2 o = y * th;
        pk[j] = cvtpk(o[0], o[1]);
      }
      st16<true>(rw, WS_ACT + (unsigned)((rt * 112 + cb * 8 + ct * 2 + s) * 64 + l) * 16, __builtin_bit_cast(bf16x8, u32x4{pk[0], pk[1], pk[2], pk[3]}));
      prefetch_w2<8>(p, c, 24 * (2 * t + s), 24 * (2 * t + s) + 24);
    }
  }
  publish(ws, FL_D + (rb * 16 + cb) * 64, c.epoch);
}

constexpr int E_NSLOT = 10, E_NST = 14, E_WIN = 6, E_PRE = 8;
struct ESched {
  int posA[28], posW[E_NST], n_top[E_NST], n_mid[E_NST];
  constexpr ESched() : posA(), posW(), n_top(), n_mid() {
    int n = 0;
    for (int i = 0; i < E_WIN; ++i) posA[i] = n++;
    for (int st = 0; st < E_NST; ++st) {
      n_top[st] = n;
      if (E_PRE + st < E_NST) { n += 2; posW[E_PRE + st] = n - 1; }
      for (int j = 0; j < 2; ++j) if (E_WIN + 2 * st + j < 28) posA[E_WIN + 2 * st + j] = n++;
      n_mid[st] = n;
    }
  }
};
DI void phaseE(const Params& p, const Ctx& c, u32x4 hkeep) {
  constexpr ESched sch{};
  char* ws = p.ws;
  LAS char* lds = c.lds;
  const int l = c.l, w = c.w, hh = c.hh, b = c.b;
  const int rb = (b & 7) * 4 + ((b >> 3) & 3), cb = b >> 5;
  const int m = w & 1, kq = w >> 1;
  const int rt = 2 * rb + m, kstep = 4 * cb + kq;
  const bool early = b < 224;
  if (early) prefetch_w2<7>(p, c, 16 * 6, 16 * 8); else prefetch_w2<7>(p, c, 0, 16 * E_PRE);
  bf16x8 resv = __builtin_bit_cast(bf16x8, hkeep);
  const int q4 = (l & 15) >> 2, p4 = l & 3, blk = (l >> 4) & 1;
  const int wa0 = kq * 4096 + (4 * hh + q4) * 128 + (((2 * blk + (p4 >> 1)) ^ (((q4 >> 1) & 1) << 2)) * 16) + (p4 & 1) * 8;
  const int wa1 = wa0 ^ 64;
  bf16x8 wf[3][2][2];
  auto load_w = [&](auto ST) {
    constexpr int st = ST.value;
    const LAS char* sl = lds + ((st + 4) % E_NSLOT) * 16384;
#pragma unroll
    for (int i = 0; i < 2; ++i) {
      wf[st % 3][i][0] = cat8(trread(sl + wa0 + i * 2048), trread(sl + wa0 + i * 2048 + 1024));
      wf[st % 3][i][1] = cat8(trread(sl + wa1 + i * 2048), trread(sl + wa1 + i * 2048 + 1024));
    }
  };
  if (!early) wait_bar<0>();
  load_w(IC<0>{}); load_w(IC<1>{});
  await(ws, FL_D + (rb >> 1) * 16 * 64, 14, c.epoch);
  const char* abase = ws + WS_ACT + ((size_t)rt * 112 + kq * 28) * 1024;
  bf16x8 af[28];
  auto load_a = [&](auto I) { constexpr int i = I.value; gload16<(i & 3) * 1024, true>(af[i], abase + (i >> 2) * 4096, l * 16); };
  sfor<0, E_WIN>(load_a);
  f32x16 acc[2] = {};
  sfor<0, E_NST>([&](auto ST) {
    constexpr int st = ST.value;
    if constexpr (st == 2) asm volatile("s_waitcnt lgkmcnt(0)\n\ts_barrier" ::: "memory");
    if constexpr (st == 6 || st == 8 || st == 10) asm volatile("s_waitcnt vmcnt(%0) lgkmcnt(0)\n\ts_barrier" ::"i"(sch.n_top[st] - sch.posW[st + 3] - 1) : "memory");
    if constexpr (E_PRE + st < E_NST) prefetch_w2<8>(p, c, 16 * (st + E_PRE), 16 * (st + E_PRE + 1));
    if constexpr (E_WIN + 2 * st < 28) load_a(IC<E_WIN + 2 * st>{});
    if constexpr (E_WIN + 2 * st + 1 < 28) load_a(IC<E_WIN + 2 * st + 1>{});
    asm volatile("s_waitcnt vmcnt(%2)" : "+v"(af[2 * st]), "+v"(af[2 * st + 1]) : "i"(sch.n_mid[st] - sch.posA[2 * st + 1] - 1) : "memory");
    acc[0] = MFMA(wf[st % 3][0][0], af[2 * st], acc[0]);
    acc[1] = MFMA(wf[st % 3][0][1], af[2 * st], acc[1]);
    if constexpr (st + 2 < E_NST) { SBAR(); load_w(IC<st + 2>{}); SBAR(); }
    acc[0] = MFMA(wf[st % 3][1][0], af[2 * st + 1], acc[0]);
    acc[1] = MFMA(wf[st % 3][1][1], af[2 * st + 1], acc[1]);
    SBAR();
  });
  { asm volatile("" : "+v"(resv));
    const u32x4 ru = __builtin_bit_cast(u32x4, resv);
#pragma unroll
    for (int k = 0; k < 4; ++k) { const float on = kq == k ? 1.f : 0.f;
#pragma unroll
      for (int j = 0; j < 4; ++j) { acc[k >> 1][8 * (k & 1) + 2 * j] += on * bf_lo(ru[j]); acc[k >> 1][8 * (k & 1) + 2 * j + 1] += on * bf_hi(ru[j]); } } }
  constexpr int PS = 272, PQ = 64 * PS;
  LAS char* xa = lds;
#pragma unroll
  for (int ct = 0; ct < 2; ++ct)
#pragma unroll
    for (int g = 0; g < 4; ++g)
      *(LAS f32x4*)(xa + kq * PQ + (32 * m + (l & 31)) * PS + (32 * ct + 8 * g + 4 * hh) * 4) = f32x4{acc[ct][4 * g], acc[ct][4 * g + 1], acc[ct][4 * g + 2], acc[ct][4 * g + 3]};
  lds_bar();
  { const int row = c.tid >> 3, c8 = c.tid & 7;
    f32x4 t0[4], t1[4];
#pragma unroll
    for (int q = 0; q < 4; ++q) { t0[q] = *(const LAS f32x4*)(xa + q * PQ + row * PS + c8 * 32); t1[q] = *(const LAS f32x4*)(xa + q * PQ + row * PS + c8 * 32 + 16); }
    const f32x4 s0 = (t0[0] + t0[1]) + (t0[2] + t0[3]), s1 = (t1[0] + t1[1]) + (t1[2] + t1[3]);
    const __amdgpu_buffer_rsrc_t ro = make_rsrc(p.out, S * D * 2);
    st16<false>(ro, (unsigned)(rb * 64 + row) * 1024u + cb * 128 + c8 * 16, pack8(s0[0], s0[1], s0[2], s0[3], s1[0], s1[1], s1[2], s1[3])); }
}

DI Ctx make_ctx(char* lds_g) {
  Ctx c;
  c.lds = (LAS char*)lds_g; c.lds0 = (unsigned)(uintptr_t)lds_g;
  c.tid = threadIdx.x; c.l = c.tid & 63; c.w = rfl(c.tid >> 6); c.hh = c.l >> 5; c.b = blockIdx.x; c.epoch = 0;
  return c;
}

__global__ __launch_bounds__(512, 2) void block_mega(Params p) {
  extern __shared__ __attribute__((aligned(16))) char lds_g[];
  Ctx c = make_ctx(lds_g);
  c.epoch = __hip_atomic_load((GAS unsigned*)(p.ws + WS_FLAG) + (threadIdx.x >> 12), __ATOMIC_RELAXED, __HIP_MEMORY_SCOPE_AGENT);
  phaseA(p, c);
  phaseB(p, c);
  u32x4 hkeep;
  phaseC(p, c, hkeep);
  if (c.b < 224) phaseD(p, c);
  phaseE(p, c, hkeep);
  if (c.b == 0 && c.tid == 0) __hip_atomic_store((GAS unsigned*)(p.ws + WS_FLAG), c.epoch, __ATOMIC_RELAXED, __HIP_MEMORY_SCOPE_AGENT);
  asm volatile("mega_code_end:");
}

extern "C" void kernel_launch(void* const* d_in, const int* in_sizes, int n_in, void* d_out, int out_size, void* d_ws, size_t ws_size, hipStream_t stream) {
  Params p;
  p.x = (const u16*)d_in[0]; p.wqkv = (const u16*)d_in[1]; p.wo = (const u16*)d_in[2];
  p.w1 = (const u16*)d_in[4]; p.b1 = (const u16*)d_in[5]; p.w2 = (const u16*)d_in[6];
  p.ws = (char*)d_ws; p.out = (u16*)d_out;
  (void)hipFuncSetAttribute((const void*)block_mega, hipFuncAttributeMaxDynamicSharedMemorySize, LDS_BYTES);
  block_mega<<<256, 512, LDS_BYTES, stream>>>(p);
}
```

```cpp
#include <hip/hip_runtime.h>
#include <hip/hip_bf16.h>
#include <type_traits>

typedef unsigned short u16;
using bf16x8 = __attribute__((ext_vector_type(8))) short;
using s16x4 = __attribute__((ext_vector_type(4))) short;
using f32x16 = __attribute__((ext_vector_type(16))) float;
using f32x4 = __attribute__((ext_vector_type(4))) float;
using f32x2 = __attribute__((ext_vector_type(2))) float;
using u32x4 = __attribute__((ext_vector_type(4))) unsigned;
using u32x2 = __attribute__((ext_vector_type(2))) unsigned;
using bf16x2_t = __attribute__((ext_vector_type(2))) __bf16;
#define DI __device__ __forceinline__
#define LAS __attribute__((address_space(3)))
#define GAS __attribute__((address_space(1)))
#define MFMA(a, b, c) __builtin_amdgcn_mfma_f32_32x32x16_bf16((a), (b), (c), 0, 0, 0)
#define SBAR() __builtin_amdgcn_sched_barrier(0)

constexpr int S = 2048, D = 512, F = 1792;
constexpr float LN_EPS = 1e-5f;
constexpr float LOG2E = 1.4426950408889634f;
constexpr float QSCALE = 0.25f * LOG2E;
constexpr float BOUND_SAFE = 40.f;
constexpr int LDS_BYTES = 163840;

constexpr unsigned WS_FLAG = 0;
constexpr unsigned FL_A = 4096, FL_B = FL_A + 256 * 64, FL_C = FL_B + 256 * 64, FL_D = FL_C + 256 * 64;
constexpr unsigned WS_KMAX = 128u << 10, WS_STAT = 256u << 10;
constexpr unsigned WS_Q = 1u << 20, WS_KV = 3u << 20  , WS_O = 7u << 20, WS_HF = 9u << 20, WS_ACT = 11u << 20;
constexpr unsigned WS_USED = 19u << 20;

struct Params { const u16 *x, *wqkv, *wo, *w1, *b1, *w2; char* ws; u16* out; };

template <int N> using IC = std::integral_constant<int, N>;
template <int B, int E, class Fn> DI void sfor(Fn&& f) { if constexpr (B < E) { f(IC<B>{}); sfor<B + 1, E>(f); } }

DI int rfl(int v) { return __builtin_amdgcn_readfirstlane(v); }
DI unsigned cvtpk(float lo, float hi) { const f32x2 v = {lo, hi}; return __builtin_bit_cast(unsigned, __builtin_convertvector(v, bf16x2_t)); }
DI float bf_lo(unsigned u) { return __uint_as_float(u << 16); }
DI float bf_hi(unsigned u) { return __uint_as_float(u & 0xffff0000u); }
constexpr unsigned ONES2 = 0x3F803F80u;
#define MFMA16(a, b, c) __builtin_amdgcn_mfma_f32_16x16x32_bf16((a), (b), (c), 0, 0, 0)
DI f32x2 unpk(unsigned u) { return f32x2{__uint_as_float(u << 16), __uint_as_float(u & 0xffff0000u)}; }
DI bf16x8 rowsum_sel(int l) { const unsigned on = ((l & 15) == ((l >> 4) & 1)) ? ONES2 : 0u; return __builtin_bit_cast(bf16x8, u32x4{on, on, on, on}); }
DI void dma16(const void* sbase, unsigned voff, unsigned lds) {
  asm volatile("s_mov_b32 m0, %2\n\ts_nop 0\n\tglobal_load_lds_dwordx4 %0, %1" ::"v"(voff), "s"(sbase), "s"(lds) : "memory");
}
DI void dma16_sc1(const void* sbase, unsigned voff, unsigned lds) {
  asm volatile("s_mov_b32 m0, %2\n\ts_nop 0\n\tglobal_load_lds_dwordx4 %0, %1 sc1" ::"v"(voff), "s"(sbase), "s"(lds) : "memory");
}
template <int IMM, bool SC1> DI void gload16(bf16x8& dst, const void* sbase, unsigned voff) {
  if constexpr (SC1) asm volatile("global_load_dwordx4 %0, %1, %2 offset:%3 sc1" : "=v"(dst) : "v"(voff), "s"(sbase), "i"(IMM) : "memory");
  else asm volatile("global_load_dwordx4 %0, %1, %2 offset:%3" : "=v"(dst) : "v"(voff), "s"(sbase), "i"(IMM) : "memory");
}
template <int IMM, bool SC1 = false> DI void gload8(u32x2& dst, const void* sbase, unsigned voff) {
  if constexpr (SC1) asm volatile("global_load_dwordx2 %0, %1, %2 offset:%3 sc1" : "=v"(dst) : "v"(voff), "s"(sbase), "i"(IMM) : "memory");
  else asm volatile("global_load_dwordx2 %0, %1, %2 offset:%3" : "=v"(dst) : "v"(voff), "s"(sbase), "i"(IMM) : "memory");
}
template <bool SC1> DI void gload4(float& dst, const void* sbase, unsigned voff) {
  if constexpr (SC1) asm volatile("global_load_dword %0, %1, %2 sc1" : "=v"(dst) : "v"(voff), "s"(sbase) : "memory");
  else asm volatile("global_load_dword %0, %1, %2" : "=v"(dst) : "v"(voff), "s"(sbase) : "memory");
}
typedef __attribute__((ext_vector_type(4))) unsigned v4u_;
DI __amdgpu_buffer_rsrc_t make_rsrc(void* p, unsigned bytes) { return __builtin_amdgcn_make_buffer_rsrc(p, 0, bytes, 0x00020000); }
template <bool SC1> DI void st16(__amdgpu_buffer_rsrc_t r, unsigned off, bf16x8 v) { __builtin_amdgcn_raw_buffer_store_b128(__builtin_bit_cast(v4u_, v), r, off, 0, SC1 ? 16 : 0); }
template <bool SC1> DI void st8(__amdgpu_buffer_rsrc_t r, unsigned off, u32x2 v) { __builtin_amdgcn_raw_buffer_store_b64(v, r, off, 0, SC1 ? 16 : 0); }
template <bool SC1> DI void st4(__amdgpu_buffer_rsrc_t r, unsigned off, unsigned v) { __builtin_amdgcn_raw_buffer_store_b32(v, r, off, 0, SC1 ? 16 : 0); }

template <int N> DI void wait_bar() { asm volatile("s_waitcnt vmcnt(%0) lgkmcnt(0)\n\ts_barrier" ::"i"(N) : "memory"); }
DI void lds_bar() { asm volatile("s_waitcnt lgkmcnt(0)\n\ts_barrier" ::: "memory"); }
DI s16x4 trread(const LAS char* p) {
  typedef short v4i16_t __attribute__((ext_vector_type(4)));
  return __builtin_bit_cast(s16x4, __builtin_amdgcn_ds_read_tr16_b64_v4i16((LAS v4i16_t*)p));
}
DI bf16x8 cat8(s16x4 a, s16x4 b) { return __builtin_shufflevector(a, b, 0, 1, 2, 3, 4, 5, 6, 7); }
DI bf16x8 pack8(float a0, float a1, float a2, float a3, float a4, float a5, float a6, float a7) {
  u32x4 w = {cvtpk(a0, a1), cvtpk(a2, a3), cvtpk(a4, a5), cvtpk(a6, a7)};
  return __builtin_bit_cast(bf16x8, w);
}
template <int CTRL> DI float dpp(float v) { return __uint_as_float(__builtin_amdgcn_update_dpp(0u, __float_as_uint(v), CTRL, 0xf, 0xf, true)); }
DI float sum8(float v) { v += dpp<0xB1>(v); v += dpp<0x4E>(v); v += dpp<0x141>(v); return v; }
DI float sumx32(float v) { auto r = __builtin_amdgcn_permlane32_swap(__float_as_uint(v), __float_as_uint(v), false, false); return __uint_as_float(r[0]) + __uint_as_float(r[1]); }
DI float maxx32(float v) { auto r = __builtin_amdgcn_permlane32_swap(__float_as_uint(v), __float_as_uint(v), false, false); return fmaxf(__uint_as_float(r[0]), __uint_as_float(r[1])); }
DI float maxx16(float v) { auto r = __builtin_amdgcn_permlane16_swap(__float_as_uint(v), __float_as_uint(v), false, false); return fmaxf(__uint_as_float(r[0]), __uint_as_float(r[1])); }
DI float max16(float v) { v = fmaxf(v, dpp<0xB1>(v)); v = fmaxf(v, dpp<0x4E>(v)); v = fmaxf(v, dpp<0x141>(v)); v = fmaxf(v, dpp<0x140>(v)); return v; }

DI void publish(char* ws, unsigned flag_off, unsigned epoch) {
  asm volatile("s_waitcnt vmcnt(0) lgkmcnt(0)\n\ts_barrier" ::: "memory");
  if (threadIdx.x == 0) __hip_atomic_store((GAS unsigned*)(ws + flag_off), epoch, __ATOMIC_RELAXED, __HIP_MEMORY_SCOPE_AGENT);
}
DI void await(char* ws, unsigned flag_off, int n, unsigned epoch) {
  if (threadIdx.x >= 448) {
    const int l = threadIdx.x & 63;
    GAS unsigned* f = (GAS unsigned*)(ws + flag_off + (l < n ? l : 0) * 64);
    for (unsigned spins = 0; spins < (1u << 20); ++spins) {
      const unsigned v = __hip_atomic_load(f, __ATOMIC_RELAXED, __HIP_MEMORY_SCOPE_AGENT);
      if (__all(v == epoch)) break;
      __builtin_amdgcn_s_sleep(1);
    }
  }
  asm volatile("s_waitcnt lgkmcnt(0)\n\ts_barrier" ::: "memory");
}

DI int d_rb(int b) { return 4 * ((b & 7) >> 1) + ((b >> 3) & 3); }
DI int d_cb(int b) { return 7 * (b & 1) + (b >> 5); }

struct Ctx { LAS char* lds; unsigned lds0; int tid, l, w, hh, b; unsigned epoch; };

DI void phaseA(const Params& p, Ctx& c) {
  char* ws = p.ws;
  LAS char* lds = c.lds;
  const int l = c.l, w = c.w, hh = c.hh, b = c.b;
  const int rb = 8 * ((b & 7) >> 1) + ((b >> 3) & 7), hg = 4 * (b & 1) + (b >> 6);
  bf16x8 ft[3] = {};
  {
    unsigned long long pc; unsigned len;
    asm volatile("s_getpc_b64 %0\n.Lmega_here%=:\n\ts_mov_b32 %1, mega_code_end-.Lmega_here%=" : "=s"(pc), "=s"(len));
    const unsigned j = (unsigned)(b >> 3);
    const unsigned oc = j * 1024u + (w & 1) * 32768u + l * 16u;
    ft[2] = *(const GAS bf16x8*)((const char*)pc + (w < 2 && oc + 16 <= len ? oc : 0u));
    ft[0] = *(const GAS bf16x8*)((const char*)p.x + (size_t)(512 * ((b & 7) >> 1) + 256 * (b & 1) + 8 * j + w) * 1024 + l * 16);
    { const unsigned pair = 2u * (w < 6 ? w : 5) + (l >> 5);
      ft[1] = *(const GAS bf16x8*)((const char*)p.wqkv + (size_t)(128 * ((b & 7) >> 1) + 4 * j + pair / 3) * 3072 + (pair % 3) * 1024 + (b & 1) * 512 + (l & 31) * 16); }
  }
  const int q4 = (l & 15) >> 2, p4 = l & 3, blk = (l >> 4) & 1;
  LAS float* tmean = (LAS float*)(lds + 0);
  LAS float* trstd = tmean + 64;
  LAS float* tcs = tmean + 128;
  f32x16 acc[3] = {};
  const int m = w & 1, g = (w >> 1) & 1;
  if (w < 4) {
    const bf16x8 sel = rowsum_sel(l);
    const int swz = (l >> 1) & 7;
    const int xrow = (32 * m + (l & 31)) * 128;
    const int wa = 8192 + (8 * hh + q4) * 128 + (((4 * g + 2 * blk + (p4 >> 1)) ^ (((q4 >> 1) & 1) << 2)) * 16) + (p4 & 1) * 8;
    int xo[4], wo_[2];
#pragma unroll
    for (int i = 0; i < 4; ++i) xo[i] = xrow + (((2 * (i ^ m) + hh) ^ swz) * 16);
    wo_[0] = wa + m * 2048; wo_[1] = wa + (1 - m) * 2048;
    bf16x8 xf[2][4], wf[2][4][3];
    f32x4 cs[3] = {};
    auto ld_w = [&](auto SET, const LAS char* sl, int i, int sgm) { constexpr int q = SET.value; const int o = wo_[i & 1] + (i & 2) * 2048 + sgm * 8192;
      wf[q][i][sgm] = cat8(trread(sl + o), trread(sl + o + 512)); };
    lds_bar();
#pragma unroll
    for (int i = 0; i < 4; ++i) { xf[0][i] = *(const LAS bf16x8*)(lds + xo[i]); for (int sgm = 0; sgm < 3; ++sgm) ld_w(IC<0>{}, lds, i, sgm); }
    sfor<0, 8>([&](auto ST) {
      constexpr int st = ST.value, q = st & 1, qn = (st + 1) & 1;
      if constexpr (st + 1 < 8) lds_bar(); else asm volatile("s_waitcnt lgkmcnt(0)" ::: "memory");
      asm volatile("" : "+v"(xf[q][0]), "+v"(xf[q][1]), "+v"(xf[q][2]), "+v"(xf[q][3]), "+v"(wf[q][0][0]), "+v"(wf[q][0][1]), "+v"(wf[q][0][2]), "+v"(wf[q][1][0]), "+v"(wf[q][1][1]), "+v"(wf[q][1][2]),
                   "+v"(wf[q][2][0]), "+v"(wf[q][2][1]), "+v"(wf[q][2][2]), "+v"(wf[q][3][0]), "+v"(wf[q][3][1]), "+v"(wf[q][3][2]));
      const LAS char* sn = lds + ((st + 1) % 5) * 32768;
#pragma unroll
      for (int i = 0; i < 4; ++i) {
        acc[0] = MFMA(wf[q][i][0], xf[q][i], acc[0]);
        SBAR();
        if constexpr (st + 1 < 8) { ld_w(IC<qn>{}, sn, i, 0); xf[qn][i] = *(const LAS bf16x8*)(sn + xo[i]); }
        SBAR();
        acc[1] = MFMA(wf[q][i][1], xf[q][i], acc[1]);
        SBAR();
        if constexpr (st + 1 < 8) ld_w(IC<qn>{}, sn, i, 1);
        SBAR();
        acc[2] = MFMA(xf[q][i], wf[q][i][2], acc[2]);
        SBAR();
        if constexpr (st + 1 < 8) ld_w(IC<qn>{}, sn, i, 2);
        if ((i & 1) == 0) { cs[0] = MFMA16(wf[q][i][0], sel, cs[0]); cs[1] = MFMA16(wf[q][i][1], sel, cs[1]); cs[2] = MFMA16(wf[q][i][2], sel, cs[2]); }
        SBAR();
      }
    });
    if ((l & 15) < 2) {
#pragma unroll
      for (int t = 0; t < 3; ++t) *(LAS f32x4*)(tcs + (w * 3 + t) * 32 + 16 * (l & 15) + 4 * (l >> 4)) = cs[t];
    }
  } else {
    const int h = w - 4;
    const unsigned xo0 = (unsigned)(64 * rb + 16 * h + (l >> 3)) * 1024u + (unsigned)(((l & 7) ^ ((l >> 4) & 7)) * 16);
    const unsigned xo1 = (unsigned)(64 * rb + 16 * h + 8 + (l >> 3)) * 1024u + (unsigned)(((l & 7) ^ ((4 + (l >> 4)) & 7)) * 16);
    const unsigned wof = (unsigned)(16 * h + (l >> 3)) * 3072u + (unsigned)hg * 128u + (unsigned)(((l & 7) ^ (((l >> 4) & 1) << 2)) * 16);
    auto issue = [&](int st) {
      const unsigned dst = c.lds0 + (st % 5) * 32768 + h * 2048;
      const char* wsrc = (const char*)p.wqkv + (size_t)st * 64 * 3072;
      dma16((const char*)p.x + st * 128, xo0, dst);
      dma16((const char*)p.x + st * 128, xo1, dst + 1024);
#pragma unroll
      for (int sgm = 0; sgm < 3; ++sgm) { dma16(wsrc + sgm * 1024, wof, dst + 8192 + sgm * 8192); dma16(wsrc + sgm * 1024 + 8 * 3072, wof, dst + 8192 + sgm * 8192 + 1024); }
    };
    issue(0); issue(1); issue(2); issue(3); issue(4);
    f32x2 rs[2] = {}, rq[2] = {};
    u32x4 sv[2][2];
    wait_bar<32>();
    sv[0][0] = *(const LAS u32x4*)(lds + h * 2048 + l * 16); sv[0][1] = *(const LAS u32x4*)(lds + h * 2048 + 1024 + l * 16);
    sfor<0, 8>([&](auto ST) {
      constexpr int st = ST.value, q = st & 1, qn = (st + 1) & 1;
      if constexpr (st + 1 < 8) {
        constexpr int last = 4 + st > 7 ? 7 : 4 + st;
        wait_bar<8 * (last - (st + 1))>();
        if constexpr (st + 5 < 8) issue(st + 5);
        const LAS char* sn = lds + ((st + 1) % 5) * 32768 + h * 2048 + l * 16;
        sv[qn][0] = *(const LAS u32x4*)sn; sv[qn][1] = *(const LAS u32x4*)(sn + 1024);
      }
#pragma unroll
      for (int r = 0; r < 2; ++r)
#pragma unroll
        for (int i = 0; i < 4; ++i) { const f32x2 v = unpk(sv[q][r][i]); rs[r] += v; rq[r] = __builtin_elementwise_fma(v, v, rq[r]); }
      asm volatile("" : "+v"(rs[0]), "+v"(rq[0]), "+v"(rs[1]), "+v"(rq[1]));
    });
#pragma unroll
    for (int r = 0; r < 2; ++r) {
      const float s1 = sum8(rs[r][0] + rs[r][1]), s2 = sum8(rq[r][0] + rq[r][1]);
      if ((l & 7) == 0) { const float mean = s1 * (1.f / D); const int row = 16 * h + 8 * r + (l >> 3);
        tmean[row] = mean; trstd[row] = __builtin_amdgcn_rsqf(fmaxf(s2 * (1.f / D) - mean * mean, 0.f) + LN_EPS); }
    }
  }
  { asm volatile("" : "+v"(c.epoch), "+v"(ft[0]), "+v"(ft[1]), "+v"(ft[2])); c.epoch += 1u; }
  lds_bar();
  const __amdgpu_buffer_rsrc_t rw = make_rsrc(ws, WS_USED);
  if (w < 4) {
    const int tile = 2 * rb + m;
    const float mean_l = tmean[32 * m + (l & 31)], rstd_l = trstd[32 * m + (l & 31)];
    f32x4 cv[2][4], mm[4], rr[4];
#pragma unroll
    for (int gg = 0; gg < 4; ++gg) {
#pragma unroll
      for (int t = 0; t < 2; ++t) cv[t][gg] = *(const LAS f32x4*)(tcs + (w * 3 + t) * 32 + 8 * gg + 4 * hh) + *(const LAS f32x4*)(tcs + ((w ^ 1) * 3 + t) * 32 + 8 * gg + 4 * hh);
      mm[gg] = *(const LAS f32x4*)(tmean + 32 * m + 8 * gg + 4 * hh); rr[gg] = *(const LAS f32x4*)(trstd + 32 * m + 8 * gg + 4 * hh);
    }
    const int n = l & 31;
    const float ncs = -(tcs[(w * 3 + 2) * 32 + n] + tcs[((w ^ 1) * 3 + 2) * 32 + n]);
#pragma unroll
    for (int s = 0; s < 2; ++s) {
      f32x2 v[4];
#pragma unroll
      for (int j = 0; j < 4; ++j) { const int r = 8 * s + 2 * j; const f32x2 a = {acc[2][r], acc[2][r + 1]}, m2 = {mm[r >> 2][r & 3], mm[r >> 2][(r & 3) + 1]}, r2 = {rr[r >> 2][r & 3], rr[r >> 2][(r & 3) + 1]};
        v[j] = __builtin_elementwise_fma(m2, f32x2{ncs, ncs}, a) * r2; }
      st16<true>(rw, WS_KV + (unsigned)((4 * hg + 2 * g + (n >> 4)) * 64 + tile) * 2048 + 1024 + s * 512 + hh * 256 + (n & 15) * 16,
               pack8(v[0][0], v[0][1], v[1][0], v[1][1], v[2][0], v[2][1], v[3][0], v[3][1]));
    }
#pragma unroll
    for (int t = 1; t >= 0; --t) {
      const float sc = t == 0 ? rstd_l * QSCALE : rstd_l, nm = -mean_l * sc;
#pragma unroll
      for (int s = 0; s < 2; ++s) {
        f32x2 v[4];
#pragma unroll
        for (int j = 0; j < 4; ++j) { const int r = 8 * s + 2 * j; const f32x2 a = {acc[t][r], acc[t][r + 1]}, cc = {cv[t][r >> 2][r & 3], cv[t][r >> 2][(r & 3) + 1]};
          v[j] = __builtin_elementwise_fma(a, f32x2{sc, sc}, cc * nm); }
        const int head = 4 * hg + 2 * g + s;
        st16<true>(rw, (t == 0 ? WS_Q + (unsigned)(head * 64 + tile) * 1024 : WS_KV + (unsigned)(head * 64 + tile) * 2048) + l * 16, pack8(v[0][0], v[0][1], v[1][0], v[1][1], v[2][0], v[2][1], v[3][0], v[3][1]));
        if (t == 1) {
          f32x2 n2 = v[0] * v[0]; n2 = __builtin_elementwise_fma(v[1], v[1], n2); n2 = __builtin_elementwise_fma(v[2], v[2], n2); n2 = __builtin_elementwise_fma(v[3], v[3], n2);
          const float mx = maxx16(max16(sumx32(n2[0] + n2[1])));
          if (l == 0) st4<true>(rw, WS_KMAX + (unsigned)(head * 64 + tile) * 4, __float_as_uint(mx));
        }
      }
    }
  }
  publish(ws, FL_A + (hg * 32 + rb) * 64, c.epoch);
}

DI unsigned wpiece_lane(int l, unsigned row_bytes) { return (unsigned)(l >> 3) * row_bytes + (unsigned)(((l & 7) ^ (((l >> 4) & 1) << 2)) * 16); }
template <int NW, int W0 = 0> DI void prefetch_wo(const Params& p, const Ctx& c) {
  if (c.w < W0 || c.w >= W0 + NW) return;
  const unsigned off = wpiece_lane(c.l, 1024) + (unsigned)(c.b >> 5) * 128u;
  for (int pc = c.w - W0; pc < 64; pc += NW) dma16((const char*)p.wo + (size_t)(8 * pc) * 1024, off, c.lds0 + pc * 1024);
}
DI constexpr unsigned w1_stage_base(int j) { return j < 6 ? 65536 + j * 16384 : (j - 6) * 16384; }
template <int NW, int W0 = 0> DI void prefetch_w1(const Params& p, const Ctx& c, int p0, int p1) {
  if (c.w < W0 || c.w >= W0 + NW) return;
  const unsigned off = wpiece_lane(c.l, F * 2) + (unsigned)d_cb(c.b) * 256u;
  for (int pc = p0 + c.w - W0; pc < p1; pc += NW) {
    const int j = pc >> 4, seg = (pc >> 3) & 1, r8 = pc & 7;
    dma16((const char*)p.w1 + (size_t)(64 * j + 8 * r8) * (F * 2) + seg * 128, off, c.lds0 + w1_stage_base(j) + seg * 8192 + r8 * 1024);
  }
}
template <int NW> DI void prefetch_w2(const Params& p, const Ctx& c, int p0, int p1) {
  if (c.w >= NW) return;
  const unsigned off = wpiece_lane(c.l, 1024) + (unsigned)(c.b >> 5) * 128u;
  for (int pc = p0 + c.w; pc < p1; pc += NW) {
    const int st = pc >> 4, kq = (pc >> 2) & 3, r8 = pc & 3;
    dma16((const char*)p.w2 + (size_t)(448 * kq + 32 * st + 8 * r8) * 1024, off, c.lds0 + ((st + 4) % 10) * 16384 + kq * 4096 + r8 * 1024);
  }
}

DI void warm_first(const Params& p, const Ctx& c) {
  const int h = c.w - 4;
  const unsigned off1 = (unsigned)c.b * 7168u + (unsigned)h * 2048u + (unsigned)c.l * 16u;
  dma16((const char*)p.w1, off1, c.lds0 + 139520);
  dma16((const char*)p.w2, off1, c.lds0 + 139520);
  if (h < 3) { dma16((const char*)p.w1 + 1024, off1, c.lds0 + 139520); dma16((const char*)p.w2 + 1024, off1, c.lds0 + 139520); }
}
DI void warm_w1_late(const Params& p, const Ctx& c) {
  const int h = c.w - 4;
  const unsigned j = (unsigned)(c.b >> 3);
  for (int pc = h; pc < 35; pc += 4)
    dma16((const char*)p.w1, (pc < 21 ? 128u * 3584u + j * 21504u + pc * 1024u : 384u * 3584u + j * 14336u + (pc - 21) * 1024u) + c.l * 16u, c.lds0 + 139520);
}
DI void warm_w2(const Params& p, const Ctx& c) {
  const int h = c.w - 4;
  const unsigned off = (unsigned)(c.b >> 3) * 57344u + (unsigned)h * 14336u + (unsigned)c.l * 16u;
#pragma unroll
  for (int i = 0; i < 14; ++i) dma16((const char*)p.w2 + i * 1024, off, c.lds0 + 139520);
}

constexpr int B_ONES = 131072;
DI void phaseB(const Params& p, const Ctx& c) {
  char* ws = p.ws;
  LAS char* lds = c.lds;
  const int l = c.l, w = c.w, hh = c.hh, b = c.b, tid = c.tid;
  const int head = 4 * (b & 7) + ((b >> 3) & 3), qb = b >> 5;
  for (int i = tid; i < (4096 + 512) / 16; i += 512) { const unsigned v = (i & 31) == 0 ? ONES2 : 0u; ((LAS u32x4*)(lds + B_ONES))[i] = u32x4{v, v, v, v}; }
  await(ws, FL_A + (head >> 2) * 32 * 64, 32, c.epoch);
  const unsigned l16 = l * 16;
  if (w >= 4) {
    const char* kvg = ws + WS_KV + (size_t)head * 131072;
    const int h = w - 4;
    auto piece = [&](int pc) { dma16_sc1(kvg + pc * 1024, l16, c.lds0 + pc * 1024); };
#pragma unroll
    for (int i = 0; i < 4; ++i) piece(4 * h + i);
#pragma unroll
    for (int i = 0; i < 12; ++i) piece(16 + 4 * i + h);
    wait_bar<12>();
#pragma unroll
    for (int i = 0; i < 16; ++i) piece(64 + 4 * i + h);
    wait_bar<24>();
    wait_bar<16>();
    warm_first(p, c);
    wait_bar<0>();
    if (b < 224) prefetch_w1<4, 4>(p, c, 80, 96);
    lds_bar();
    prefetch_wo<4, 4>(p, c);
    lds_bar();
    if (b < 224) prefetch_w1<4, 4>(p, c, 0, 32);
    warm_w2(p, c);
  } else {
    bf16x8 qa, qb_;
    float kmx;
    gload4<true>(kmx, ws + WS_KMAX + head * 256, l * 4);
    gload16<0, true>(qa, ws + WS_Q + (size_t)(head * 64 + qb * 8 + w) * 1024, l * 16);
    gload16<0, true>(qb_, ws + WS_Q + (size_t)(head * 64 + qb * 8 + w + 4) * 1024, l * 16);
    asm volatile("s_waitcnt vmcnt(0)" : "+v"(qa), "+v"(qb_), "+v"(kmx)::"memory");
    kmx = maxx32(maxx16(max16(kmx)));
    auto qbound = [&](bf16x8 q) { const u32x4 u = __builtin_bit_cast(u32x4, q); f32x2 t = {0.f, 0.f}; for (int i = 0; i < 4; ++i) { const f32x2 v = unpk(u[i]); t = __builtin_elementwise_fma(v, v, t); }
      return __builtin_amdgcn_sqrtf(sumx32(t[0] + t[1]) * kmx) * 1.01f + 1e-6f; };
    const float bna = qbound(qa), bnb = qbound(qb_);
    const bool safe = __all(bna <= BOUND_SAFE && bnb <= BOUND_SAFE);
    f32x16 oa = {}, ob = {};
    const int lo5 = l & 31;
    const bool isv = lo5 < 16;
    const int va0 = isv ? 1024 + hh * 256 + lo5 * 16 : B_ONES + (lo5 == 16 ? 0 : (lo5 >= 20 && lo5 < 28) ? 64 : 16);
    lds_bar();
    if (safe) {
      f32x16 nga, ngb; for (int i = 0; i < 16; ++i) { nga[i] = -bna; ngb[i] = -bnb; }
      f32x16 s0, s1;
      bf16x8 pa, pb = {};
      bf16x8 kf, v0, v1 = {};
      unsigned ka = c.lds0 + l * 16, va = c.lds0 + va0;
      const unsigned vstep = isv ? 4096u : 0u;
      asm volatile("ds_read_b128 %1, %3\n\tds_read_b128 %2, %4\n\ts_waitcnt lgkmcnt(0)\n\t"
                   "v_mfma_f32_32x32x16_bf16 %0, %1, %5, %6\n\ts_nop 7\n\ts_nop 3"
                   : "=&{v[192:207]}"(s0), "=&v"(kf), "=&v"(v0) : "v"(ka), "v"(va), "v"(qa), "v"(nga) : "memory");
#define E_(b, i) "v_exp_f32 v[" #b "+" #i "], v[" #b "+" #i "]\n\t"
#define C_(d, i, b, j, k) "v_cvt_pk_bf16_f32 v[" #d "+" #i "], v[" #b "+" #j "], v[" #b "+" #k "]\n\t"
#define MID_(SC) E_(SC, 4) E_(SC, 5) E_(SC, 6) E_(SC, 7) C_(224, 0, SC, 0, 1) C_(224, 1, SC, 2, 3) E_(SC, 8) E_(SC, 9) C_(224, 2, SC, 4, 5) C_(224, 3, SC, 6, 7) E_(SC, 10) E_(SC, 11)
#define END_(SC) E_(SC, 12) E_(SC, 13) E_(SC, 14) E_(SC, 15) C_(228, 0, SC, 8, 9) C_(228, 1, SC, 10, 11) C_(228, 2, SC, 12, 13) C_(228, 3, SC, 14, 15)
#define UNIT_A(KOFF, V1OFF) \
      "v_mfma_f32_32x32x16_bf16 v[208:223], %[kf], %[qb], %[ngb]\n\t" \
      "ds_read_b128 %[kf], %[ka] offset:" #KOFF "\n\t" \
      E_(192, 0) E_(192, 1) E_(192, 2) E_(192, 3) \
      "v_mfma_f32_32x32x16_bf16 %[ob], %[v1], v[228:231], %[ob]\n\t" \
      "ds_read_b128 %[v1], %[va] offset:" #V1OFF "\n\t" \
      MID_(192) \
      "s_waitcnt lgkmcnt(2)\n\t" \
      "v_mfma_f32_32x32x16_bf16 %[oa], %[v0], v[224:227], %[oa]\n\t" \
      END_(192)
#define UNIT_B(V0OFF) \
      "s_waitcnt lgkmcnt(0)\n\t" \
      "v_mfma_f32_32x32x16_bf16 v[192:207], %[kf], %[qa], %[nga]\n\t" \
      E_(208, 0) E_(208, 1) E_(208, 2) E_(208, 3) \
      "v_mfma_f32_32x32x16_bf16 %[oa], %[v1], v[228:231], %[oa]\n\t" \
      MID_(208) \
      "v_mfma_f32_32x32x16_bf16 %[ob], %[v0], v[224:227], %[ob]\n\t" \
      "ds_read_b128 %[v0], %[va] offset:" #V0OFF "\n\t" \
      END_(208)
#define RUN_GROUPS(NG) do { int cnt_ = (NG); \
      asm volatile("1:\n\t" UNIT_A(2048, 512) UNIT_B(2048) UNIT_A(4096, 2560) UNIT_B(4096) \
        "v_add_u32 %[ka], 0x1000, %[ka]\n\tv_add_u32 %[va], %[va], %[vs]\n\t" \
        "s_sub_u32 %[cnt], %[cnt], 1\n\ts_cmp_lg_u32 %[cnt], 0\n\ts_cbranch_scc1 1b\n\ts_waitcnt lgkmcnt(0)\n\ts_nop 7\n\ts_nop 3" \
        : "+{v[192:207]}"(s0), "=&{v[208:223]}"(s1), "=&{v[224:227]}"(pa), "+{v[228:231]}"(pb), [oa] "+v"(oa), [ob] "+v"(ob), [kf] "+v"(kf), [v0] "+v"(v0), [v1] "+v"(v1), \
          [ka] "+v"(ka), [va] "+v"(va), [cnt] "+s"(cnt_) \
        : [qa] "v"(qa), [qb] "v"(qb_), [nga] "v"(nga), [ngb] "v"(ngb), [vs] "v"(vstep) : "memory", "scc"); } while (0)
      RUN_GROUPS(3);
      lds_bar();
      RUN_GROUPS(4);
      lds_bar();
      RUN_GROUPS(8);
      lds_bar();
      RUN_GROUPS(1);
      lds_bar();
      RUN_GROUPS(8);
      lds_bar();
      RUN_GROUPS(8);
      ob = MFMA(v1, pb, ob);
    } else {
      float ca = -3.0e38f, cb = -3.0e38f;
      const f32x16 zero = {};
      auto online = [&](const bf16x8& kf, const bf16x8& v0, const bf16x8& v1, const bf16x8& q, f32x16& o, float& mrun) {
        f32x16 s = MFMA(kf, q, zero);
        float tm = s[0];
#pragma unroll
        for (int i = 1; i < 16; ++i) tm = fmaxf(tm, s[i]);
        tm = maxx32(tm);
        const float mn = fmaxf(mrun, tm), f = __builtin_amdgcn_exp2f(mrun - mn);
        mrun = mn;
#pragma unroll
        for (int i = 0; i < 16; ++i) { o[i] *= f; s[i] = __builtin_amdgcn_exp2f(s[i] - mn); }
        o = MFMA(v0, pack8(s[0], s[1], s[2], s[3], s[4], s[5], s[6], s[7]), o);
        o = MFMA(v1, pack8(s[8], s[9], s[10], s[11], s[12], s[13], s[14], s[15]), o);
      };
      for (int t = 0; t < 64; ++t) {
        if (t == 6 || t == 14 || t == 30 || t == 32 || t == 48) lds_bar();
        const bf16x8 kf = *(const LAS bf16x8*)(lds + l * 16 + t * 2048);
        const int vo = isv ? va0 + t * 2048 : va0;
        const bf16x8 v0 = *(const LAS bf16x8*)(lds + vo), v1 = *(const LAS bf16x8*)(lds + vo + 512);
        online(kf, v0, v1, qa, oa, ca);
        online(kf, v0, v1, qb_, ob, cb);
      }
    }
    const float ia = __builtin_amdgcn_rcpf(sumx32(oa[8])), ib = __builtin_amdgcn_rcpf(sumx32(ob[8]));
    const __amdgpu_buffer_rsrc_t rw = make_rsrc(ws, WS_USED);
    st16<true>(rw, WS_O + (unsigned)(((qb * 8 + w) * 32 + head) * 64 + l) * 16, pack8(oa[0] * ia, oa[1] * ia, oa[2] * ia, oa[3] * ia, oa[4] * ia, oa[5] * ia, oa[6] * ia, oa[7] * ia));
    st16<true>(rw, WS_O + (unsigned)(((qb * 8 + w + 4) * 32 + head) * 64 + l) * 16, pack8(ob[0] * ib, ob[1] * ib, ob[2] * ib, ob[3] * ib, ob[4] * ib, ob[5] * ib, ob[6] * ib, ob[7] * ib));
  }
  {
    publish(ws, FL_B + (qb * 32 + head) * 64, c.epoch);
    if (b < 224) prefetch_w1<7>(p, c, 32, 80);
  }
}

template <int KQ, int HGAP> DI void quarter_reduce_t(int m, int l, LAS f32x4* xch, const f32x16 (&acc)[2], float (&val)[8]) {
#pragma unroll
  for (int d = 0; d < 4; ++d) {
    if (d == KQ) continue;
    xch[(d >> 1) * HGAP + (((d * 4 + KQ) * 2 + m) * 2 + 0) * 64 + l] = f32x4{acc[d >> 1][8 * (d & 1)], acc[d >> 1][8 * (d & 1) + 1], acc[d >> 1][8 * (d & 1) + 2], acc[d >> 1][8 * (d & 1) + 3]};
    xch[(d >> 1) * HGAP + (((d * 4 + KQ) * 2 + m) * 2 + 1) * 64 + l] = f32x4{acc[d >> 1][8 * (d & 1) + 4], acc[d >> 1][8 * (d & 1) + 5], acc[d >> 1][8 * (d & 1) + 6], acc[d >> 1][8 * (d & 1) + 7]};
  }
  lds_bar();
  f32x4 t[4][2];
#pragma unroll
  for (int s = 0; s < 4; ++s) { if (s == KQ) continue; t[s][0] = xch[(KQ >> 1) * HGAP + (((KQ * 4 + s) * 2 + m) * 2 + 0) * 64 + l]; t[s][1] = xch[(KQ >> 1) * HGAP + (((KQ * 4 + s) * 2 + m) * 2 + 1) * 64 + l]; }
#pragma unroll
  for (int j = 0; j < 8; ++j) val[j] = acc[KQ >> 1][8 * (KQ & 1) + j];
#pragma unroll
  for (int s = 0; s < 4; ++s) { if (s == KQ) continue; for (int j = 0; j < 4; ++j) { val[j] += t[s][0][j]; val[4 + j] += t[s][1][j]; } }
}
template <int HGAP> DI void quarter_reduce(const Ctx& c, LAS f32x4* xch, const f32x16 (&acc)[2], float (&val)[8]) {
  const int m = c.w & 1, kq = c.w >> 1;
  if (kq == 0) quarter_reduce_t<0, HGAP>(m, c.l, xch, acc, val);
  else if (kq == 1) quarter_reduce_t<1, HGAP>(m, c.l, xch, acc, val);
  else if (kq == 2) quarter_reduce_t<2, HGAP>(m, c.l, xch, acc, val);
  else quarter_reduce_t<3, HGAP>(m, c.l, xch, acc, val);
}

DI void phaseC(const Params& p, const Ctx& c, u32x4& hkeep) {
  char* ws = p.ws;
  LAS char* lds = c.lds;
  const int l = c.l, w = c.w, hh = c.hh, b = c.b;
  const int rb = (b & 7) * 4 + ((b >> 3) & 3), cb = b >> 5;
  const int m = w & 1, kq = w >> 1;
  const int rt = 2 * rb + m, kstep = 4 * cb + kq;
  const GAS u32x2* xp = (const GAS u32x2*)((const char*)p.x + (size_t)rt * 32 * 1024 + kstep * 32 + (l & 31) * 1024 + hh * 8);
  const u32x2 xr0 = xp[0], xr1 = xp[2];
  const int q4 = (l & 15) >> 2, p4 = l & 3, blk = (l >> 4) & 1;
  const int wa0 = (128 * kq + 4 * hh + q4) * 128 + (((2 * blk + (p4 >> 1)) ^ (((q4 >> 1) & 1) << 2)) * 16) + (p4 & 1) * 8;
  const int wa1 = wa0 ^ 64;
  bf16x8 wf[8][2];
#pragma unroll
  for (int i = 0; i < 8; ++i) {
    wf[i][0] = cat8(trread(lds + wa0 + i * 2048), trread(lds + wa0 + i * 2048 + 1024));
    wf[i][1] = cat8(trread(lds + wa1 + i * 2048), trread(lds + wa1 + i * 2048 + 1024));
  }
  await(ws, FL_B + (rb >> 2) * 32 * 64, 32, c.epoch);
  const char* abase = ws + WS_O + ((size_t)rt * 32 + kq * 8) * 1024;
  bf16x8 af[8];
  sfor<0, 8>([&](auto I) { constexpr int i = I.value; gload16<(i & 3) * 1024, true>(af[i], abase + (i >> 2) * 4096, l * 16); });
  f32x16 acc[2] = {};
  sfor<0, 8>([&](auto I) {
    constexpr int i = I.value;
    asm volatile("s_waitcnt vmcnt(%1)" : "+v"(af[i]) : "i"(7 - i) : "memory");
    acc[0] = MFMA(wf[i][0], af[i], acc[0]);
    acc[1] = MFMA(wf[i][1], af[i], acc[1]);
    SBAR();
  });
  float val[8];
  quarter_reduce<0>(c, (LAS f32x4*)lds, acc, val);
  const u32x4 o = {cvtpk(val[0] + bf_lo(xr0[0]), val[1] + bf_hi(xr0[0])), cvtpk(val[2] + bf_lo(xr0[1]), val[3] + bf_hi(xr0[1])),
                   cvtpk(val[4] + bf_lo(xr1[0]), val[5] + bf_hi(xr1[0])), cvtpk(val[6] + bf_lo(xr1[1]), val[7] + bf_hi(xr1[1]))};
  hkeep = o;
  const __amdgpu_buffer_rsrc_t rw = make_rsrc(ws, WS_USED);
  st16<true>(rw, WS_HF + (unsigned)((rt * 32 + kstep) * 64 + l) * 16, __builtin_bit_cast(bf16x8, o));
  f32x2 sm = {0.f, 0.f}, sq = {0.f, 0.f};
#pragma unroll
  for (int i = 0; i < 4; ++i) { const f32x2 v = unpk(o[i]); sm += v; sq = __builtin_elementwise_fma(v, v, sq); }
  LAS f32x2* tst = (LAS f32x2*)(lds + 0);
  tst[((m * 4 + kq) * 2 + hh) * 32 + (l & 31)] = f32x2{sm[0] + sm[1], sq[0] + sq[1]};
  lds_bar();
  if (w < 2 && l < 32) {
    f32x2 t8[8];
#pragma unroll
    for (int i = 0; i < 8; ++i) t8[i] = tst[(w * 8 + i) * 32 + l];
    const f32x2 t = ((t8[0] + t8[1]) + (t8[2] + t8[3])) + ((t8[4] + t8[5]) + (t8[6] + t8[7]));
    st8<true>(rw, WS_STAT + (unsigned)(((2 * rb + w) * 8 + cb) * 32 + l) * 8, __builtin_bit_cast(u32x2, t));
  }
  publish(ws, FL_C + ((rb >> 1) * 16 + (rb & 1) * 8 + cb) * 64, c.epoch);
  if (b < 224) prefetch_w1<7>(p, c, 96, 128);
}

constexpr int D_WIN = 6;
DI void phaseD(const Params& p, const Ctx& c) {
  char* ws = p.ws;
  LAS char* lds = c.lds;
  const int l = c.l, w = c.w, hh = c.hh, b = c.b;
  const int rb = d_rb(b), cb = d_cb(b);
  const int m = w & 3, nh = w >> 2;
  const int rt = 4 * rb + m;
  LAS float* tcs = (LAS float*)(lds + 49152);
  LAS float* tb = tcs + 128;
  if (c.tid < 128) tcs[c.tid] = 0.f;
  const bf16x8 bv = *(const GAS bf16x8*)((const char*)p.b1 + cb * 256 + (l & 15) * 16);
  const int par = m >> 1;
  const int q4 = (l & 15) >> 2, p4 = l & 3, blk = (l >> 4) & 1;
  const int wl = nh * 8192 + (4 * hh + q4) * 128 + (((4 * (m & 1) + 2 * blk + (p4 >> 1)) ^ (((q4 >> 1) & 1) << 2)) * 16) + (p4 & 1) * 8;
  const int wa_e = wl + par * 2048, wa_o = wl + (1 - par) * 2048;
  bf16x8 wf[3][2];
  auto load_w = [&](auto I) {
    constexpr int i = I.value;
    constexpr int o = (int)w1_stage_base(i >> 2) + (i & 2) * 2048;
    const int wa = (i & 1) ? wa_o : wa_e;
    wf[i % 3][0] = cat8(trread(lds + wa + o), trread(lds + wa + o + 1024));
    wf[i % 3][1] = cat8(trread(lds + (wa ^ 64) + o), trread(lds + (wa ^ 64) + o + 1024));
  };
  load_w(IC<0>{}); load_w(IC<1>{});
  await(ws, FL_C + rb * 16 * 64, 16, c.epoch);
  const char* abase = ws + WS_HF + (size_t)rt * 32 * 1024;
  const unsigned aoff0 = l * 16 + par * 1024, aoff1 = l * 16 + (1 - par) * 1024;
  bf16x8 af[32];
  auto load_a = [&](auto I) { constexpr int i = I.value;
    if constexpr (i & 1) gload16<((i >> 1) & 1) * 2048, true>(af[i], abase + (i >> 2) * 4096, aoff1);
    else gload16<((i >> 1) & 1) * 2048, true>(af[i], abase + (i >> 2) * 4096, aoff0); };
  sfor<0, 4>(load_a);
  u32x2 st[4];
  sfor<0, 4>([&](auto I) { constexpr int i = I.value; gload8<i * 256, true>(st[i], ws + WS_STAT + (size_t)rt * 2048, (l & 31) * 8 + hh * 1024); });
  sfor<4, D_WIN>(load_a);
  f32x16 acc[2] = {};
  f32x4 cs = {};
  const bf16x8 sel = rowsum_sel(l);
  sfor<0, 32>([&](auto I) {
    constexpr int i = I.value;
    if constexpr (i + D_WIN < 32) load_a(IC<i + D_WIN>{});
    constexpr int issued = i + D_WIN < 32 ? i + D_WIN + 1 : 32;
    asm volatile("s_waitcnt vmcnt(%1)" : "+v"(af[i]) : "i"(issued - i - 1 + (i < 4 ? 4 : 0)) : "memory");
    if constexpr (i == 20) asm volatile("s_barrier" ::: "memory");
    acc[0] = MFMA(wf[i % 3][0], af[i], acc[0]);
    acc[1] = MFMA(wf[i % 3][1], af[i], acc[1]);
    if constexpr ((i & 1) == 0) cs = MFMA16(wf[i % 3][0], sel, cs);
    if constexpr (i + 2 < 32) { SBAR(); load_w(IC<i + 2>{}); }
    SBAR();
  });
  if ((l & 15) < 2) {
    LAS float* d = tcs + nh * 64 + (m & 1) * 32 + 16 * (l & 15) + 4 * (l >> 4);
#pragma unroll
    for (int j = 0; j < 4; ++j) __hip_atomic_fetch_add(d + j, cs[j], __ATOMIC_RELAXED, __HIP_MEMORY_SCOPE_WORKGROUP);
  }
  if (w == 0 && l < 16) { const u32x4 u = __builtin_bit_cast(u32x4, bv);
    *(LAS f32x4*)(tb + l * 8) = f32x4{bf_lo(u[0]), bf_hi(u[0]), bf_lo(u[1]), bf_hi(u[1])}; *(LAS f32x4*)(tb + l * 8 + 4) = f32x4{bf_lo(u[2]), bf_hi(u[2]), bf_lo(u[3]), bf_hi(u[3])}; }
  asm volatile("" : "+v"(st[0]), "+v"(st[1]), "+v"(st[2]), "+v"(st[3]));
  const f32x2 ss = __builtin_bit_cast(f32x2, st[0]) + __builtin_bit_cast(f32x2, st[1]) + __builtin_bit_cast(f32x2, st[2]) + __builtin_bit_cast(f32x2, st[3]);
  const float sm = sumx32(ss[0]), sq = sumx32(ss[1]);
  const float mean = sm * (1.f / D), rstd = __builtin_amdgcn_rsqf(fmaxf(sq * (1.f / D) - mean * mean, 0.f) + LN_EPS);
  const float nmr = -mean * rstd;
  lds_bar();
  const __amdgpu_buffer_rsrc_t rw = make_rsrc(ws, WS_USED);
#pragma unroll
  for (int t = 0; t < 2; ++t) {
    const int ct = 2 * nh + ((m & 1) ^ t);
    f32x4 cv[4], bb[4];
#pragma unroll
    for (int g = 0; g < 4; ++g) { cv[g] = *(const LAS f32x4*)(tcs + 32 * ct + 8 * g + 4 * hh); bb[g] = *(const LAS f32x4*)(tb + 32 * ct + 8 * g + 4 * hh); }
#pragma unroll
    for (int s = 0; s < 2; ++s) {
      unsigned pk[4];
#pragma unroll
      for (int j = 0; j < 4; ++j) {
        const int r = 8 * s + 2 * j;
        const f32x2 a = {acc[t][r], acc[t][r + 1]}, c2 = {cv[r >> 2][r & 3], cv[r >> 2][(r & 3) + 1]}, b2 = {bb[r >> 2][r & 3], bb[r >> 2][(r & 3) + 1]};
        const f32x2 y = __builtin_elementwise_fma(a, f32x2{rstd, rstd}, __builtin_elementwise_fma(c2, f32x2{nmr, nmr}, b2));
        const f32x2 z = y * LOG2E;
        const f32x2 u = {__builtin_amdgcn_exp2f(z[0]), __builtin_amdgcn_exp2f(z[1])};
        const f32x2 d = __builtin_elementwise_fma(u, u + 2.f, f32x2{2.f, 2.f});
        const f32x2 r2 = {__builtin_amdgcn_rcpf(d[0]), __builtin_amdgcn_rcpf(d[1])};
        const f32x2 th = __builtin_elementwise_fma(r2, f32x2{-2.f, -2.f}, f32x2{1.f, 1.f});
        const f32x2 o = y * th;
        pk[j] = cvtpk(o[0], o[1]);
      }
      st16<true>(rw, WS_ACT + (unsigned)((rt * 112 + cb * 8 + ct * 2 + s) * 64 + l) * 16, __builtin_bit_cast(bf16x8, u32x4{pk[0], pk[1], pk[2], pk[3]}));
      prefetch_w2<8>(p, c, 24 * (2 * t + s), 24 * (2 * t + s) + 24);
    }
  }
  publish(ws, FL_D + (rb * 16 + cb) * 64, c.epoch);
}

constexpr int E_NSLOT = 10, E_NST = 14, E_WIN = 6, E_PRE = 8;
struct ESched {
  int posA[28], posW[E_NST], n_top[E_NST], n_mid[E_NST];
  constexpr ESched() : posA(), posW(), n_top(), n_mid() {
    int n = 0;
    for (int i = 0; i < E_WIN; ++i) posA[i] = n++;
    for (int st = 0; st < E_NST; ++st) {
      n_top[st] = n;
      if (E_PRE + st < E_NST) { n += 2; posW[E_PRE + st] = n - 1; }
      for (int j = 0; j < 2; ++j) if (E_WIN + 2 * st + j < 28) posA[E_WIN + 2 * st + j] = n++;
      n_mid[st] = n;
    }
  }
};
DI void phaseE(const Params& p, const Ctx& c, u32x4 hkeep) {
  constexpr ESched sch{};
  char* ws = p.ws;
  LAS char* lds = c.lds;
  const int l = c.l, w = c.w, hh = c.hh, b = c.b;
  const int rb = (b & 7) * 4 + ((b >> 3) & 3), cb = b >> 5;
  const int m = w & 1, kq = w >> 1;
  const int rt = 2 * rb + m, kstep = 4 * cb + kq;
  const bool early = b < 224;
  if (early) prefetch_w2<7>(p, c, 16 * 6, 16 * 8); else prefetch_w2<7>(p, c, 0, 16 * E_PRE);
  bf16x8 resv = __builtin_bit_cast(bf16x8, hkeep);
  const int q4 = (l & 15) >> 2, p4 = l & 3, blk = (l >> 4) & 1;
  const int wa0 = kq * 4096 + (4 * hh + q4) * 128 + (((2 * blk + (p4 >> 1)) ^ (((q4 >> 1) & 1) << 2)) * 16) + (p4 & 1) * 8;
  const int wa1 = wa0 ^ 64;
  bf16x8 wf[3][2][2];
  auto load_w = [&](auto ST) {
    constexpr int st = ST.value;
    const LAS char* sl = lds + ((st + 4) % E_NSLOT) * 16384;
#pragma unroll
    for (int i = 0; i < 2; ++i) {
      wf[st % 3][i][0] = cat8(trread(sl + wa0 + i * 2048), trread(sl + wa0 + i * 2048 + 1024));
      wf[st % 3][i][1] = cat8(trread(sl + wa1 + i * 2048), trread(sl + wa1 + i * 2048 + 1024));
    }
  };
  if (!early) wait_bar<0>();
  load_w(IC<0>{}); load_w(IC<1>{});
  await(ws, FL_D + (rb >> 1) * 16 * 64, 14, c.epoch);
  const char* abase = ws + WS_ACT + ((size_t)rt * 112 + kq * 28) * 1024;
  bf16x8 af[28];
  auto load_a = [&](auto I) { constexpr int i = I.value; gload16<(i & 3) * 1024, true>(af[i], abase + (i >> 2) * 4096, l * 16); };
  sfor<0, E_WIN>(load_a);
  f32x16 acc[2] = {};
  sfor<0, E_NST>([&](auto ST) {
    constexpr int st = ST.value;
    if constexpr (st == 2) asm volatile("s_waitcnt lgkmcnt(0)\n\ts_barrier" ::: "memory");
    if constexpr (st == 6 || st == 8 || st == 10) asm volatile("s_waitcnt vmcnt(%0) lgkmcnt(0)\n\ts_barrier" ::"i"(sch.n_top[st] - sch.posW[st + 3] - 1) : "memory");
    if constexpr (E_PRE + st < E_NST) prefetch_w2<8>(p, c, 16 * (st + E_PRE), 16 * (st + E_PRE + 1));
    if constexpr (E_WIN + 2 * st < 28) load_a(IC<E_WIN + 2 * st>{});
    if constexpr (E_WIN + 2 * st + 1 < 28) load_a(IC<E_WIN + 2 * st + 1>{});
    asm volatile("s_waitcnt vmcnt(%2)" : "+v"(af[2 * st]), "+v"(af[2 * st + 1]) : "i"(sch.n_mid[st] - sch.posA[2 * st + 1] - 1) : "memory");
    acc[0] = MFMA(wf[st % 3][0][0], af[2 * st], acc[0]);
    acc[1] = MFMA(wf[st % 3][0][1], af[2 * st], acc[1]);
    if constexpr (st + 2 < E_NST) { SBAR(); load_w(IC<st + 2>{}); SBAR(); }
    acc[0] = MFMA(wf[st % 3][1][0], af[2 * st + 1], acc[0]);
    acc[1] = MFMA(wf[st % 3][1][1], af[2 * st + 1], acc[1]);
    SBAR();
  });
  { asm volatile("" : "+v"(resv));
    const u32x4 ru = __builtin_bit_cast(u32x4, resv);
#pragma unroll
    for (int k = 0; k < 4; ++k) { const float on = kq == k ? 1.f : 0.f;
#pragma unroll
      for (int j = 0; j < 4; ++j) { acc[k >> 1][8 * (k & 1) + 2 * j] += on * bf_lo(ru[j]); acc[k >> 1][8 * (k & 1) + 2 * j + 1] += on * bf_hi(ru[j]); } } }
  constexpr int PS = 272, PQ = 64 * PS;
  LAS char* xa = lds;
#pragma unroll
  for (int ct = 0; ct < 2; ++ct)
#pragma unroll
    for (int g = 0; g < 4; ++g)
      *(LAS f32x4*)(xa + kq * PQ + (32 * m + (l & 31)) * PS + (32 * ct + 8 * g + 4 * hh) * 4) = f32x4{acc[ct][4 * g], acc[ct][4 * g + 1], acc[ct][4 * g + 2], acc[ct][4 * g + 3]};
  lds_bar();
  { const int row = c.tid >> 3, c8 = c.tid & 7;
    f32x4 t0[4], t1[4];
#pragma unroll
    for (int q = 0; q < 4; ++q) { t0[q] = *(const LAS f32x4*)(xa + q * PQ + row * PS + c8 * 32); t1[q] = *(const LAS f32x4*)(xa + q * PQ + row * PS + c8 * 32 + 16); }
    const f32x4 s0 = (t0[0] + t0[1]) + (t0[2] + t0[3]), s1 = (t1[0] + t1[1]) + (t1[2] + t1[3]);
    const __amdgpu_buffer_rsrc_t ro = make_rsrc(p.out, S * D * 2);
    st16<false>(ro, (unsigned)(rb * 64 + row) * 1024u + cb * 128 + c8 * 16, pack8(s0[0], s0[1], s0[2], s0[3], s1[0], s1[1], s1[2], s1[3])); }
}

DI Ctx make_ctx(char* lds_g) {
  Ctx c;
  c.lds = (LAS char*)lds_g; c.lds0 = (unsigned)(uintptr_t)lds_g;
  c.tid = threadIdx.x; c.l = c.tid & 63; c.w = rfl(c.tid >> 6); c.hh = c.l >> 5; c.b = blockIdx.x; c.epoch = 0;
  return c;
}

__global__ __launch_bounds__(512, 2) void block_mega(Params p) {
  extern __shared__ __attribute__((aligned(16))) char lds_g[];
  Ctx c = make_ctx(lds_g);
  c.epoch = __hip_atomic_load((GAS unsigned*)(p.ws + WS_FLAG) + (threadIdx.x >> 12), __ATOMIC_RELAXED, __HIP_MEMORY_SCOPE_AGENT);
  phaseA(p, c);
  phaseB(p, c);
  u32x4 hkeep;
  phaseC(p, c, hkeep);
  if (c.b < 224) phaseD(p, c);
  phaseE(p, c, hkeep);
  if (c.b == 0 && c.tid == 0) __hip_atomic_store((GAS unsigned*)(p.ws + WS_FLAG), c.epoch, __ATOMIC_RELAXED, __HIP_MEMORY_SCOPE_AGENT);
  asm volatile("mega_code_end:");
}

extern "C" void kernel_launch(void* const* d_in, const int* in_sizes, int n_in, void* d_out, int out_size, void* d_ws, size_t ws_size, hipStream_t stream) {
  Params p;
  p.x = (const u16*)d_in[0]; p.wqkv = (const u16*)d_in[1]; p.wo = (const u16*)d_in[2];
  p.w1 = (const u16*)d_in[4]; p.b1 = (const u16*)d_in[5]; p.w2 = (const u16*)d_in[6];
  p.ws = (char*)d_ws; p.out = (u16*)d_out;
  (void)hipFuncSetAttribute((const void*)block_mega, hipFuncAttributeMaxDynamicSharedMemorySize, LDS_BYTES);
  block_mega<<<256, 512, LDS_BYTES, stream>>>(p);
}
```

```cpp
#include <hip/hip_runtime.h>
#include <hip/hip_bf16.h>
#include <type_traits>

typedef unsigned short u16;
using bf16x8 = __attribute__((ext_vector_type(8))) short;
using s16x4 = __attribute__((ext_vector_type(4))) short;
using f32x16 = __attribute__((ext_vector_type(16))) float;
using f32x4 = __attribute__((ext_vector_type(4))) float;
using f32x2 = __attribute__((ext_vector_type(2))) float;
using u32x4 = __attribute__((ext_vector_type(4))) unsigned;
using u32x2 = __attribute__((ext_vector_type(2))) unsigned;
using bf16x2_t = __attribute__((ext_vector_type(2))) __bf16;
#define DI __device__ __forceinline__
#define LAS __attribute__((address_space(3)))
#define GAS __attribute__((address_space(1)))
#define MFMA(a, b, c) __builtin_amdgcn_mfma_f32_32x32x16_bf16((a), (b), (c), 0, 0, 0)
#define SBAR() __builtin_amdgcn_sched_barrier(0)

constexpr int S = 2048, D = 512, F = 1792;
constexpr float LN_EPS = 1e-5f;
constexpr float LOG2E = 1.4426950408889634f;
constexpr float QSCALE = 0.25f * LOG2E;
constexpr float BOUND_SAFE = 40.f;
constexpr int LDS_BYTES = 163840;

constexpr unsigned WS_FLAG = 0;
constexpr unsigned FL_A = 4096, FL_B = FL_A + 256 * 64, FL_C = FL_B + 256 * 64, FL_D = FL_C + 256 * 64;
constexpr unsigned WS_KMAX = 128u << 10, WS_STAT = 256u << 10;
constexpr unsigned WS_Q = 1u << 20, WS_KV = 3u << 20  , WS_O = 7u << 20, WS_HF = 9u << 20, WS_ACT = 11u << 20;
constexpr unsigned WS_USED = 19u << 20;

struct Params { const u16 *x, *wqkv, *wo, *w1, *b1, *w2; char* ws; u16* out; };

template <int N> using IC = std::integral_constant<int, N>;
template <int B, int E, class Fn> DI void sfor(Fn&& f) { if constexpr (B < E) { f(IC<B>{}); sfor<B + 1, E>(f); } }

DI int rfl(int v) { return __builtin_amdgcn_readfirstlane(v); }
DI unsigned cvtpk(float lo, float hi) { const f32x2 v = {lo, hi}; return __builtin_bit_cast(unsigned, __builtin_convertvector(v, bf16x2_t)); }
DI float bf_lo(unsigned u) { return __uint_as_float(u << 16); }
DI float bf_hi(unsigned u) { return __uint_as_float(u & 0xffff0000u); }
constexpr unsigned ONES2 = 0x3F803F80u;
#define MFMA16(a, b, c) __builtin_amdgcn_mfma_f32_16x16x32_bf16((a), (b), (c), 0, 0, 0)
DI f32x2 unpk(unsigned u) { return f32x2{__uint_as_float(u << 16), __uint_as_float(u & 0xffff0000u)}; }
DI bf16x8 rowsum_sel(int l) { const unsigned on = ((l & 15) == ((l >> 4) & 1)) ? ONES2 : 0u; return __builtin_bit_cast(bf16x8, u32x4{on, on, on, on}); }
DI void dma16(const void* sbase, unsigned voff, unsigned lds) {
  asm volatile("s_mov_b32 m0, %2\n\ts_nop 0\n\tglobal_load_lds_dwordx4 %0, %1" ::"v"(voff), "s"(sbase), "s"(lds) : "memory");
}
DI void dma16_sc1(const void* sbase, unsigned voff, unsigned lds) {
  asm volatile("s_mov_b32 m0, %2\n\ts_nop 0\n\tglobal_load_lds_dwordx4 %0, %1 sc1" ::"v"(voff), "s"(sbase), "s"(lds) : "memory");
}
template <int IMM, bool SC1> DI void gload16(bf16x8& dst, const void* sbase, unsigned voff) {
  if constexpr (SC1) asm volatile("global_load_dwordx4 %0, %1, %2 offset:%3 sc1" : "=v"(dst) : "v"(voff), "s"(sbase), "i"(IMM) : "memory");
  else asm volatile("global_load_dwordx4 %0, %1, %2 offset:%3" : "=v"(dst) : "v"(voff), "s"(sbase), "i"(IMM) : "memory");
}
template <int IMM, bool SC1 = false> DI void gload8(u32x2& dst, const void* sbase, unsigned voff) {
  if constexpr (SC1) asm volatile("global_load_dwordx2 %0, %1, %2 offset:%3 sc1" : "=v"(dst) : "v"(voff), "s"(sbase), "i"(IMM) : "memory");
  else asm volatile("global_load_dwordx2 %0, %1, %2 offset:%3" : "=v"(dst) : "v"(voff), "s"(sbase), "i"(IMM) : "memory");
}
template <bool SC1> DI void gload4(float& dst, const void* sbase, unsigned voff) {
  if constexpr (SC1) asm volatile("global_load_dword %0, %1, %2 sc1" : "=v"(dst) : "v"(voff), "s"(sbase) : "memory");
  else asm volatile("global_load_dword %0, %1, %2" : "=v"(dst) : "v"(voff), "s"(sbase) : "memory");
}
typedef __attribute__((ext_vector_type(4))) unsigned v4u_;
DI __amdgpu_buffer_rsrc_t make_rsrc(void* p, unsigned bytes) { return __builtin_amdgcn_make_buffer_rsrc(p, 0, bytes, 0x00020000); }
template <bool SC1> DI void st16(__amdgpu_buffer_rsrc_t r, unsigned off, bf16x8 v) { __builtin_amdgcn_raw_buffer_store_b128(__builtin_bit_cast(v4u_, v), r, off, 0, SC1 ? 16 : 0); }
template <bool SC1> DI void st8(__amdgpu_buffer_rsrc_t r, unsigned off, u32x2 v) { __builtin_amdgcn_raw_buffer_store_b64(v, r, off, 0, SC1 ? 16 : 0); }
template <bool SC1> DI void st4(__amdgpu_buffer_rsrc_t r, unsigned off, unsigned v) { __builtin_amdgcn_raw_buffer_store_b32(v, r, off, 0, SC1 ? 16 : 0); }

template <int N> DI void wait_bar() { asm volatile("s_waitcnt vmcnt(%0) lgkmcnt(0)\n\ts_barrier" ::"i"(N) : "memory"); }
DI void lds_bar() { asm volatile("s_waitcnt lgkmcnt(0)\n\ts_barrier" ::: "memory"); }
DI s16x4 trread(const LAS char* p) {
  typedef short v4i16_t __attribute__((ext_vector_type(4)));
  return __builtin_bit_cast(s16x4, __builtin_amdgcn_ds_read_tr16_b64_v4i16((LAS v4i16_t*)p));
}
DI bf16x8 cat8(s16x4 a, s16x4 b) { return __builtin_shufflevector(a, b, 0, 1, 2, 3, 4, 5, 6, 7); }
DI bf16x8 pack8(float a0, float a1, float a2, float a3, float a4, float a5, float a6, float a7) {
  u32x4 w = {cvtpk(a0, a1), cvtpk(a2, a3), cvtpk(a4, a5), cvtpk(a6, a7)};
  return __builtin_bit_cast(bf16x8, w);
}
template <int CTRL> DI float dpp(float v) { return __uint_as_float(__builtin_amdgcn_update_dpp(0u, __float_as_uint(v), CTRL, 0xf, 0xf, true)); }
DI float sum8(float v) { v += dpp<0xB1>(v); v += dpp<0x4E>(v); v += dpp<0x141>(v); return v; }
DI float sumx32(float v) { auto r = __builtin_amdgcn_permlane32_swap(__float_as_uint(v), __float_as_uint(v), false, false); return __uint_as_float(r[0]) + __uint_as_float(r[1]); }
DI float maxx32(float v) { auto r = __builtin_amdgcn_permlane32_swap(__float_as_uint(v), __float_as_uint(v), false, false); return fmaxf(__uint_as_float(r[0]), __uint_as_float(r[1])); }
DI float maxx16(float v) { auto r = __builtin_amdgcn_permlane16_swap(__float_as_uint(v), __float_as_uint(v), false, false); return fmaxf(__uint_as_float(r[0]), __uint_as_float(r[1])); }
DI float max16(float v) { v = fmaxf(v, dpp<0xB1>(v)); v = fmaxf(v, dpp<0x4E>(v)); v = fmaxf(v, dpp<0x141>(v)); v = fmaxf(v, dpp<0x140>(v)); return v; }

DI void publish(char* ws, unsigned flag_off, unsigned epoch) {
  asm volatile("s_waitcnt vmcnt(0) lgkmcnt(0)\n\ts_barrier" ::: "memory");
  if (threadIdx.x == 0) __hip_atomic_store((GAS unsigned*)(ws + flag_off), epoch, __ATOMIC_RELAXED, __HIP_MEMORY_SCOPE_AGENT);
}
DI void await(char* ws, unsigned flag_off, int n, unsigned epoch) {
  if (threadIdx.x >= 448) {
    const int l = threadIdx.x & 63;
    GAS unsigned* f = (GAS unsigned*)(ws + flag_off + (l < n ? l : 0) * 64);
    for (unsigned spins = 0; spins < (1u << 20); ++spins) {
      const unsigned v = __hip_atomic_load(f, __ATOMIC_RELAXED, __HIP_MEMORY_SCOPE_AGENT);
      if (__all(v == epoch)) break;
      __builtin_amdgcn_s_sleep(1);
    }
  }
  asm volatile("s_waitcnt lgkmcnt(0)\n\ts_barrier" ::: "memory");
}

DI int d_rb(int b) { return 4 * ((b & 7) >> 1) + ((b >> 3) & 3); }
DI int d_cb(int b) { return 7 * (b & 1) + (b >> 5); }

struct Ctx { LAS char* lds; unsigned lds0; int tid, l, w, hh, b; unsigned epoch; };

DI void phaseA(const Params& p, Ctx& c) {
  char* ws = p.ws;
  LAS char* lds = c.lds;
  const int l = c.l, w = c.w, hh = c.hh, b = c.b;
  const int rb = 8 * ((b & 7) >> 1) + ((b >> 3) & 7), hg = 4 * (b & 1) + (b >> 6);
  bf16x8 ft[3] = {};
  {
    unsigned long long pc; unsigned len;
    asm volatile("s_getpc_b64 %0\n.Lmega_here%=:\n\ts_mov_b32 %1, mega_code_end-.Lmega_here%=" : "=s"(pc), "=s"(len));
    const unsigned j = (unsigned)(b >> 3);
    const unsigned oc = j * 1024u + (w & 1) * 32768u + l * 16u;
    ft[2] = *(const GAS bf16x8*)((const char*)pc + (w < 2 && oc + 16 <= len ? oc : 0u));
    ft[0] = *(const GAS bf16x8*)((const char*)p.x + (size_t)(512 * ((b & 7) >> 1) + 256 * (b & 1) + 8 * j + w) * 1024 + l * 16);
    { const unsigned pair = 2u * (w < 6 ? w : 5) + (l >> 5);
      ft[1] = *(const GAS bf16x8*)((const char*)p.wqkv + (size_t)(128 * ((b & 7) >> 1) + 4 * j + pair / 3) * 3072 + (pair % 3) * 1024 + (b & 1) * 512 + (l & 31) * 16); }
  }
  const int q4 = (l & 15) >> 2, p4 = l & 3, blk = (l >> 4) & 1;
  LAS float* tmean = (LAS float*)(lds + 0);
  LAS float* trstd = tmean + 64;
  LAS float* tcs = tmean + 128;
  f32x16 acc[3] = {};
  const int m = w & 1, g = (w >> 1) & 1;
  if (w < 4) {
    const bf16x8 sel = rowsum_sel(l);
    const int swz = (l >> 1) & 7;
    const int xrow = (32 * m + (l & 31)) * 128;
    const int wa = 8192 + (8 * hh + q4) * 128 + (((4 * g + 2 * blk + (p4 >> 1)) ^ (((q4 >> 1) & 1) << 2)) * 16) + (p4 & 1) * 8;
    int xo[4], wo_[2];
#pragma unroll
    for (int i = 0; i < 4; ++i) xo[i] = xrow + (((2 * (i ^ m) + hh) ^ swz) * 16);
    wo_[0] = wa + m * 2048; wo_[1] = wa + (1 - m) * 2048;
    bf16x8 xf[2][4], wf[2][4][3];
    f32x4 cs[3] = {};
    auto ld_w = [&](auto SET, const LAS char* sl, int i, int sgm) { constexpr int q = SET.value; const int o = wo_[i & 1] + (i & 2) * 2048 + sgm * 8192;
      wf[q][i][sgm] = cat8(trread(sl + o), trread(sl + o + 512)); };
    lds_bar();
#pragma unroll
    for (int i = 0; i < 4; ++i) { xf[0][i] = *(const LAS bf16x8*)(lds + xo[i]); for (int sgm = 0; sgm < 3; ++sgm) ld_w(IC<0>{}, lds, i, sgm); }
    sfor<0, 8>([&](auto ST) {
      constexpr int st = ST.value, q = st & 1, qn = (st + 1) & 1;
      if constexpr (st + 1 < 8) lds_bar(); else asm volatile("s_waitcnt lgkmcnt(0)" ::: "memory");
      asm volatile("" : "+v"(xf[q][0]), "+v"(xf[q][1]), "+v"(xf[q][2]), "+v"(xf[q][3]), "+v"(wf[q][0][0]), "+v"(wf[q][0][1]), "+v"(wf[q][0][2]), "+v"(wf[q][1][0]), "+v"(wf[q][1][1]), "+v"(wf[q][1][2]),
                   "+v"(wf[q][2][0]), "+v"(wf[q][2][1]), "+v"(wf[q][2][2]), "+v"(wf[q][3][0]), "+v"(wf[q][3][1]), "+v"(wf[q][3][2]));
      const LAS char* sn = lds + ((st + 1) % 5) * 32768;
#pragma unroll
      for (int i = 0; i < 4; ++i) {
        acc[0] = MFMA(wf[q][i][0], xf[q][i], acc[0]);
        SBAR();
        if constexpr (st + 1 < 8) { ld_w(IC<qn>{}, sn, i, 0); xf[qn][i] = *(const LAS bf16x8*)(sn + xo[i]); }
        SBAR();
        acc[1] = MFMA(wf[q][i][1], xf[q][i], acc[1]);
        SBAR();
        if constexpr (st + 1 < 8) ld_w(IC<qn>{}, sn, i, 1);
        SBAR();
        acc[2] = MFMA(xf[q][i], wf[q][i][2], acc[2]);
        SBAR();
        if constexpr (st + 1 < 8) ld_w(IC<qn>{}, sn, i, 2);
        if ((i & 1) == 0) { cs[0] = MFMA16(wf[q][i][0], sel, cs[0]); cs[1] = MFMA16(wf[q][i][1], sel, cs[1]); cs[2] = MFMA16(wf[q][i][2], sel, cs[2]); }
        SBAR();
      }
    });
    if ((l & 15) < 2) {
#pragma unroll
      for (int t = 0; t < 3; ++t) *(LAS f32x4*)(tcs + (w * 3 + t) * 32 + 16 * (l & 15) + 4 * (l >> 4)) = cs[t];
    }
  } else {
    const int h = w - 4;
    const unsigned xo0 = (unsigned)(64 * rb + 16 * h + (l >> 3)) * 1024u + (unsigned)(((l & 7) ^ ((l >> 4) & 7)) * 16);
    const unsigned xo1 = (unsigned)(64 * rb + 16 * h + 8 + (l >> 3)) * 1024u + (unsigned)(((l & 7) ^ ((4 + (l >> 4)) & 7)) * 16);
    const unsigned wof = (unsigned)(16 * h + (l >> 3)) * 3072u + (unsigned)hg * 128u + (unsigned)(((l & 7) ^ (((l >> 4) & 1) << 2)) * 16);
    auto issue = [&](int st) {
      const unsigned dst = c.lds0 + (st % 5) * 32768 + h * 2048;
      const char* wsrc = (const char*)p.wqkv + (size_t)st * 64 * 3072;
      dma16((const char*)p.x + st * 128, xo0, dst);
      dma16((const char*)p.x + st * 128, xo1, dst + 1024);
#pragma unroll
      for (int sgm = 0; sgm < 3; ++sgm) { dma16(wsrc + sgm * 1024, wof, dst + 8192 + sgm * 8192); dma16(wsrc + sgm * 1024 + 8 * 3072, wof, dst + 8192 + sgm * 8192 + 1024); }
    };
    issue(0); issue(1); issue(2); issue(3); issue(4);
    f32x2 rs[2] = {}, rq[2] = {};
    u32x4 sv[2][2];
    wait_bar<32>();
    sv[0][0] = *(const LAS u32x4*)(lds + h * 2048 + l * 16); sv[0][1] = *(const LAS u32x4*)(lds + h * 2048 + 1024 + l * 16);
    sfor<0, 8>([&](auto ST) {
      constexpr int st = ST.value, q = st & 1, qn = (st + 1) & 1;
      if constexpr (st + 1 < 8) {
        constexpr int last = 4 + st > 7 ? 7 : 4 + st;
        wait_bar<8 * (last - (st + 1))>();
        if constexpr (st + 5 < 8) issue(st + 5);
        const LAS char* sn = lds + ((st + 1) % 5) * 32768 + h * 2048 + l * 16;
        sv[qn][0] = *(const LAS u32x4*)sn; sv[qn][1] = *(const LAS u32x4*)(sn + 1024);
      }
#pragma unroll
      for (int r = 0; r < 2; ++r)
#pragma unroll
        for (int i = 0; i < 4; ++i) { const f32x2 v = unpk(sv[q][r][i]); rs[r] += v; rq[r] = __builtin_elementwise_fma(v, v, rq[r]); }
      asm volatile("" : "+v"(rs[0]), "+v"(rq[0]), "+v"(rs[1]), "+v"(rq[1]));
    });
#pragma unroll
    for (int r = 0; r < 2; ++r) {
      const float s1 = sum8(rs[r][0] + rs[r][1]), s2 = sum8(rq[r][0] + rq[r][1]);
      if ((l & 7) == 0) { const float mean = s1 * (1.f / D); const int row = 16 * h + 8 * r + (l >> 3);
        tmean[row] = mean; trstd[row] = __builtin_amdgcn_rsqf(fmaxf(s2 * (1.f / D) - mean * mean, 0.f) + LN_EPS); }
    }
  }
  { asm volatile("" : "+v"(c.epoch), "+v"(ft[0]), "+v"(ft[1]), "+v"(ft[2])); c.epoch += 1u; }
  lds_bar();
  const __amdgpu_buffer_rsrc_t rw = make_rsrc(ws, WS_USED);
  if (w < 4) {
    const int tile = 2 * rb + m;
    const float mean_l = tmean[32 * m + (l & 31)], rstd_l = trstd[32 * m + (l & 31)];
    f32x4 cv[2][4], mm[4], rr[4];
#pragma unroll
    for (int gg = 0; gg < 4; ++gg) {
#pragma unroll
      for (int t = 0; t < 2; ++t) cv[t][gg] = *(const LAS f32x4*)(tcs + (w * 3 + t) * 32 + 8 * gg + 4 * hh) + *(const LAS f32x4*)(tcs + ((w ^ 1) * 3 + t) * 32 + 8 * gg + 4 * hh);
      mm[gg] = *(const LAS f32x4*)(tmean + 32 * m + 8 * gg + 4 * hh); rr[gg] = *(const LAS f32x4*)(trstd + 32 * m + 8 * gg + 4 * hh);
    }
    const int n = l & 31;
    const float ncs = -(tcs[(w * 3 + 2) * 32 + n] + tcs[((w ^ 1) * 3 + 2) * 32 + n]);
#pragma unroll
    for (int s = 0; s < 2; ++s) {
      f32x2 v[4];
#pragma unroll
      for (int j = 0; j < 4; ++j) { const int r = 8 * s + 2 * j; const f32x2 a = {acc[2][r], acc[2][r + 1]}, m2 = {mm[r >> 2][r & 3], mm[r >> 2][(r & 3) + 1]}, r2 = {rr[r >> 2][r & 3], rr[r >> 2][(r & 3) + 1]};
        v[j] = __builtin_elementwise_fma(m2, f32x2{ncs, ncs}, a) * r2; }
      st16<true>(rw, WS_KV + (unsigned)((4 * hg + 2 * g + (n >> 4)) * 64 + tile) * 2048 + 1024 + s * 512 + hh * 256 + (n & 15) * 16,
               pack8(v[0][0], v[0][1], v[1][0], v[1][1], v[2][0], v[2][1], v[3][0], v[3][1]));
    }
#pragma unroll
    for (int t = 1; t >= 0; --t) {
      const float sc = t == 0 ? rstd_l * QSCALE : rstd_l, nm = -mean_l * sc;
#pragma unroll
      for (int s = 0; s < 2; ++s) {
        f32x2 v[4];
#pragma unroll
        for (int j = 0; j < 4; ++j) { const int r = 8 * s + 2 * j; const f32x2 a = {acc[t][r], acc[t][r + 1]}, cc = {cv[t][r >> 2][r & 3], cv[t][r >> 2][(r & 3) + 1]};
          v[j] = __builtin_elementwise_fma(a, f32x2{sc, sc}, cc * nm); }
        const int head = 4 * hg + 2 * g + s;
        st16<true>(rw, (t == 0 ? WS_Q + (unsigned)(head * 64 + tile) * 1024 : WS_KV + (unsigned)(head * 64 + tile) * 2048) + l * 16, pack8(v[0][0], v[0][1], v[1][0], v[1][1], v[2][0], v[2][1], v[3][0], v[3][1]));
        if (t == 1) {
          f32x2 n2 = v[0] * v[0]; n2 = __builtin_elementwise_fma(v[1], v[1], n2); n2 = __builtin_elementwise_fma(v[2], v[2], n2); n2 = __builtin_elementwise_fma(v[3], v[3], n2);
          const float mx = maxx16(max16(sumx32(n2[0] + n2[1])));
          if (l == 0) st4<true>(rw, WS_KMAX + (unsigned)(head * 64 + tile) * 4, __float_as_uint(mx));
        }
      }
    }
  }
  publish(ws, FL_A + (hg * 32 + rb) * 64, c.epoch);
}

DI unsigned wpiece_lane(int l, unsigned row_bytes) { return (unsigned)(l >> 3) * row_bytes + (unsigned)(((l & 7) ^ (((l >> 4) & 1) << 2)) * 16); }
template <int NW, int W0 = 0> DI void prefetch_wo(const Params& p, const Ctx& c) {
  if (c.w < W0 || c.w >= W0 + NW) return;
  const unsigned off = wpiece_lane(c.l, 1024) + (unsigned)(c.b >> 5) * 128u;
  for (int pc = c.w - W0; pc < 64; pc += NW) dma16((const char*)p.wo + (size_t)(8 * pc) * 1024, off, c.lds0 + pc * 1024);
}
DI constexpr unsigned w1_stage_base(int j) { return j < 6 ? 65536 + j * 16384 : (j - 6) * 16384; }
template <int NW, int W0 = 0> DI void prefetch_w1(const Params& p, const Ctx& c, int p0, int p1) {
  if (c.w < W0 || c.w >= W0 + NW) return;
  const unsigned off = wpiece_lane(c.l, F * 2) + (unsigned)d_cb(c.b) * 256u;
  for (int pc = p0 + c.w - W0; pc < p1; pc += NW) {
    const int j = pc >> 4, seg = (pc >> 3) & 1, r8 = pc & 7;
    dma16((const char*)p.w1 + (size_t)(64 * j + 8 * r8) * (F * 2) + seg * 128, off, c.lds0 + w1_stage_base(j) + seg * 8192 + r8 * 1024);
  }
}
template <int NW> DI void prefetch_w2(const Params& p, const Ctx& c, int p0, int p1) {
  if (c.w >= NW) return;
  const unsigned off = wpiece_lane(c.l, 1024) + (unsigned)(c.b >> 5) * 128u;
  for (int pc = p0 + c.w; pc < p1; pc += NW) {
    const int st = pc >> 4, kq = (pc >> 2) & 3, r8 = pc & 3;
    dma16((const char*)p.w2 + (size_t)(448 * kq + 32 * st + 8 * r8) * 1024, off, c.lds0 + ((st + 4) % 10) * 16384 + kq * 4096 + r8 * 1024);
  }
}

DI void warm_first(const Params& p, const Ctx& c) {
  const int h = c.w - 4;
  const unsigned off1 = (unsigned)c.b * 7168u + (unsigned)h * 2048u + (unsigned)c.l * 16u;
  dma16((const char*)p.w1, off1, c.lds0 + 139520);
  dma16((const char*)p.w2, off1, c.lds0 + 139520);
  if (h < 3) { dma16((const char*)p.w1 + 1024, off1, c.lds0 + 139520); dma16((const char*)p.w2 + 1024, off1, c.lds0 + 139520); }
}
DI void warm_w1_late(const Params& p, const Ctx& c) {
  const int h = c.w - 4;
  const unsigned j = (unsigned)(c.b >> 3);
  for (int pc = h; pc < 35; pc += 4)
    dma16((const char*)p.w1, (pc < 21 ? 128u * 3584u + j * 21504u + pc * 1024u : 384u * 3584u + j * 14336u + (pc - 21) * 1024u) + c.l * 16u, c.lds0 + 139520);
}
DI void warm_w2(const Params& p, const Ctx& c) {
  const int h = c.w - 4;
  const unsigned off = (unsigned)(c.b >> 3) * 57344u + (unsigned)h * 14336u + (unsigned)c.l * 16u;
#pragma unroll
  for (int i = 0; i < 14; ++i) dma16((const char*)p.w2 + i * 1024, off, c.lds0 + 139520);
}

constexpr int B_ONES = 131072;
DI void phaseB(const Params& p, const Ctx& c) {
  char* ws = p.ws;
  LAS char* lds = c.lds;
  const int l = c.l, w = c.w, hh = c.hh, b = c.b, tid = c.tid;
  const int head = 4 * (b & 7) + ((b >> 3) & 3), qb = b >> 5;
  for (int i = tid; i < (4096 + 512) / 16; i += 512) { const unsigned v = (i & 31) == 0 ? ONES2 : 0u; ((LAS u32x4*)(lds + B_ONES))[i] = u32x4{v, v, v, v}; }
  await(ws, FL_A + (head >> 2) * 32 * 64, 32, c.epoch);
  const unsigned l16 = l * 16;
  if (w >= 4) {
    const char* kvg = ws + WS_KV + (size_t)head * 131072;
    const int h = w - 4;
    auto piece = [&](int pc) { dma16_sc1(kvg + pc * 1024, l16, c.lds0 + pc * 1024); };
#pragma unroll
    for (int i = 0; i < 4; ++i) piece(4 * h + i);
#pragma unroll
    for (int i = 0; i < 12; ++i) piece(16 + 4 * i + h);
    wait_bar<12>();
#pragma unroll
    for (int i = 0; i < 16; ++i) piece(64 + 4 * i + h);
    wait_bar<24>();
    wait_bar<16>();
    warm_first(p, c);
    wait_bar<0>();
    if (b < 224) prefetch_w1<4, 4>(p, c, 80, 96);
    lds_bar();
    prefetch_wo<4, 4>(p, c);
    lds_bar();
    if (b < 224) prefetch_w1<4, 4>(p, c, 0, 32);
    warm_w2(p, c);
  } else {
    bf16x8 qa, qb_;
    float kmx;
    gload4<true>(kmx, ws + WS_KMAX + head * 256, l * 4);
    gload16<0, true>(qa, ws + WS_Q + (size_t)(head * 64 + qb * 8 + w) * 1024, l * 16);
    gload16<0, true>(qb_, ws + WS_Q + (size_t)(head * 64 + qb * 8 + w + 4) * 1024, l * 16);
    asm volatile("s_waitcnt vmcnt(0)" : "+v"(qa), "+v"(qb_), "+v"(kmx)::"memory");
    kmx = maxx32(maxx16(max16(kmx)));
    auto qbound = [&](bf16x8 q) { const u32x4 u = __builtin_bit_cast(u32x4, q); f32x2 t = {0.f, 0.f}; for (int i = 0; i < 4; ++i) { const f32x2 v = unpk(u[i]); t = __builtin_elementwise_fma(v, v, t); }
      return __builtin_amdgcn_sqrtf(sumx32(t[0] + t[1]) * kmx) * 1.01f + 1e-6f; };
    const float bna = qbound(qa), bnb = qbound(qb_);
    const bool safe = __all(bna <= BOUND_SAFE && bnb <= BOUND_SAFE);
    f32x16 oa = {}, ob = {};
    const int lo5 = l & 31;
    const bool isv = lo5 < 16;
    const int va0 = isv ? 1024 + hh * 256 + lo5 * 16 : B_ONES + (lo5 == 16 ? 0 : (lo5 >= 20 && lo5 < 28) ? 64 : 16);
    lds_bar();
    if (safe) {
      f32x16 nga, ngb; for (int i = 0; i < 16; ++i) { nga[i] = -bna; ngb[i] = -bnb; }
      f32x16 s0, s1;
      bf16x8 pa, pb = {};
      bf16x8 kf, v0, v1 = {};
      unsigned ka = c.lds0 + l * 16, va = c.lds0 + va0;
      const unsigned vstep = isv ? 4096u : 0u;
      asm volatile("ds_read_b128 %1, %3\n\tds_read_b128 %2, %4\n\ts_waitcnt lgkmcnt(0)\n\t"
                   "v_mfma_f32_32x32x16_bf16 %0, %1, %5, %6\n\ts_nop 7\n\ts_nop 3"
                   : "=&{v[192:207]}"(s0), "=&v"(kf), "=&v"(v0) : "v"(ka), "v"(va), "v"(qa), "v"(nga) : "memory");
#define E_(b, i) "v_exp_f32 v[" #b "+" #i "], v[" #b "+" #i "]\n\t"
#define C_(d, i, b, j, k) "v_cvt_pk_bf16_f32 v[" #d "+" #i "], v[" #b "+" #j "], v[" #b "+" #k "]\n\t"
#define MID_(SC) E_(SC, 4) E_(SC, 5) E_(SC, 6) E_(SC, 7) C_(224, 0, SC, 0, 1) C_(224, 1, SC, 2, 3) E_(SC, 8) E_(SC, 9) C_(224, 2, SC, 4, 5) C_(224, 3, SC, 6, 7) E_(SC, 10) E_(SC, 11)
#define END_(SC) E_(SC, 12) E_(SC, 13) E_(SC, 14) E_(SC, 15) C_(228, 0, SC, 8, 9) C_(228, 1, SC, 10, 11) C_(228, 2, SC, 12, 13) C_(228, 3, SC, 14, 15)
#define UNIT_A(KOFF, V1OFF) \
      "v_mfma_f32_32x32x16_bf16 v[208:223], %[kf], %[qb], %[ngb]\n\t" \
      "ds_read_b128 %[kf], %[ka] offset:" #KOFF "\n\t" \
      E_(192, 0) E_(192, 1) E_(192, 2) E_(192, 3) \
      "v_mfma_f32_32x32x16_bf16 %[ob], %[v1], v[228:231], %[ob]\n\t" \
      "ds_read_b128 %[v1], %[va] offset:" #V1OFF "\n\t" \
      MID_(192) \
      "s_waitcnt lgkmcnt(2)\n\t" \
      "v_mfma_f32_32x32x16_bf16 %[oa], %[v0], v[224:227], %[oa]\n\t" \
      END_(192)
#define UNIT_B(V0OFF) \
      "s_waitcnt lgkmcnt(0)\n\t" \
      "v_mfma_f32_32x32x16_bf16 v[192:207], %[kf], %[qa], %[nga]\n\t" \
      E_(208, 0) E_(208, 1) E_(208, 2) E_(208, 3) \
      "v_mfma_f32_32x32x16_bf16 %[oa], %[v1], v[228:231], %[oa]\n\t" \
      MID_(208) \
      "v_mfma_f32_32x32x16_bf16 %[ob], %[v0], v[224:227], %[ob]\n\t" \
      "ds_read_b128 %[v0], %[va] offset:" #V0OFF "\n\t" \
      END_(208)
#define RUN_GROUPS(NG) do { int cnt_ = (NG); \
      asm volatile("1:\n\t" UNIT_A(2048, 512) UNIT_B(2048) UNIT_A(4096, 2560) UNIT_B(4096) \
        "v_add_u32 %[ka], 0x1000, %[ka]\n\tv_add_u32 %[va], %[va], %[vs]\n\t" \
        "s_sub_u32 %[cnt], %[cnt], 1\n\ts_cmp_lg_u32 %[cnt], 0\n\ts_cbranch_scc1 1b\n\ts_waitcnt lgkmcnt(0)\n\ts_nop 7\n\ts_nop 3" \
        : "+{v[192:207]}"(s0), "=&{v[208:223]}"(s1), "=&{v[224:227]}"(pa), "+{v[228:231]}"(pb), [oa] "+v"(oa), [ob] "+v"(ob), [kf] "+v"(kf), [v0] "+v"(v0), [v1] "+v"(v1), \
          [ka] "+v"(ka), [va] "+v"(va), [cnt] "+s"(cnt_) \
        : [qa] "v"(qa), [qb] "v"(qb_), [nga] "v"(nga), [ngb] "v"(ngb), [vs] "v"(vstep) : "memory", "scc"); } while (0)
      RUN_GROUPS(3);
      lds_bar();
      RUN_GROUPS(4);
      lds_bar();
      RUN_GROUPS(8);
      lds_bar();
      RUN_GROUPS(1);
      lds_bar();
      RUN_GROUPS(8);
      lds_bar();
      RUN_GROUPS(8);
      ob = MFMA(v1, pb, ob);
    } else {
      float ca = -3.0e38f, cb = -3.0e38f;
      const f32x16 zero = {};
      auto online = [&](const bf16x8& kf, const bf16x8& v0, const bf16x8& v1, const bf16x8& q, f32x16& o, float& mrun) {
        f32x16 s = MFMA(kf, q, zero);
        float tm = s[0];
#pragma unroll
        for (int i = 1; i < 16; ++i) tm = fmaxf(tm, s[i]);
        tm = maxx32(tm);
        const float mn = fmaxf(mrun, tm), f = __builtin_amdgcn_exp2f(mrun - mn);
        mrun = mn;
#pragma unroll
        for (int i = 0; i < 16; ++i) { o[i] *= f; s[i] = __builtin_amdgcn_exp2f(s[i] - mn); }
        o = MFMA(v0, pack8(s[0], s[1], s[2], s[3], s[4], s[5], s[6], s[7]), o);
        o = MFMA(v1, pack8(s[8], s[9], s[10], s[11], s[12], s[13], s[14], s[15]), o);
      };
      for (int t = 0; t < 64; ++t) {
        if (t == 6 || t == 14 || t == 30 || t == 32 || t == 48) lds_bar();
        const bf16x8 kf = *(const LAS bf16x8*)(lds + l * 16 + t * 2048);
        const int vo = isv ? va0 + t * 2048 : va0;
        const bf16x8 v0 = *(const LAS bf16x8*)(lds + vo), v1 = *(const LAS bf16x8*)(lds + vo + 512);
        online(kf, v0, v1, qa, oa, ca);
        online(kf, v0, v1, qb_, ob, cb);
      }
    }
    const float ia = __builtin_amdgcn_rcpf(sumx32(oa[8])), ib = __builtin_amdgcn_rcpf(sumx32(ob[8]));
    const __amdgpu_buffer_rsrc_t rw = make_rsrc(ws, WS_USED);
    st16<true>(rw, WS_O + (unsigned)(((qb * 8 + w) * 32 + head) * 64 + l) * 16, pack8(oa[0] * ia, oa[1] * ia, oa[2] * ia, oa[3] * ia, oa[4] * ia, oa[5] * ia, oa[6] * ia, oa[7] * ia));
    st16<true>(rw, WS_O + (unsigned)(((qb * 8 + w + 4) * 32 + head) * 64 + l) * 16, pack8(ob[0] * ib, ob[1] * ib, ob[2] * ib, ob[3] * ib, ob[4] * ib, ob[5] * ib, ob[6] * ib, ob[7] * ib));
  }
  {
    publish(ws, FL_B + (qb * 32 + head) * 64, c.epoch);
    if (b < 224) prefetch_w1<7>(p, c, 32, 80);
  }
}

template <int KQ, int HGAP> DI void quarter_reduce_t(int m, int l, LAS f32x4* xch, const f32x16 (&acc)[2], float (&val)[8]) {
#pragma unroll
  for (int d = 0; d < 4; ++d) {
    if (d == KQ) continue;
    xch[(d >> 1) * HGAP + (((d * 4 + KQ) * 2 + m) * 2 + 0) * 64 + l] = f32x4{acc[d >> 1][8 * (d & 1)], acc[d >> 1][8 * (d & 1) + 1], acc[d >> 1][8 * (d & 1) + 2], acc[d >> 1][8 * (d & 1) + 3]};
    xch[(d >> 1) * HGAP + (((d * 4 + KQ) * 2 + m) * 2 + 1) * 64 + l] = f32x4{acc[d >> 1][8 * (d & 1) + 4], acc[d >> 1][8 * (d & 1) + 5], acc[d >> 1][8 * (d & 1) + 6], acc[d >> 1][8 * (d & 1) + 7]};
  }
  lds_bar();
  f32x4 t[4][2];
#pragma unroll
  for (int s = 0; s < 4; ++s) { if (s == KQ) continue; t[s][0] = xch[(KQ >> 1) * HGAP + (((KQ * 4 + s) * 2 + m) * 2 + 0) * 64 + l]; t[s][1] = xch[(KQ >> 1) * HGAP + (((KQ * 4 + s) * 2 + m) * 2 + 1) * 64 + l]; }
#pragma unroll
  for (int j = 0; j < 8; ++j) val[j] = acc[KQ >> 1][8 * (KQ & 1) + j];
#pragma unroll
  for (int s = 0; s < 4; ++s) { if (s == KQ) continue; for (int j = 0; j < 4; ++j) { val[j] += t[s][0][j]; val[4 + j] += t[s][1][j]; } }
}
template <int HGAP> DI void quarter_reduce(const Ctx& c, LAS f32x4* xch, const f32x16 (&acc)[2], float (&val)[8]) {
  const int m = c.w & 1, kq = c.w >> 1;
  if (kq == 0) quarter_reduce_t<0, HGAP>(m, c.l, xch, acc, val);
  else if (kq == 1) quarter_reduce_t<1, HGAP>(m, c.l, xch, acc, val);
  else if (kq == 2) quarter_reduce_t<2, HGAP>(m, c.l, xch, acc, val);
  else quarter_reduce_t<3, HGAP>(m, c.l, xch, acc, val);
}

DI void phaseC(const Params& p, const Ctx& c, u32x4& hkeep) {
  char* ws = p.ws;
  LAS char* lds = c.lds;
  const int l = c.l, w = c.w, hh = c.hh, b = c.b;
  const int rb = (b & 7) * 4 + ((b >> 3) & 3), cb = b >> 5;
  const int m = w & 1, kq = w >> 1;
  const int rt = 2 * rb + m, kstep = 4 * cb + kq;
  const GAS u32x2* xp = (const GAS u32x2*)((const char*)p.x + (size_t)rt * 32 * 1024 + kstep * 32 + (l & 31) * 1024 + hh * 8);
  const u32x2 xr0 = xp[0], xr1 = xp[2];
  const int q4 = (l & 15) >> 2, p4 = l & 3, blk = (l >> 4) & 1;
  const int wa0 = (128 * kq + 4 * hh + q4) * 128 + (((2 * blk + (p4 >> 1)) ^ (((q4 >> 1) & 1) << 2)) * 16) + (p4 & 1) * 8;
  const int wa1 = wa0 ^ 64;
  bf16x8 wf[8][2];
#pragma unroll
  for (int i = 0; i < 8; ++i) {
    wf[i][0] = cat8(trread(lds + wa0 + i * 2048), trread(lds + wa0 + i * 2048 + 1024));
    wf[i][1] = cat8(trread(lds + wa1 + i * 2048), trread(lds + wa1 + i * 2048 + 1024));
  }
  await(ws, FL_B + (rb >> 2) * 32 * 64, 32, c.epoch);
  const char* abase = ws + WS_O + ((size_t)rt * 32 + kq * 8) * 1024;
  bf16x8 af[8];
  sfor<0, 8>([&](auto I) { constexpr int i = I.value; gload16<(i & 3) * 1024, true>(af[i], abase + (i >> 2) * 4096, l * 16); });
  f32x16 acc[2] = {};
  sfor<0, 8>([&](auto I) {
    constexpr int i = I.value;
    asm volatile("s_waitcnt vmcnt(%1)" : "+v"(af[i]) : "i"(7 - i) : "memory");
    acc[0] = MFMA(wf[i][0], af[i], acc[0]);
    acc[1] = MFMA(wf[i][1], af[i], acc[1]);
    SBAR();
  });
  float val[8];
  quarter_reduce<0>(c, (LAS f32x4*)lds, acc, val);
  const u32x4 o = {cvtpk(val[0] + bf_lo(xr0[0]), val[1] + bf_hi(xr0[0])), cvtpk(val[2] + bf_lo(xr0[1]), val[3] + bf_hi(xr0[1])),
                   cvtpk(val[4] + bf_lo(xr1[0]), val[5] + bf_hi(xr1[0])), cvtpk(val[6] + bf_lo(xr1[1]), val[7] + bf_hi(xr1[1]))};
  hkeep = o;
  const __amdgpu_buffer_rsrc_t rw = make_rsrc(ws, WS_USED);
  st16<true>(rw, WS_HF + (unsigned)((rt * 32 + kstep) * 64 + l) * 16, __builtin_bit_cast(bf16x8, o));
  f32x2 sm = {0.f, 0.f}, sq = {0.f, 0.f};
#pragma unroll
  for (int i = 0; i < 4; ++i) { const f32x2 v = unpk(o[i]); sm += v; sq = __builtin_elementwise_fma(v, v, sq); }
  LAS f32x2* tst = (LAS f32x2*)(lds + 0);
  tst[((m * 4 + kq) * 2 + hh) * 32 + (l & 31)] = f32x2{sm[0] + sm[1], sq[0] + sq[1]};
  lds_bar();
  if (w < 2 && l < 32) {
    f32x2 t8[8];
#pragma unroll
    for (int i = 0; i < 8; ++i) t8[i] = tst[(w * 8 + i) * 32 + l];
    const f32x2 t = ((t8[0] + t8[1]) + (t8[2] + t8[3])) + ((t8[4] + t8[5]) + (t8[6] + t8[7]));
    st8<true>(rw, WS_STAT + (unsigned)(((2 * rb + w) * 8 + cb) * 32 + l) * 8, __builtin_bit_cast(u32x2, t));
  }
  publish(ws, FL_C + ((rb >> 1) * 16 + (rb & 1) * 8 + cb) * 64, c.epoch);
  if (b < 224) prefetch_w1<7>(p, c, 96, 128);
}

constexpr int D_WIN = 6;
DI void phaseD(const Params& p, const Ctx& c) {
  char* ws = p.ws;
  LAS char* lds = c.lds;
  const int l = c.l, w = c.w, hh = c.hh, b = c.b;
  const int rb = d_rb(b), cb = d_cb(b);
  const int m = w & 3, nh = w >> 2;
  const int rt = 4 * rb + m;
  LAS float* tcs = (LAS float*)(lds + 49152);
  LAS float* tb = tcs + 128;
  if (c.tid < 128) tcs[c.tid] = 0.f;
  const bf16x8 bv = *(const GAS bf16x8*)((const char*)p.b1 + cb * 256 + (l & 15) * 16);
  const int par = m >> 1;
  const int q4 = (l & 15) >> 2, p4 = l & 3, blk = (l >> 4) & 1;
  const int wl = nh * 8192 + (4 * hh + q4) * 128 + (((4 * (m & 1) + 2 * blk + (p4 >> 1)) ^ (((q4 >> 1) & 1) << 2)) * 16) + (p4 & 1) * 8;
  const int wa_e = wl + par * 2048, wa_o = wl + (1 - par) * 2048;
  bf16x8 wf[3][2];
  auto load_w = [&](auto I) {
    constexpr int i = I.value;
    constexpr int o = (int)w1_stage_base(i >> 2) + (i & 2) * 2048;
    const int wa = (i & 1) ? wa_o : wa_e;
    wf[i % 3][0] = cat8(trread(lds + wa + o), trread(lds + wa + o + 1024));
    wf[i % 3][1] = cat8(trread(lds + (wa ^ 64) + o), trread(lds + (wa ^ 64) + o + 1024));
  };
  load_w(IC<0>{}); load_w(IC<1>{});
  await(ws, FL_C + rb * 16 * 64, 16, c.epoch);
  const char* abase = ws + WS_HF + (size_t)rt * 32 * 1024;
  const unsigned aoff0 = l * 16 + par * 1024, aoff1 = l * 16 + (1 - par) * 1024;
  bf16x8 af[32];
  auto load_a = [&](auto I) { constexpr int i = I.value;
    if constexpr (i & 1) gload16<((i >> 1) & 1) * 2048, true>(af[i], abase + (i >> 2) * 4096, aoff1);
    else gload16<((i >> 1) & 1) * 2048, true>(af[i], abase + (i >> 2) * 4096, aoff0); };
  sfor<0, 4>(load_a);
  u32x2 st[4];
  sfor<0, 4>([&](auto I) { constexpr int i = I.value; gload8<i * 256, true>(st[i], ws + WS_STAT + (size_t)rt * 2048, (l & 31) * 8 + hh * 1024); });
  sfor<4, D_WIN>(load_a);
  f32x16 acc[2] = {};
  f32x4 cs = {};
  const bf16x8 sel = rowsum_sel(l);
  sfor<0, 32>([&](auto I) {
    constexpr int i = I.value;
    if constexpr (i + D_WIN < 32) load_a(IC<i + D_WIN>{});
    constexpr int issued = i + D_WIN < 32 ? i + D_WIN + 1 : 32;
    asm volatile("s_waitcnt vmcnt(%1)" : "+v"(af[i]) : "i"(issued - i - 1 + (i < 4 ? 4 : 0)) : "memory");
    if constexpr (i == 20) asm volatile("s_barrier" ::: "memory");
    acc[0] = MFMA(wf[i % 3][0], af[i], acc[0]);
    acc[1] = MFMA(wf[i % 3][1], af[i], acc[1]);
    if constexpr ((i & 1) == 0) cs = MFMA16(wf[i % 3][0], sel, cs);
    if constexpr (i + 2 < 32) { SBAR(); load_w(IC<i + 2>{}); }
    SBAR();
  });
  if ((l & 15) < 2) {
    LAS float* d = tcs + nh * 64 + (m & 1) * 32 + 16 * (l & 15) + 4 * (l >> 4);
#pragma unroll
    for (int j = 0; j < 4; ++j) __hip_atomic_fetch_add(d + j, cs[j], __ATOMIC_RELAXED, __HIP_MEMORY_SCOPE_WORKGROUP);
  }
  if (w == 0 && l < 16) { const u32x4 u = __builtin_bit_cast(u32x4, bv);
    *(LAS f32x4*)(tb + l * 8) = f32x4{bf_lo(u[0]), bf_hi(u[0]), bf_lo(u[1]), bf_hi(u[1])}; *(LAS f32x4*)(tb + l * 8 + 4) = f32x4{bf_lo(u[2]), bf_hi(u[2]), bf_lo(u[3]), bf_hi(u[3])}; }
  asm volatile("" : "+v"(st[0]), "+v"(st[1]), "+v"(st[2]), "+v"(st[3]));
  const f32x2 ss = __builtin_bit_cast(f32x2, st[0]) + __builtin_bit_cast(f32x2, st[1]) + __builtin_bit_cast(f32x2, st[2]) + __builtin_bit_cast(f32x2, st[3]);
  const float sm = sumx32(ss[0]), sq = sumx32(ss[1]);
  const float mean = sm * (1.f / D), rstd = __builtin_amdgcn_rsqf(fmaxf(sq * (1.f / D) - mean * mean, 0.f) + LN_EPS);
  const float nmr = -mean * rstd;
  lds_bar();
  const __amdgpu_buffer_rsrc_t rw = make_rsrc(ws, WS_USED);
#pragma unroll
  for (int t = 0; t < 2; ++t) {
    const int ct = 2 * nh + ((m & 1) ^ t);
    f32x4 cv[4], bb[4];
#pragma unroll
    for (int g = 0; g < 4; ++g) { cv[g] = *(const LAS f32x4*)(tcs + 32 * ct + 8 * g + 4 * hh); bb[g] = *(const LAS f32x4*)(tb + 32 * ct + 8 * g + 4 * hh); }
#pragma unroll
    for (int s = 0; s < 2; ++s) {
      unsigned pk[4];
#pragma unroll
      for (int j = 0; j < 4; ++j) {
        const int r = 8 * s + 2 * j;
        const f32x2 a = {acc[t][r], acc[t][r + 1]}, c2 = {cv[r >> 2][r & 3], cv[r >> 2][(r & 3) + 1]}, b2 = {bb[r >> 2][r & 3], bb[r >> 2][(r & 3) + 1]};
        const f32x2 y = __builtin_elementwise_fma(a, f32x2{rstd, rstd}, __builtin_elementwise_fma(c2, f32x2{nmr, nmr}, b2));
        const f32x2 z = y * LOG2E;
        const f32x2 u = {__builtin_amdgcn_exp2f(z[0]), __builtin_amdgcn_exp2f(z[1])};
        const f32x2 d = __builtin_elementwise_fma(u, u + 2.f, f32x2{2.f, 2.f});
        const f32x2 r2 = {__builtin_amdgcn_rcpf(d[0]), __builtin_amdgcn_rcpf(d[1])};
        const f32x2 th = __builtin_elementwise_fma(r2, f32x2{-2.f, -2.f}, f32x2{1.f, 1.f});
        const f32x2 o = y * th;
        pk[j] = cvtpk(o[0], o[1]);
      }
      st16<true>(rw, WS_ACT + (unsigned)((rt * 112 + cb * 8 + ct * 2 + s) * 64 + l) * 16, __builtin_bit_cast(bf16x8, u32x4{pk[0], pk[1], pk[2], pk[3]}));
      prefetch_w2<8>(p, c, 24 * (2 * t + s), 24 * (2 * t + s) + 24);
    }
  }
  publish(ws, FL_D + (rb * 16 + cb) * 64, c.epoch);
}

constexpr int E_NSLOT = 10, E_NST = 14, E_WIN = 6, E_PRE = 8;
struct ESched {
  int posA[28], posW[E_NST], n_top[E_NST], n_mid[E_NST];
  constexpr ESched() : posA(), posW(), n_top(), n_mid() {
    int n = 0;
    for (int i = 0; i < E_WIN; ++i) posA[i] = n++;
    for (int st = 0; st < E_NST; ++st) {
      n_top[st] = n;
      if (E_PRE + st < E_NST) { n += 2; posW[E_PRE + st] = n - 1; }
      for (int j = 0; j < 2; ++j) if (E_WIN + 2 * st + j < 28) posA[E_WIN + 2 * st + j] = n++;
      n_mid[st] = n;
    }
  }
};
DI void phaseE(const Params& p, const Ctx& c, u32x4 hkeep) {
  constexpr ESched sch{};
  char* ws = p.ws;
  LAS char* lds = c.lds;
  const int l = c.l, w = c.w, hh = c.hh, b = c.b;
  const int rb = (b & 7) * 4 + ((b >> 3) & 3), cb = b >> 5;
  const int m = w & 1, kq = w >> 1;
  const int rt = 2 * rb + m, kstep = 4 * cb + kq;
  const bool early = b < 224;
  if (early) prefetch_w2<7>(p, c, 16 * 6, 16 * 8); else prefetch_w2<7>(p, c, 0, 16 * E_PRE);
  bf16x8 resv = __builtin_bit_cast(bf16x8, hkeep);
  const int q4 = (l & 15) >> 2, p4 = l & 3, blk = (l >> 4) & 1;
  const int wa0 = kq * 4096 + (4 * hh + q4) * 128 + (((2 * blk + (p4 >> 1)) ^ (((q4 >> 1) & 1) << 2)) * 16) + (p4 & 1) * 8;
  const int wa1 = wa0 ^ 64;
  bf16x8 wf[3][2][2];
  auto load_w = [&](auto ST) {
    constexpr int st = ST.value;
    const LAS char* sl = lds + ((st + 4) % E_NSLOT) * 16384;
#pragma unroll
    for (int i = 0; i < 2; ++i) {
      wf[st % 3][i][0] = cat8(trread(sl + wa0 + i * 2048), trread(sl + wa0 + i * 2048 + 1024));
      wf[st % 3][i][1] = cat8(trread(sl + wa1 + i * 2048), trread(sl + wa1 + i * 2048 + 1024));
    }
  };
  if (!early) wait_bar<0>();
  load_w(IC<0>{}); load_w(IC<1>{});
  await(ws, FL_D + (rb >> 1) * 16 * 64, 14, c.epoch);
  const char* abase = ws + WS_ACT + ((size_t)rt * 112 + kq * 28) * 1024;
  bf16x8 af[28];
  auto load_a = [&](auto I) { constexpr int i = I.value; gload16<(i & 3) * 1024, true>(af[i], abase + (i >> 2) * 4096, l * 16); };
  sfor<0, E_WIN>(load_a);
  f32x16 acc[2] = {};
  sfor<0, E_NST>([&](auto ST) {
    constexpr int st = ST.value;
    if constexpr (st == 2) asm volatile("s_waitcnt lgkmcnt(0)\n\ts_barrier" ::: "memory");
    if constexpr (st == 6 || st == 8 || st == 10) asm volatile("s_waitcnt vmcnt(%0) lgkmcnt(0)\n\ts_barrier" ::"i"(sch.n_top[st] - sch.posW[st + 3] - 1) : "memory");
    if constexpr (E_PRE + st < E_NST) prefetch_w2<8>(p, c, 16 * (st + E_PRE), 16 * (st + E_PRE + 1));
    if constexpr (E_WIN + 2 * st < 28) load_a(IC<E_WIN + 2 * st>{});
    if constexpr (E_WIN + 2 * st + 1 < 28) load_a(IC<E_WIN + 2 * st + 1>{});
    asm volatile("s_waitcnt vmcnt(%2)" : "+v"(af[2 * st]), "+v"(af[2 * st + 1]) : "i"(sch.n_mid[st] - sch.posA[2 * st + 1] - 1) : "memory");
    acc[0] = MFMA(wf[st % 3][0][0], af[2 * st], acc[0]);
    acc[1] = MFMA(wf[st % 3][0][1], af[2 * st], acc[1]);
    if constexpr (st + 2 < E_NST) { SBAR(); load_w(IC<st + 2>{}); SBAR(); }
    acc[0] = MFMA(wf[st % 3][1][0], af[2 * st + 1], acc[0]);
    acc[1] = MFMA(wf[st % 3][1][1], af[2 * st + 1], acc[1]);
    SBAR();
  });
  { asm volatile("" : "+v"(resv));
    const u32x4 ru = __builtin_bit_cast(u32x4, resv);
#pragma unroll
    for (int k = 0; k < 4; ++k) { const float on = kq == k ? 1.f : 0.f;
#pragma unroll
      for (int j = 0; j < 4; ++j) { acc[k >> 1][8 * (k & 1) + 2 * j] += on * bf_lo(ru[j]); acc[k >> 1][8 * (k & 1) + 2 * j + 1] += on * bf_hi(ru[j]); } } }
  constexpr int PS = 272, PQ = 64 * PS;
  LAS char* xa = lds;
#pragma unroll
  for (int ct = 0; ct < 2; ++ct)
#pragma unroll
    for (int g = 0; g < 4; ++g)
      *(LAS f32x4*)(xa + kq * PQ + (32 * m + (l & 31)) * PS + (32 * ct + 8 * g + 4 * hh) * 4) = f32x4{acc[ct][4 * g], acc[ct][4 * g + 1], acc[ct][4 * g + 2], acc[ct][4 * g + 3]};
  lds_bar();
  { const int row = c.tid >> 3, c8 = c.tid & 7;
    f32x4 t0[4], t1[4];
#pragma unroll
    for (int q = 0; q < 4; ++q) { t0[q] = *(const LAS f32x4*)(xa + q * PQ + row * PS + c8 * 32); t1[q] = *(const LAS f32x4*)(xa + q * PQ + row * PS + c8 * 32 + 16); }
    const f32x4 s0 = (t0[0] + t0[1]) + (t0[2] + t0[3]), s1 = (t1[0] + t1[1]) + (t1[2] + t1[3]);
    const __amdgpu_buffer_rsrc_t ro = make_rsrc(p.out, S * D * 2);
    st16<false>(ro, (unsigned)(rb * 64 + row) * 1024u + cb * 128 + c8 * 16, pack8(s0[0], s0[1], s0[2], s0[3], s1[0], s1[1], s1[2], s1[3])); }
}

DI Ctx make_ctx(char* lds_g) {
  Ctx c;
  c.lds = (LAS char*)lds_g; c.lds0 = (unsigned)(uintptr_t)lds_g;
  c.tid = threadIdx.x; c.l = c.tid & 63; c.w = rfl(c.tid >> 6); c.hh = c.l >> 5; c.b = blockIdx.x; c.epoch = 0;
  return c;
}

template <int N> __device__ __forceinline__ float padchain(float x) { if constexpr (N > 0) { x = __builtin_fmaf(x, 1.0001f + N * 1e-6f, 0.5f + N); asm volatile("" : "+v"(x)); return padchain<N - 1>(x); } else return x; }
__global__ __launch_bounds__(512, 2) void block_mega(Params p) {
  extern __shared__ __attribute__((aligned(16))) char lds_g[];
  const unsigned long long rt0_ = __builtin_amdgcn_s_memrealtime();
  Ctx c = make_ctx(lds_g);
  c.epoch = __hip_atomic_load((GAS unsigned*)(p.ws + WS_FLAG) + (threadIdx.x >> 12), __ATOMIC_RELAXED, __HIP_MEMORY_SCOPE_AGENT);
  phaseA(p, c);
  phaseB(p, c);
  u32x4 hkeep;
  phaseC(p, c, hkeep);
  if (c.b < 224) phaseD(p, c);
  phaseE(p, c, hkeep);
  if (c.b == 0 && c.tid == 0) __hip_atomic_store((GAS unsigned*)(p.ws + WS_FLAG), c.epoch, __ATOMIC_RELAXED, __HIP_MEMORY_SCOPE_AGENT);
  if (blockIdx.x == 100000) { float x = threadIdx.x;
#pragma unroll
    for (int i = 0; i < 30; ++i) x = padchain<400>(x + i);
    p.out[0] = (u16)x; }
  while (__builtin_amdgcn_s_memrealtime() - rt0_ < 7000ull) __builtin_amdgcn_s_sleep(1);
  asm volatile("mega_code_end:");
}

extern "C" void kernel_launch(void* const* d_in, const int* in_sizes, int n_in, void* d_out, int out_size, void* d_ws, size_t ws_size, hipStream_t stream) {
  Params p;
  p.x = (const u16*)d_in[0]; p.wqkv = (const u16*)d_in[1]; p.wo = (const u16*)d_in[2];
  p.w1 = (const u16*)d_in[4]; p.b1 = (const u16*)d_in[5]; p.w2 = (const u16*)d_in[6];
  p.ws = (char*)d_ws; p.out = (u16*)d_out;
  (void)hipFuncSetAttribute((const void*)block_mega, hipFuncAttributeMaxDynamicSharedMemorySize, LDS_BYTES);
  block_mega<<<256, 512, LDS_BYTES, stream>>>(p);
}
```

```cpp
#include <hip/hip_runtime.h>
#include <hip/hip_bf16.h>
#include <type_traits>

typedef unsigned short u16;
using bf16x8 = __attribute__((ext_vector_type(8))) short;
using s16x4 = __attribute__((ext_vector_type(4))) short;
using f32x16 = __attribute__((ext_vector_type(16))) float;
using f32x4 = __attribute__((ext_vector_type(4))) float;
using f32x2 = __attribute__((ext_vector_type(2))) float;
using u32x4 = __attribute__((ext_vector_type(4))) unsigned;
using u32x2 = __attribute__((ext_vector_type(2))) unsigned;
using bf16x2_t = __attribute__((ext_vector_type(2))) __bf16;
#define DI __device__ __forceinline__
#define LAS __attribute__((address_space(3)))
#define GAS __attribute__((address_space(1)))
#define MFMA(a, b, c) __builtin_amdgcn_mfma_f32_32x32x16_bf16((a), (b), (c), 0, 0, 0)
#define SBAR() __builtin_amdgcn_sched_barrier(0)

constexpr int S = 2048, D = 512, F = 1792;
constexpr float LN_EPS = 1e-5f;
constexpr float LOG2E = 1.4426950408889634f;
constexpr float QSCALE = 0.25f * LOG2E;
constexpr float BOUND_SAFE = 40.f;
constexpr int LDS_BYTES = 163840;

constexpr unsigned WS_FLAG = 0;
constexpr unsigned FL_A = 4096, FL_B = FL_A + 256 * 64, FL_C = FL_B + 256 * 64, FL_D = FL_C + 256 * 64;
constexpr unsigned WS_KMAX = 128u << 10, WS_STAT = 256u << 10;
constexpr unsigned WS_Q = 1u << 20, WS_KV = 3u << 20  , WS_O = 7u << 20, WS_HF = 9u << 20, WS_ACT = 11u << 20;
constexpr unsigned WS_USED = 19u << 20;

struct Params { const u16 *x, *wqkv, *wo, *w1, *b1, *w2; char* ws; u16* out; };

template <int N> using IC = std::integral_constant<int, N>;
template <int B, int E, class Fn> DI void sfor(Fn&& f) { if constexpr (B < E) { f(IC<B>{}); sfor<B + 1, E>(f); } }

DI int rfl(int v) { return __builtin_amdgcn_readfirstlane(v); }
DI unsigned cvtpk(float lo, float hi) { const f32x2 v = {lo, hi}; return __builtin_bit_cast(unsigned, __builtin_convertvector(v, bf16x2_t)); }
DI float bf_lo(unsigned u) { return __uint_as_float(u << 16); }
DI float bf_hi(unsigned u) { return __uint_as_float(u & 0xffff0000u); }
constexpr unsigned ONES2 = 0x3F803F80u;
#define MFMA16(a, b, c) __builtin_amdgcn_mfma_f32_16x16x32_bf16((a), (b), (c), 0, 0, 0)
DI f32x2 unpk(unsigned u) { return f32x2{__uint_as_float(u << 16), __uint_as_float(u & 0xffff0000u)}; }
DI bf16x8 rowsum_sel(int l) { const unsigned on = ((l & 15) == ((l >> 4) & 1)) ? ONES2 : 0u; return __builtin_bit_cast(bf16x8, u32x4{on, on, on, on}); }
DI void dma16(const void* sbase, unsigned voff, unsigned lds) {
  asm volatile("s_mov_b32 m0, %2\n\ts_nop 0\n\tglobal_load_lds_dwordx4 %0, %1" ::"v"(voff), "s"(sbase), "s"(lds) : "memory");
}
DI void dma16_sc1(const void* sbase, unsigned voff, unsigned lds) {
  asm volatile("s_mov_b32 m0, %2\n\ts_nop 0\n\tglobal_load_lds_dwordx4 %0, %1 sc1" ::"v"(voff), "s"(sbase), "s"(lds) : "memory");
}
template <int IMM, bool SC1> DI void gload16(bf16x8& dst, const void* sbase, unsigned voff) {
  if constexpr (SC1) asm volatile("global_load_dwordx4 %0, %1, %2 offset:%3 sc1" : "=v"(dst) : "v"(voff), "s"(sbase), "i"(IMM) : "memory");
  else asm volatile("global_load_dwordx4 %0, %1, %2 offset:%3" : "=v"(dst) : "v"(voff), "s"(sbase), "i"(IMM) : "memory");
}
template <int IMM, bool SC1 = false> DI void gload8(u32x2& dst, const void* sbase, unsigned voff) {
  if constexpr (SC1) asm volatile("global_load_dwordx2 %0, %1, %2 offset:%3 sc1" : "=v"(dst) : "v"(voff), "s"(sbase), "i"(IMM) : "memory");
  else asm volatile("global_load_dwordx2 %0, %1, %2 offset:%3" : "=v"(dst) : "v"(voff), "s"(sbase), "i"(IMM) : "memory");
}
template <bool SC1> DI void gload4(float& dst, const void* sbase, unsigned voff) {
  if constexpr (SC1) asm volatile("global_load_dword %0, %1, %2 sc1" : "=v"(dst) : "v"(voff), "s"(sbase) : "memory");
  else asm volatile("global_load_dword %0, %1, %2" : "=v"(dst) : "v"(voff), "s"(sbase) : "memory");
}
typedef __attribute__((ext_vector_type(4))) unsigned v4u_;
DI __amdgpu_buffer_rsrc_t make_rsrc(void* p, unsigned bytes) { return __builtin_amdgcn_make_buffer_rsrc(p, 0, bytes, 0x00020000); }
template <bool SC1> DI void st16(__amdgpu_buffer_rsrc_t r, unsigned off, bf16x8 v) { __builtin_amdgcn_raw_buffer_store_b128(__builtin_bit_cast(v4u_, v), r, off, 0, SC1 ? 16 : 0); }
template <bool SC1> DI void st8(__amdgpu_buffer_rsrc_t r, unsigned off, u32x2 v) { __builtin_amdgcn_raw_buffer_store_b64(v, r, off, 0, SC1 ? 16 : 0); }
template <bool SC1> DI void st4(__amdgpu_buffer_rsrc_t r, unsigned off, unsigned v) { __builtin_amdgcn_raw_buffer_store_b32(v, r, off, 0, SC1 ? 16 : 0); }

template <int N> DI void wait_bar() { asm volatile("s_waitcnt vmcnt(%0) lgkmcnt(0)\n\ts_barrier" ::"i"(N) : "memory"); }
DI void lds_bar() { asm volatile("s_waitcnt lgkmcnt(0)\n\ts_barrier" ::: "memory"); }
DI s16x4 trread(const LAS char* p) {
  typedef short v4i16_t __attribute__((ext_vector_type(4)));
  return __builtin_bit_cast(s16x4, __builtin_amdgcn_ds_read_tr16_b64_v4i16((LAS v4i16_t*)p));
}
DI bf16x8 cat8(s16x4 a, s16x4 b) { return __builtin_shufflevector(a, b, 0, 1, 2, 3, 4, 5, 6, 7); }
DI bf16x8 pack8(float a0, float a1, float a2, float a3, float a4, float a5, float a6, float a7) {
  u32x4 w = {cvtpk(a0, a1), cvtpk(a2, a3), cvtpk(a4, a5), cvtpk(a6, a7)};
  return __builtin_bit_cast(bf16x8, w);
}
template <int CTRL> DI float dpp(float v) { return __uint_as_float(__builtin_amdgcn_update_dpp(0u, __float_as_uint(v), CTRL, 0xf, 0xf, true)); }
DI float sum8(float v) { v += dpp<0xB1>(v); v += dpp<0x4E>(v); v += dpp<0x141>(v); return v; }
DI float sumx32(float v) { auto r = __builtin_amdgcn_permlane32_swap(__float_as_uint(v), __float_as_uint(v), false, false); return __uint_as_float(r[0]) + __uint_as_float(r[1]); }
DI float maxx32(float v) { auto r = __builtin_amdgcn_permlane32_swap(__float_as_uint(v), __float_as_uint(v), false, false); return fmaxf(__uint_as_float(r[0]), __uint_as_float(r[1])); }
DI float maxx16(float v) { auto r = __builtin_amdgcn_permlane16_swap(__float_as_uint(v), __float_as_uint(v), false, false); return fmaxf(__uint_as_float(r[0]), __uint_as_float(r[1])); }
DI float max16(float v) { v = fmaxf(v, dpp<0xB1>(v)); v = fmaxf(v, dpp<0x4E>(v)); v = fmaxf(v, dpp<0x141>(v)); v = fmaxf(v, dpp<0x140>(v)); return v; }

DI void publish(char* ws, unsigned flag_off, unsigned epoch) {
  asm volatile("s_waitcnt vmcnt(0) lgkmcnt(0)\n\ts_barrier" ::: "memory");
  if (threadIdx.x == 0) __hip_atomic_store((GAS unsigned*)(ws + flag_off), epoch, __ATOMIC_RELAXED, __HIP_MEMORY_SCOPE_AGENT);
}
DI void await(char* ws, unsigned flag_off, int n, unsigned epoch) {
  if (threadIdx.x >= 448) {
    const int l = threadIdx.x & 63;
    GAS unsigned* f = (GAS unsigned*)(ws + flag_off + (l < n ? l : 0) * 64);
    for (unsigned spins = 0; spins < (1u << 20); ++spins) {
      const unsigned v = __hip_atomic_load(f, __ATOMIC_RELAXED, __HIP_MEMORY_SCOPE_AGENT);
      if (__all(v == epoch)) break;
      __builtin_amdgcn_s_sleep(1);
    }
  }
  asm volatile("s_waitcnt lgkmcnt(0)\n\ts_barrier" ::: "memory");
}

DI int d_rb(int b) { return 4 * ((b & 7) >> 1) + ((b >> 3) & 3); }
DI int d_cb(int b) { return 7 * (b & 1) + (b >> 5); }

struct Ctx { LAS char* lds; unsigned lds0; int tid, l, w, hh, b; unsigned epoch; };

DI void phaseA(const Params& p, Ctx& c) {
  char* ws = p.ws;
  LAS char* lds = c.lds;
  const int l = c.l, w = c.w, hh = c.hh, b = c.b;
  const int rb = 8 * ((b & 7) >> 1) + ((b >> 3) & 7), hg = 4 * (b & 1) + (b >> 6);
  bf16x8 ft[3] = {};
  {
    unsigned long long pc; unsigned len;
    asm volatile("s_getpc_b64 %0\n.Lmega_here%=:\n\ts_mov_b32 %1, mega_code_end-.Lmega_here%=" : "=s"(pc), "=s"(len));
    const unsigned j = (unsigned)(b >> 3);
    const unsigned oc = j * 1024u + (w & 1) * 32768u + l * 16u;
    ft[2] = *(const GAS bf16x8*)((const char*)pc + (w < 2 && oc + 16 <= len ? oc : 0u));
    ft[0] = *(const GAS bf16x8*)((const char*)p.x + (size_t)(512 * ((b & 7) >> 1) + 256 * (b & 1) + 8 * j + w) * 1024 + l * 16);
    { const unsigned pair = 2u * (w < 6 ? w : 5) + (l >> 5);
      ft[1] = *(const GAS bf16x8*)((const char*)p.wqkv + (size_t)(128 * ((b & 7) >> 1) + 4 * j + pair / 3) * 3072 + (pair % 3) * 1024 + (b & 1) * 512 + (l & 31) * 16); }
  }
  const int q4 = (l & 15) >> 2, p4 = l & 3, blk = (l >> 4) & 1;
  LAS float* tmean = (LAS float*)(lds + 0);
  LAS float* trstd = tmean + 64;
  LAS float* tcs = tmean + 128;
  f32x16 acc[3] = {};
  const int m = w & 1, g = (w >> 1) & 1;
  if (w < 4) {
    const bf16x8 sel = rowsum_sel(l);
    const int swz = (l >> 1) & 7;
    const int xrow = (32 * m + (l & 31)) * 128;
    const int wa = 8192 + (8 * hh + q4) * 128 + (((4 * g + 2 * blk + (p4 >> 1)) ^ (((q4 >> 1) & 1) << 2)) * 16) + (p4 & 1) * 8;
    int xo[4], wo_[2];
#pragma unroll
    for (int i = 0; i < 4; ++i) xo[i] = xrow + (((2 * (i ^ m) + hh) ^ swz) * 16);
    wo_[0] = wa + m * 2048; wo_[1] = wa + (1 - m) * 2048;
    bf16x8 xf[2][4], wf[2][4][3];
    f32x4 cs[3] = {};
    auto ld_w = [&](auto SET, const LAS char* sl, int i, int sgm) { constexpr int q = SET.value; const int o = wo_[i & 1] + (i & 2) * 2048 + sgm * 8192;
      wf[q][i][sgm] = cat8(trread(sl + o), trread(sl + o + 512)); };
    lds_bar();
#pragma unroll
    for (int i = 0; i < 4; ++i) { xf[0][i] = *(const LAS bf16x8*)(lds + xo[i]); for (int sgm = 0; sgm < 3; ++sgm) ld_w(IC<0>{}, lds, i, sgm); }
    sfor<0, 8>([&](auto ST) {
      constexpr int st = ST.value, q = st & 1, qn = (st + 1) & 1;
      if constexpr (st + 1 < 8) lds_bar(); else asm volatile("s_waitcnt lgkmcnt(0)" ::: "memory");
      asm volatile("" : "+v"(xf[q][0]), "+v"(xf[q][1]), "+v"(xf[q][2]), "+v"(xf[q][3]), "+v"(wf[q][0][0]), "+v"(wf[q][0][1]), "+v"(wf[q][0][2]), "+v"(wf[q][1][0]), "+v"(wf[q][1][1]), "+v"(wf[q][1][2]),
                   "+v"(wf[q][2][0]), "+v"(wf[q][2][1]), "+v"(wf[q][2][2]), "+v"(wf[q][3][0]), "+v"(wf[q][3][1]), "+v"(wf[q][3][2]));
      const LAS char* sn = lds + ((st + 1) % 5) * 32768;
#pragma unroll
      for (int i = 0; i < 4; ++i) {
        acc[0] = MFMA(wf[q][i][0], xf[q][i], acc[0]);
        SBAR();
        if constexpr (st + 1 < 8) { ld_w(IC<qn>{}, sn, i, 0); xf[qn][i] = *(const LAS bf16x8*)(sn + xo[i]); }
        SBAR();
        acc[1] = MFMA(wf[q][i][1], xf[q][i], acc[1]);
        SBAR();
        if constexpr (st + 1 < 8) ld_w(IC<qn>{}, sn, i, 1);
        SBAR();
        acc[2] = MFMA(xf[q][i], wf[q][i][2], acc[2]);
        SBAR();
        if constexpr (st + 1 < 8) ld_w(IC<qn>{}, sn, i, 2);
        if ((i & 1) == 0) { cs[0] = MFMA16(wf[q][i][0], sel, cs[0]); cs[1] = MFMA16(wf[q][i][1], sel, cs[1]); cs[2] = MFMA16(wf[q][i][2], sel, cs[2]); }
        SBAR();
      }
    });
    if ((l & 15) < 2) {
#pragma unroll
      for (int t = 0; t < 3; ++t) *(LAS f32x4*)(tcs + (w * 3 + t) * 32 + 16 * (l & 15) + 4 * (l >> 4)) = cs[t];
    }
  } else {
    const int h = w - 4;
    const unsigned xo0 = (unsigned)(64 * rb + 16 * h + (l >> 3)) * 1024u + (unsigned)(((l & 7) ^ ((l >> 4) & 7)) * 16);
    const unsigned xo1 = (unsigned)(64 * rb + 16 * h + 8 + (l >> 3)) * 1024u + (unsigned)(((l & 7) ^ ((4 + (l >> 4)) & 7)) * 16);
    const unsigned wof = (unsigned)(16 * h + (l >> 3)) * 3072u + (unsigned)hg * 128u + (unsigned)(((l & 7) ^ (((l >> 4) & 1) << 2)) * 16);
    auto issue = [&](int st) {
      const unsigned dst = c.lds0 + (st % 5) * 32768 + h * 2048;
      const char* wsrc = (const char*)p.wqkv + (size_t)st * 64 * 3072;
      dma16((const char*)p.x + st * 128, xo0, dst);
      dma16((const char*)p.x + st * 128, xo1, dst + 1024);
#pragma unroll
      for (int sgm = 0; sgm < 3; ++sgm) { dma16(wsrc + sgm * 1024, wof, dst + 8192 + sgm * 8192); dma16(wsrc + sgm * 1024 + 8 * 3072, wof, dst + 8192 + sgm * 8192 + 1024); }
    };
    issue(0); issue(1); issue(2); issue(3); issue(4);
    f32x2 rs[2] = {}, rq[2] = {};
    u32x4 sv[2][2];
    wait_bar<32>();
    sv[0][0] = *(const LAS u32x4*)(lds + h * 2048 + l * 16); sv[0][1] = *(const LAS u32x4*)(lds + h * 2048 + 1024 + l * 16);
    sfor<0, 8>([&](auto ST) {
      constexpr int st = ST.value, q = st & 1, qn = (st + 1) & 1;
      if constexpr (st + 1 < 8) {
        constexpr int last = 4 + st > 7 ? 7 : 4 + st;
        wait_bar<8 * (last - (st + 1))>();
        if constexpr (st + 5 < 8) issue(st + 5);
        const LAS char* sn = lds + ((st + 1) % 5) * 32768 + h * 2048 + l * 16;
        sv[qn][0] = *(const LAS u32x4*)sn; sv[qn][1] = *(const LAS u32x4*)(sn + 1024);
      }
#pragma unroll
      for (int r = 0; r < 2; ++r)
#pragma unroll
        for (int i = 0; i < 4; ++i) { const f32x2 v = unpk(sv[q][r][i]); rs[r] += v; rq[r] = __builtin_elementwise_fma(v, v, rq[r]); }
      asm volatile("" : "+v"(rs[0]), "+v"(rq[0]), "+v"(rs[1]), "+v"(rq[1]));
    });
#pragma unroll
    for (int r = 0; r < 2; ++r) {
      const float s1 = sum8(rs[r][0] + rs[r][1]), s2 = sum8(rq[r][0] + rq[r][1]);
      if ((l & 7) == 0) { const float mean = s1 * (1.f / D); const int row = 16 * h + 8 * r + (l >> 3);
        tmean[row] = mean; trstd[row] = __builtin_amdgcn_rsqf(fmaxf(s2 * (1.f / D) - mean * mean, 0.f) + LN_EPS); }
    }
  }
  { asm volatile("" : "+v"(c.epoch), "+v"(ft[0]), "+v"(ft[1]), "+v"(ft[2])); c.epoch += 1u; }
  lds_bar();
  const __amdgpu_buffer_rsrc_t rw = make_rsrc(ws, WS_USED);
  if (w < 4) {
    const int tile = 2 * rb + m;
    const float mean_l = tmean[32 * m + (l & 31)], rstd_l = trstd[32 * m + (l & 31)];
    f32x4 cv[2][4], mm[4], rr[4];
#pragma unroll
    for (int gg = 0; gg < 4; ++gg) {
#pragma unroll
      for (int t = 0; t < 2; ++t) cv[t][gg] = *(const LAS f32x4*)(tcs + (w * 3 + t) * 32 + 8 * gg + 4 * hh) + *(const LAS f32x4*)(tcs + ((w ^ 1) * 3 + t) * 32 + 8 * gg + 4 * hh);
      mm[gg] = *(const LAS f32x4*)(tmean + 32 * m + 8 * gg + 4 * hh); rr[gg] = *(const LAS f32x4*)(trstd + 32 * m + 8 * gg + 4 * hh);
    }
    const int n = l & 31;
    const float ncs = -(tcs[(w * 3 + 2) * 32 + n] + tcs[((w ^ 1) * 3 + 2) * 32 + n]);
#pragma unroll
    for (int s = 0; s < 2; ++s) {
      f32x2 v[4];
#pragma unroll
      for (int j = 0; j < 4; ++j) { const int r = 8 * s + 2 * j; const f32x2 a = {acc[2][r], acc[2][r + 1]}, m2 = {mm[r >> 2][r & 3], mm[r >> 2][(r & 3) + 1]}, r2 = {rr[r >> 2][r & 3], rr[r >> 2][(r & 3) + 1]};
        v[j] = __builtin_elementwise_fma(m2, f32x2{ncs, ncs}, a) * r2; }
      st16<true>(rw, WS_KV + (unsigned)((4 * hg + 2 * g + (n >> 4)) * 64 + tile) * 2048 + 1024 + s * 512 + hh * 256 + (n & 15) * 16,
               pack8(v[0][0], v[0][1], v[1][0], v[1][1], v[2][0], v[2][1], v[3][0], v[3][1]));
    }
#pragma unroll
    for (int t = 1; t >= 0; --t) {
      const float sc = t == 0 ? rstd_l * QSCALE : rstd_l, nm = -mean_l * sc;
#pragma unroll
      for (int s = 0; s < 2; ++s) {
        f32x2 v[4];
#pragma unroll
        for (int j = 0; j < 4; ++j) { const int r = 8 * s + 2 * j; const f32x2 a = {acc[t][r], acc[t][r + 1]}, cc = {cv[t][r >> 2][r & 3], cv[t][r >> 2][(r & 3) + 1]};
          v[j] = __builtin_elementwise_fma(a, f32x2{sc, sc}, cc * nm); }
        const int head = 4 * hg + 2 * g + s;
        st16<true>(rw, (t == 0 ? WS_Q + (unsigned)(head * 64 + tile) * 1024 : WS_KV + (unsigned)(head * 64 + tile) * 2048) + l * 16, pack8(v[0][0], v[0][1], v[1][0], v[1][1], v[2][0], v[2][1], v[3][0], v[3][1]));
        if (t == 1) {
          f32x2 n2 = v[0] * v[0]; n2 = __builtin_elementwise_fma(v[1], v[1], n2); n2 = __builtin_elementwise_fma(v[2], v[2], n2); n2 = __builtin_elementwise_fma(v[3], v[3], n2);
          const float mx = maxx16(max16(sumx32(n2[0] + n2[1])));
          if (l == 0) st4<true>(rw, WS_KMAX + (unsigned)(head * 64 + tile) * 4, __float_as_uint(mx));
        }
      }
    }
  }
  publish(ws, FL_A + (hg * 32 + rb) * 64, c.epoch);
}

DI unsigned wpiece_lane(int l, unsigned row_bytes) { return (unsigned)(l >> 3) * row_bytes + (unsigned)(((l & 7) ^ (((l >> 4) & 1) << 2)) * 16); }
template <int NW, int W0 = 0> DI void prefetch_wo(const Params& p, const Ctx& c) {
  if (c.w < W0 || c.w >= W0 + NW) return;
  const unsigned off = wpiece_lane(c.l, 1024) + (unsigned)(c.b >> 5) * 128u;
  for (int pc = c.w - W0; pc < 64; pc += NW) dma16((const char*)p.wo + (size_t)(8 * pc) * 1024, off, c.lds0 + pc * 1024);
}
DI constexpr unsigned w1_stage_base(int j) { return j < 6 ? 65536 + j * 16384 : (j - 6) * 16384; }
template <int NW, int W0 = 0> DI void prefetch_w1(const Params& p, const Ctx& c, int p0, int p1) {
  if (c.w < W0 || c.w >= W0 + NW) return;
  const unsigned off = wpiece_lane(c.l, F * 2) + (unsigned)d_cb(c.b) * 256u;
  for (int pc = p0 + c.w - W0; pc < p1; pc += NW) {
    const int j = pc >> 4, seg = (pc >> 3) & 1, r8 = pc & 7;
    dma16((const char*)p.w1 + (size_t)(64 * j + 8 * r8) * (F * 2) + seg * 128, off, c.lds0 + w1_stage_base(j) + seg * 8192 + r8 * 1024);
  }
}
template <int NW> DI void prefetch_w2(const Params& p, const Ctx& c, int p0, int p1) {
  if (c.w >= NW) return;
  const unsigned off = wpiece_lane(c.l, 1024) + (unsigned)(c.b >> 5) * 128u;
  for (int pc = p0 + c.w; pc < p1; pc += NW) {
    const int st = pc >> 4, kq = (pc >> 2) & 3, r8 = pc & 3;
    dma16((const char*)p.w2 + (size_t)(448 * kq + 32 * st + 8 * r8) * 1024, off, c.lds0 + ((st + 4) % 10) * 16384 + kq * 4096 + r8 * 1024);
  }
}

DI void warm_first(const Params& p, const Ctx& c) {
  const int h = c.w - 4;
  const unsigned off1 = (unsigned)c.b * 7168u + (unsigned)h * 2048u + (unsigned)c.l * 16u;
  dma16((const char*)p.w1, off1, c.lds0 + 139520);
  dma16((const char*)p.w2, off1, c.lds0 + 139520);
  if (h < 3) { dma16((const char*)p.w1 + 1024, off1, c.lds0 + 139520); dma16((const char*)p.w2 + 1024, off1, c.lds0 + 139520); }
}
DI void warm_w2(const Params& p, const Ctx& c) {
  const int h = c.w - 4;
  const unsigned off = (unsigned)(c.b >> 3) * 57344u + (unsigned)h * 14336u + (unsigned)c.l * 16u;
#pragma unroll
  for (int i = 0; i < 14; ++i) dma16((const char*)p.w2 + i * 1024, off, c.lds0 + 139520);
}

constexpr int B_ONES = 131072;
DI void phaseB(const Params& p, const Ctx& c) {
  char* ws = p.ws;
  LAS char* lds = c.lds;
  const int l = c.l, w = c.w, hh = c.hh, b = c.b, tid = c.tid;
  const int head = 4 * (b & 7) + ((b >> 3) & 3), qb = b >> 5;
  for (int i = tid; i < (4096 + 512) / 16; i += 512) { const unsigned v = (i & 31) == 0 ? ONES2 : 0u; ((LAS u32x4*)(lds + B_ONES))[i] = u32x4{v, v, v, v}; }
  await(ws, FL_A + (head >> 2) * 32 * 64, 32, c.epoch);
  const unsigned l16 = l * 16;
  if (w >= 4) {
    const char* kvg = ws + WS_KV + (size_t)head * 131072;
    const int h = w - 4;
    auto piece = [&](int pc) { dma16_sc1(kvg + pc * 1024, l16, c.lds0 + pc * 1024); };
#pragma unroll
    for (int i = 0; i < 4; ++i) piece(4 * h + i);
#pragma unroll
    for (int i = 0; i < 12; ++i) piece(16 + 4 * i + h);
    wait_bar<12>();
#pragma unroll
    for (int i = 0; i < 16; ++i) piece(64 + 4 * i + h);
    wait_bar<24>();
    wait_bar<16>();
    warm_first(p, c);
    wait_bar<0>();
    if (b < 224) prefetch_w1<4, 4>(p, c, 80, 96);
    lds_bar();
    prefetch_wo<4, 4>(p, c);
    lds_bar();
    if (b < 224) prefetch_w1<4, 4>(p, c, 0, 32);
    warm_w2(p, c);
  } else {
    bf16x8 qa, qb_;
    float kmx;
    gload4<true>(kmx, ws + WS_KMAX + head * 256, l * 4);
    gload16<0, true>(qa, ws + WS_Q + (size_t)(head * 64 + qb * 8 + w) * 1024, l * 16);
    gload16<0, true>(qb_, ws + WS_Q + (size_t)(head * 64 + qb * 8 + w + 4) * 1024, l * 16);
    asm volatile("s_waitcnt vmcnt(0)" : "+v"(qa), "+v"(qb_), "+v"(kmx)::"memory");
    kmx = maxx32(maxx16(max16(kmx)));
    auto qbound = [&](bf16x8 q) { const u32x4 u = __builtin_bit_cast(u32x4, q); f32x2 t = {0.f, 0.f}; for (int i = 0; i < 4; ++i) { const f32x2 v = unpk(u[i]); t = __builtin_elementwise_fma(v, v, t); }
      return __builtin_amdgcn_sqrtf(sumx32(t[0] + t[1]) * kmx) * 1.01f + 1e-6f; };
    const float bna = qbound(qa), bnb = qbound(qb_);
    const bool safe = __all(bna <= BOUND_SAFE && bnb <= BOUND_SAFE);
    f32x16 oa = {}, ob = {};
    const int lo5 = l & 31;
    const bool isv = lo5 < 16;
    const int va0 = isv ? 1024 + hh * 256 + lo5 * 16 : B_ONES + (lo5 == 16 ? 0 : (lo5 >= 20 && lo5 < 28) ? 64 : 16);
    lds_bar();
    if (safe) {
      f32x16 nga, ngb; for (int i = 0; i < 16; ++i) { nga[i] = -bna; ngb[i] = -bnb; }
      f32x16 s0, s1;
      bf16x8 pa, pb = {};
      bf16x8 kf, v0, v1 = {};
      unsigned ka = c.lds0 + l * 16, va = c.lds0 + va0;
      const unsigned vstep = isv ? 4096u : 0u;
      asm volatile("ds_read_b128 %1, %3\n\tds_read_b128 %2, %4\n\ts_waitcnt lgkmcnt(0)\n\t"
                   "v_mfma_f32_32x32x16_bf16 %0, %1, %5, %6\n\ts_nop 7\n\ts_nop 3"
                   : "=&{v[192:207]}"(s0), "=&v"(kf), "=&v"(v0) : "v"(ka), "v"(va), "v"(qa), "v"(nga) : "memory");
#define E_(b, i) "v_exp_f32 v[" #b "+" #i "], v[" #b "+" #i "]\n\t"
#define C_(d, i, b, j, k) "v_cvt_pk_bf16_f32 v[" #d "+" #i "], v[" #b "+" #j "], v[" #b "+" #k "]\n\t"
#define MID_(SC) E_(SC, 4) E_(SC, 5) E_(SC, 6) E_(SC, 7) C_(224, 0, SC, 0, 1) C_(224, 1, SC, 2, 3) E_(SC, 8) E_(SC, 9) C_(224, 2, SC, 4, 5) C_(224, 3, SC, 6, 7) E_(SC, 10) E_(SC, 11)
#define END_(SC) E_(SC, 12) E_(SC, 13) E_(SC, 14) E_(SC, 15) C_(228, 0, SC, 8, 9) C_(228, 1, SC, 10, 11) C_(228, 2, SC, 12, 13) C_(228, 3, SC, 14, 15)
#define UNIT_A(KOFF, V1OFF) \
      "v_mfma_f32_32x32x16_bf16 v[208:223], %[kf], %[qb], %[ngb]\n\t" \
      "ds_read_b128 %[kf], %[ka] offset:" #KOFF "\n\t" \
      E_(192, 0) E_(192, 1) E_(192, 2) E_(192, 3) \
      "v_mfma_f32_32x32x16_bf16 %[ob], %[v1], v[228:231], %[ob]\n\t" \
      "ds_read_b128 %[v1], %[va] offset:" #V1OFF "\n\t" \
      MID_(192) \
      "s_waitcnt lgkmcnt(2)\n\t" \
      "v_mfma_f32_32x32x16_bf16 %[oa], %[v0], v[224:227], %[oa]\n\t" \
      END_(192)
#define UNIT_B(V0OFF) \
      "s_waitcnt lgkmcnt(0)\n\t" \
      "v_mfma_f32_32x32x16_bf16 v[192:207], %[kf], %[qa], %[nga]\n\t" \
      E_(208, 0) E_(208, 1) E_(208, 2) E_(208, 3) \
      "v_mfma_f32_32x32x16_bf16 %[oa], %[v1], v[228:231], %[oa]\n\t" \
      MID_(208) \
      "v_mfma_f32_32x32x16_bf16 %[ob], %[v0], v[224:227], %[ob]\n\t" \
      "ds_read_b128 %[v0], %[va] offset:" #V0OFF "\n\t" \
      END_(208)
#define RUN_GROUPS(NG) do { int cnt_ = (NG); \
      asm volatile("1:\n\t" UNIT_A(2048, 512) UNIT_B(2048) UNIT_A(4096, 2560) UNIT_B(4096) \
        "v_add_u32 %[ka], 0x1000, %[ka]\n\tv_add_u32 %[va], %[va], %[vs]\n\t" \
        "s_sub_u32 %[cnt], %[cnt], 1\n\ts_cmp_lg_u32 %[cnt], 0\n\ts_cbranch_scc1 1b\n\ts_waitcnt lgkmcnt(0)\n\ts_nop 7\n\ts_nop 3" \
        : "+{v[192:207]}"(s0), "=&{v[208:223]}"(s1), "=&{v[224:227]}"(pa), "+{v[228:231]}"(pb), [oa] "+v"(oa), [ob] "+v"(ob), [kf] "+v"(kf), [v0] "+v"(v0), [v1] "+v"(v1), \
          [ka] "+v"(ka), [va] "+v"(va), [cnt] "+s"(cnt_) \
        : [qa] "v"(qa), [qb] "v"(qb_), [nga] "v"(nga), [ngb] "v"(ngb), [vs] "v"(vstep) : "memory", "scc"); } while (0)
      RUN_GROUPS(3);
      lds_bar();
      RUN_GROUPS(4);
      lds_bar();
      RUN_GROUPS(8);
      lds_bar();
      RUN_GROUPS(1);
      lds_bar();
      RUN_GROUPS(8);
      lds_bar();
      RUN_GROUPS(8);
      ob = MFMA(v1, pb, ob);
    } else {
      float ca = -3.0e38f, cb = -3.0e38f;
      const f32x16 zero = {};
      auto online = [&](const bf16x8& kf, const bf16x8& v0, const bf16x8& v1, const bf16x8& q, f32x16& o, float& mrun) {
        f32x16 s = MFMA(kf, q, zero);
        float tm = s[0];
#pragma unroll
        for (int i = 1; i < 16; ++i) tm = fmaxf(tm, s[i]);
        tm = maxx32(tm);
        const float mn = fmaxf(mrun, tm), f = __builtin_amdgcn_exp2f(mrun - mn);
        mrun = mn;
#pragma unroll
        for (int i = 0; i < 16; ++i) { o[i] *= f; s[i] = __builtin_amdgcn_exp2f(s[i] - mn); }
        o = MFMA(v0, pack8(s[0], s[1], s[2], s[3], s[4], s[5], s[6], s[7]), o);
        o = MFMA(v1, pack8(s[8], s[9], s[10], s[11], s[12], s[13], s[14], s[15]), o);
      };
      for (int t = 0; t < 64; ++t) {
        if (t == 6 || t == 14 || t == 30 || t == 32 || t == 48) lds_bar();
        const bf16x8 kf = *(const LAS bf16x8*)(lds + l * 16 + t * 2048);
        const int vo = isv ? va0 + t * 2048 : va0;
        const bf16x8 v0 = *(const LAS bf16x8*)(lds + vo), v1 = *(const LAS bf16x8*)(lds + vo + 512);
        online(kf, v0, v1, qa, oa, ca);
        online(kf, v0, v1, qb_, ob, cb);
      }
    }
    const float ia = __builtin_amdgcn_rcpf(sumx32(oa[8])), ib = __builtin_amdgcn_rcpf(sumx32(ob[8]));
    const __amdgpu_buffer_rsrc_t rw = make_rsrc(ws, WS_USED);
    st16<true>(rw, WS_O + (unsigned)(((qb * 8 + w) * 32 + head) * 64 + l) * 16, pack8(oa[0] * ia, oa[1] * ia, oa[2] * ia, oa[3] * ia, oa[4] * ia, oa[5] * ia, oa[6] * ia, oa[7] * ia));
    st16<true>(rw, WS_O + (unsigned)(((qb * 8 + w + 4) * 32 + head) * 64 + l) * 16, pack8(ob[0] * ib, ob[1] * ib, ob[2] * ib, ob[3] * ib, ob[4] * ib, ob[5] * ib, ob[6] * ib, ob[7] * ib));
  }
  {
    publish(ws, FL_B + (qb * 32 + head) * 64, c.epoch);
    if (b < 224) prefetch_w1<7>(p, c, 32, 80);
  }
}

template <int KQ, int HGAP> DI void quarter_reduce_t(int m, int l, LAS f32x4* xch, const f32x16 (&acc)[2], float (&val)[8]) {
#pragma unroll
  for (int d = 0; d < 4; ++d) {
    if (d == KQ) continue;
    xch[(d >> 1) * HGAP + (((d * 4 + KQ) * 2 + m) * 2 + 0) * 64 + l] = f32x4{acc[d >> 1][8 * (d & 1)], acc[d >> 1][8 * (d & 1) + 1], acc[d >> 1][8 * (d & 1) + 2], acc[d >> 1][8 * (d & 1) + 3]};
    xch[(d >> 1) * HGAP + (((d * 4 + KQ) * 2 + m) * 2 + 1) * 64 + l] = f32x4{acc[d >> 1][8 * (d & 1) + 4], acc[d >> 1][8 * (d & 1) + 5], acc[d >> 1][8 * (d & 1) + 6], acc[d >> 1][8 * (d & 1) + 7]};
  }
  lds_bar();
  f32x4 t[4][2];
#pragma unroll
  for (int s = 0; s < 4; ++s) { if (s == KQ) continue; t[s][0] = xch[(KQ >> 1) * HGAP + (((KQ * 4 + s) * 2 + m) * 2 + 0) * 64 + l]; t[s][1] = xch[(KQ >> 1) * HGAP + (((KQ * 4 + s) * 2 + m) * 2 + 1) * 64 + l]; }
#pragma unroll
  for (int j = 0; j < 8; ++j) val[j] = acc[KQ >> 1][8 * (KQ & 1) + j];
#pragma unroll
  for (int s = 0; s < 4; ++s) { if (s == KQ) continue; for (int j = 0; j < 4; ++j) { val[j] += t[s][0][j]; val[4 + j] += t[s][1][j]; } }
}
template <int HGAP> DI void quarter_reduce(const Ctx& c, LAS f32x4* xch, const f32x16 (&acc)[2], float (&val)[8]) {
  const int m = c.w & 1, kq = c.w >> 1;
  if (kq == 0) quarter_reduce_t<0, HGAP>(m, c.l, xch, acc, val);
  else if (kq == 1) quarter_reduce_t<1, HGAP>(m, c.l, xch, acc, val);
  else if (kq == 2) quarter_reduce_t<2, HGAP>(m, c.l, xch, acc, val);
  else quarter_reduce_t<3, HGAP>(m, c.l, xch, acc, val);
}

DI void phaseC(const Params& p, const Ctx& c, u32x4& hkeep) {
  char* ws = p.ws;
  LAS char* lds = c.lds;
  const int l = c.l, w = c.w, hh = c.hh, b = c.b;
  const int rb = (b & 7) * 4 + ((b >> 3) & 3), cb = b >> 5;
  const int m = w & 1, kq = w >> 1;
  const int rt = 2 * rb + m, kstep = 4 * cb + kq;
  const GAS u32x2* xp = (const GAS u32x2*)((const char*)p.x + (size_t)rt * 32 * 1024 + kstep * 32 + (l & 31) * 1024 + hh * 8);
  const u32x2 xr0 = xp[0], xr1 = xp[2];
  const int q4 = (l & 15) >> 2, p4 = l & 3, blk = (l >> 4) & 1;
  const int wa0 = (128 * kq + 4 * hh + q4) * 128 + (((2 * blk + (p4 >> 1)) ^ (((q4 >> 1) & 1) << 2)) * 16) + (p4 & 1) * 8;
  const int wa1 = wa0 ^ 64;
  bf16x8 wf[8][2];
#pragma unroll
  for (int i = 0; i < 8; ++i) {
    wf[i][0] = cat8(trread(lds + wa0 + i * 2048), trread(lds + wa0 + i * 2048 + 1024));
    wf[i][1] = cat8(trread(lds + wa1 + i * 2048), trread(lds + wa1 + i * 2048 + 1024));
  }
  await(ws, FL_B + (rb >> 2) * 32 * 64, 32, c.epoch);
  const char* abase = ws + WS_O + ((size_t)rt * 32 + kq * 8) * 1024;
  bf16x8 af[8];
  sfor<0, 8>([&](auto I) { constexpr int i = I.value; gload16<(i & 3) * 1024, true>(af[i], abase + (i >> 2) * 4096, l * 16); });
  f32x16 acc[2] = {};
  sfor<0, 8>([&](auto I) {
    constexpr int i = I.value;
    asm volatile("s_waitcnt vmcnt(%1)" : "+v"(af[i]) : "i"(7 - i) : "memory");
    acc[0] = MFMA(wf[i][0], af[i], acc[0]);
    acc[1] = MFMA(wf[i][1], af[i], acc[1]);
    SBAR();
  });
  float val[8];
  quarter_reduce<0>(c, (LAS f32x4*)lds, acc, val);
  const u32x4 o = {cvtpk(val[0] + bf_lo(xr0[0]), val[1] + bf_hi(xr0[0])), cvtpk(val[2] + bf_lo(xr0[1]), val[3] + bf_hi(xr0[1])),
                   cvtpk(val[4] + bf_lo(xr1[0]), val[5] + bf_hi(xr1[0])), cvtpk(val[6] + bf_lo(xr1[1]), val[7] + bf_hi(xr1[1]))};
  hkeep = o;
  const __amdgpu_buffer_rsrc_t rw = make_rsrc(ws, WS_USED);
  st16<true>(rw, WS_HF + (unsigned)((rt * 32 + kstep) * 64 + l) * 16, __builtin_bit_cast(bf16x8, o));
  f32x2 sm = {0.f, 0.f}, sq = {0.f, 0.f};
#pragma unroll
  for (int i = 0; i < 4; ++i) { const f32x2 v = unpk(o[i]); sm += v; sq = __builtin_elementwise_fma(v, v, sq); }
  LAS f32x2* tst = (LAS f32x2*)(lds + 0);
  tst[((m * 4 + kq) * 2 + hh) * 32 + (l & 31)] = f32x2{sm[0] + sm[1], sq[0] + sq[1]};
  lds_bar();
  if (w < 2 && l < 32) {
    f32x2 t8[8];
#pragma unroll
    for (int i = 0; i < 8; ++i) t8[i] = tst[(w * 8 + i) * 32 + l];
    const f32x2 t = ((t8[0] + t8[1]) + (t8[2] + t8[3])) + ((t8[4] + t8[5]) + (t8[6] + t8[7]));
    st8<true>(rw, WS_STAT + (unsigned)(((2 * rb + w) * 8 + cb) * 32 + l) * 8, __builtin_bit_cast(u32x2, t));
  }
  publish(ws, FL_C + ((rb >> 1) * 16 + (rb & 1) * 8 + cb) * 64, c.epoch);
  if (b < 224) prefetch_w1<7>(p, c, 96, 128);
}

constexpr int D_WIN = 6;
DI void phaseD(const Params& p, const Ctx& c) {
  char* ws = p.ws;
  LAS char* lds = c.lds;
  const int l = c.l, w = c.w, hh = c.hh, b = c.b;
  const int rb = d_rb(b), cb = d_cb(b);
  const int m = w & 3, nh = w >> 2;
  const int rt = 4 * rb + m;
  LAS float* tcs = (LAS float*)(lds + 49152);
  LAS float* tb = tcs + 128;
  if (c.tid < 128) tcs[c.tid] = 0.f;
  const bf16x8 bv = *(const GAS bf16x8*)((const char*)p.b1 + cb * 256 + (l & 15) * 16);
  const int par = m >> 1;
  const int q4 = (l & 15) >> 2, p4 = l & 3, blk = (l >> 4) & 1;
  const int wl = nh * 8192 + (4 * hh + q4) * 128 + (((4 * (m & 1) + 2 * blk + (p4 >> 1)) ^ (((q4 >> 1) & 1) << 2)) * 16) + (p4 & 1) * 8;
  const int wa_e = wl + par * 2048, wa_o = wl + (1 - par) * 2048;
  bf16x8 wf[3][2];
  auto load_w = [&](auto I) {
    constexpr int i = I.value;
    constexpr int o = (int)w1_stage_base(i >> 2) + (i & 2) * 2048;
    const int wa = (i & 1) ? wa_o : wa_e;
    wf[i % 3][0] = cat8(trread(lds + wa + o), trread(lds + wa + o + 1024));
    wf[i % 3][1] = cat8(trread(lds + (wa ^ 64) + o), trread(lds + (wa ^ 64) + o + 1024));
  };
  load_w(IC<0>{}); load_w(IC<1>{});
  await(ws, FL_C + rb * 16 * 64, 16, c.epoch);
  const char* abase = ws + WS_HF + (size_t)rt * 32 * 1024;
  const unsigned aoff0 = l * 16 + par * 1024, aoff1 = l * 16 + (1 - par) * 1024;
  bf16x8 af[32];
  auto load_a = [&](auto I) { constexpr int i = I.value;
    if constexpr (i & 1) gload16<((i >> 1) & 1) * 2048, true>(af[i], abase + (i >> 2) * 4096, aoff1);
    else gload16<((i >> 1) & 1) * 2048, true>(af[i], abase + (i >> 2) * 4096, aoff0); };
  sfor<0, 4>(load_a);
  u32x2 st[4];
  sfor<0, 4>([&](auto I) { constexpr int i = I.value; gload8<i * 256, true>(st[i], ws + WS_STAT + (size_t)rt * 2048, (l & 31) * 8 + hh * 1024); });
  sfor<4, D_WIN>(load_a);
  f32x16 acc[2] = {};
  f32x4 cs = {};
  const bf16x8 sel = rowsum_sel(l);
  sfor<0, 32>([&](auto I) {
    constexpr int i = I.value;
    if constexpr (i + D_WIN < 32) load_a(IC<i + D_WIN>{});
    constexpr int issued = i + D_WIN < 32 ? i + D_WIN + 1 : 32;
    asm volatile("s_waitcnt vmcnt(%1)" : "+v"(af[i]) : "i"(issued - i - 1 + (i < 4 ? 4 : 0)) : "memory");
    if constexpr (i == 20) asm volatile("s_barrier" ::: "memory");
    acc[0] = MFMA(wf[i % 3][0], af[i], acc[0]);
    acc[1] = MFMA(wf[i % 3][1], af[i], acc[1]);
    if constexpr ((i & 1) == 0) cs = MFMA16(wf[i % 3][0], sel, cs);
    if constexpr (i + 2 < 32) { SBAR(); load_w(IC<i + 2>{}); }
    SBAR();
  });
  if ((l & 15) < 2) {
    LAS float* d = tcs + nh * 64 + (m & 1) * 32 + 16 * (l & 15) + 4 * (l >> 4);
#pragma unroll
    for (int j = 0; j < 4; ++j) __hip_atomic_fetch_add(d + j, cs[j], __ATOMIC_RELAXED, __HIP_MEMORY_SCOPE_WORKGROUP);
  }
  if (w == 0 && l < 16) { const u32x4 u = __builtin_bit_cast(u32x4, bv);
    *(LAS f32x4*)(tb + l * 8) = f32x4{bf_lo(u[0]), bf_hi(u[0]), bf_lo(u[1]), bf_hi(u[1])}; *(LAS f32x4*)(tb + l * 8 + 4) = f32x4{bf_lo(u[2]), bf_hi(u[2]), bf_lo(u[3]), bf_hi(u[3])}; }
  asm volatile("" : "+v"(st[0]), "+v"(st[1]), "+v"(st[2]), "+v"(st[3]));
  const f32x2 ss = __builtin_bit_cast(f32x2, st[0]) + __builtin_bit_cast(f32x2, st[1]) + __builtin_bit_cast(f32x2, st[2]) + __builtin_bit_cast(f32x2, st[3]);
  const float sm = sumx32(ss[0]), sq = sumx32(ss[1]);
  const float mean = sm * (1.f / D), rstd = __builtin_amdgcn_rsqf(fmaxf(sq * (1.f / D) - mean * mean, 0.f) + LN_EPS);
  const float nmr = -mean * rstd;
  lds_bar();
  const __amdgpu_buffer_rsrc_t rw = make_rsrc(ws, WS_USED);
#pragma unroll
  for (int t = 0; t < 2; ++t) {
    const int ct = 2 * nh + ((m & 1) ^ t);
    f32x4 cv[4], bb[4];
#pragma unroll
    for (int g = 0; g < 4; ++g) { cv[g] = *(const LAS f32x4*)(tcs + 32 * ct + 8 * g + 4 * hh); bb[g] = *(const LAS f32x4*)(tb + 32 * ct + 8 * g + 4 * hh); }
#pragma unroll
    for (int s = 0; s < 2; ++s) {
      unsigned pk[4];
#pragma unroll
      for (int j = 0; j < 4; ++j) {
        const int r = 8 * s + 2 * j;
        const f32x2 a = {acc[t][r], acc[t][r + 1]}, c2 = {cv[r >> 2][r & 3], cv[r >> 2][(r & 3) + 1]}, b2 = {bb[r >> 2][r & 3], bb[r >> 2][(r & 3) + 1]};
        const f32x2 y = __builtin_elementwise_fma(a, f32x2{rstd, rstd}, __builtin_elementwise_fma(c2, f32x2{nmr, nmr}, b2));
        const f32x2 z = y * LOG2E;
        const f32x2 u = {__builtin_amdgcn_exp2f(z[0]), __builtin_amdgcn_exp2f(z[1])};
        const f32x2 d = __builtin_elementwise_fma(u, u + 2.f, f32x2{2.f, 2.f});
        const f32x2 r2 = {__builtin_amdgcn_rcpf(d[0]), __builtin_amdgcn_rcpf(d[1])};
        const f32x2 th = __builtin_elementwise_fma(r2, f32x2{-2.f, -2.f}, f32x2{1.f, 1.f});
        const f32x2 o = y * th;
        pk[j] = cvtpk(o[0], o[1]);
      }
      st16<true>(rw, WS_ACT + (unsigned)((rt * 112 + cb * 8 + ct * 2 + s) * 64 + l) * 16, __builtin_bit_cast(bf16x8, u32x4{pk[0], pk[1], pk[2], pk[3]}));
      prefetch_w2<8>(p, c, 24 * (2 * t + s), 24 * (2 * t + s) + 24);
    }
  }
  publish(ws, FL_D + (rb * 16 + cb) * 64, c.epoch);
}

constexpr int E_NSLOT = 10, E_NST = 14, E_WIN = 6, E_PRE = 8;
struct ESched {
  int posA[28], posW[E_NST], n_top[E_NST], n_mid[E_NST];
  constexpr ESched() : posA(), posW(), n_top(), n_mid() {
    int n = 0;
    for (int i = 0; i < E_WIN; ++i) posA[i] = n++;
    for (int st = 0; st < E_NST; ++st) {
      n_top[st] = n;
      if (E_PRE + st < E_NST) { n += 2; posW[E_PRE + st] = n - 1; }
      for (int j = 0; j < 2; ++j) if (E_WIN + 2 * st + j < 28) posA[E_WIN + 2 * st + j] = n++;
      n_mid[st] = n;
    }
  }
};
DI void phaseE(const Params& p, const Ctx& c, u32x4 hkeep) {
  constexpr ESched sch{};
  char* ws = p.ws;
  LAS char* lds = c.lds;
  const int l = c.l, w = c.w, hh = c.hh, b = c.b;
  const int rb = (b & 7) * 4 + ((b >> 3) & 3), cb = b >> 5;
  const int m = w & 1, kq = w >> 1;
  const int rt = 2 * rb + m, kstep = 4 * cb + kq;
  const bool early = b < 224;
  if (early) prefetch_w2<7>(p, c, 16 * 6, 16 * 8); else prefetch_w2<7>(p, c, 0, 16 * E_PRE);
  bf16x8 resv = __builtin_bit_cast(bf16x8, hkeep);
  const int q4 = (l & 15) >> 2, p4 = l & 3, blk = (l >> 4) & 1;
  const int wa0 = kq * 4096 + (4 * hh + q4) * 128 + (((2 * blk + (p4 >> 1)) ^ (((q4 >> 1) & 1) << 2)) * 16) + (p4 & 1) * 8;
  const int wa1 = wa0 ^ 64;
  bf16x8 wf[3][2][2];
  auto load_w = [&](auto ST) {
    constexpr int st = ST.value;
    const LAS char* sl = lds + ((st + 4) % E_NSLOT) * 16384;
#pragma unroll
    for (int i = 0; i < 2; ++i) {
      wf[st % 3][i][0] = cat8(trread(sl + wa0 + i * 2048), trread(sl + wa0 + i * 2048 + 1024));
      wf[st % 3][i][1] = cat8(trread(sl + wa1 + i * 2048), trread(sl + wa1 + i * 2048 + 1024));
    }
  };
  if (!early) wait_bar<0>();
  load_w(IC<0>{}); load_w(IC<1>{});
  await(ws, FL_D + (rb >> 1) * 16 * 64, 14, c.epoch);
  const char* abase = ws + WS_ACT + ((size_t)rt * 112 + kq * 28) * 1024;
  bf16x8 af[28];
  auto load_a = [&](auto I) { constexpr int i = I.value; gload16<(i & 3) * 1024, true>(af[i], abase + (i >> 2) * 4096, l * 16); };
  sfor<0, E_WIN>(load_a);
  f32x16 acc[2] = {};
  sfor<0, E_NST>([&](auto ST) {
    constexpr int st = ST.value;
    if constexpr (st == 2) asm volatile("s_waitcnt lgkmcnt(0)\n\ts_barrier" ::: "memory");
    if constexpr (st == 6 || st == 8 || st == 10) asm volatile("s_waitcnt vmcnt(%0) lgkmcnt(0)\n\ts_barrier" ::"i"(sch.n_top[st] - sch.posW[st + 3] - 1) : "memory");
    if constexpr (E_PRE + st < E_NST) prefetch_w2<8>(p, c, 16 * (st + E_PRE), 16 * (st + E_PRE + 1));
    if constexpr (E_WIN + 2 * st < 28) load_a(IC<E_WIN + 2 * st>{});
    if constexpr (E_WIN + 2 * st + 1 < 28) load_a(IC<E_WIN + 2 * st + 1>{});
    asm volatile("s_waitcnt vmcnt(%2)" : "+v"(af[2 * st]), "+v"(af[2 * st + 1]) : "i"(sch.n_mid[st] - sch.posA[2 * st + 1] - 1) : "memory");
    acc[0] = MFMA(wf[st % 3][0][0], af[2 * st], acc[0]);
    acc[1] = MFMA(wf[st % 3][0][1], af[2 * st], acc[1]);
    if constexpr (st + 2 < E_NST) { SBAR(); load_w(IC<st + 2>{}); SBAR(); }
    acc[0] = MFMA(wf[st % 3][1][0], af[2 * st + 1], acc[0]);
    acc[1] = MFMA(wf[st % 3][1][1], af[2 * st + 1], acc[1]);
    SBAR();
  });
  { asm volatile("" : "+v"(resv));
    const u32x4 ru = __builtin_bit_cast(u32x4, resv);
#pragma unroll
    for (int k = 0; k < 4; ++k) { const float on = kq == k ? 1.f : 0.f;
#pragma unroll
      for (int j = 0; j < 4; ++j) { acc[k >> 1][8 * (k & 1) + 2 * j] += on * bf_lo(ru[j]); acc[k >> 1][8 * (k & 1) + 2 * j + 1] += on * bf_hi(ru[j]); } } }
  constexpr int PS = 272, PQ = 64 * PS;
  LAS char* xa = lds;
#pragma unroll
  for (int ct = 0; ct < 2; ++ct)
#pragma unroll
    for (int g = 0; g < 4; ++g)
      *(LAS f32x4*)(xa + kq * PQ + (32 * m + (l & 31)) * PS + (32 * ct + 8 * g + 4 * hh) * 4) = f32x4{acc[ct][4 * g], acc[ct][4 * g + 1], acc[ct][4 * g + 2], acc[ct][4 * g + 3]};
  lds_bar();
  { const int row = c.tid >> 3, c8 = c.tid & 7;
    f32x4 t0[4], t1[4];
#pragma unroll
    for (int q = 0; q < 4; ++q) { t0[q] = *(const LAS f32x4*)(xa + q * PQ + row * PS + c8 * 32); t1[q] = *(const LAS f32x4*)(xa + q * PQ + row * PS + c8 * 32 + 16); }
    const f32x4 s0 = (t0[0] + t0[1]) + (t0[2] + t0[3]), s1 = (t1[0] + t1[1]) + (t1[2] + t1[3]);
    const __amdgpu_buffer_rsrc_t ro = make_rsrc(p.out, S * D * 2);
    st16<false>(ro, (unsigned)(rb * 64 + row) * 1024u + cb * 128 + c8 * 16, pack8(s0[0], s0[1], s0[2], s0[3], s1[0], s1[1], s1[2], s1[3])); }
}

DI Ctx make_ctx(char* lds_g) {
  Ctx c;
  c.lds = (LAS char*)lds_g; c.lds0 = (unsigned)(uintptr_t)lds_g;
  c.tid = threadIdx.x; c.l = c.tid & 63; c.w = rfl(c.tid >> 6); c.hh = c.l >> 5; c.b = blockIdx.x; c.epoch = 0;
  return c;
}

__global__ __launch_bounds__(512, 2) void block_mega(Params p) {
  extern __shared__ __attribute__((aligned(16))) char lds_g[];
  Ctx c = make_ctx(lds_g);
  c.epoch = __hip_atomic_load((GAS unsigned*)(p.ws + WS_FLAG) + (threadIdx.x >> 12), __ATOMIC_RELAXED, __HIP_MEMORY_SCOPE_AGENT);
  phaseA(p, c);
  phaseB(p, c);
  u32x4 hkeep;
  phaseC(p, c, hkeep);
  if (c.b < 224) phaseD(p, c);
  phaseE(p, c, hkeep);
  if (c.b == 0 && c.tid == 0) __hip_atomic_store((GAS unsigned*)(p.ws + WS_FLAG), c.epoch, __ATOMIC_RELAXED, __HIP_MEMORY_SCOPE_AGENT);
  asm volatile("mega_code_end:");
}

extern "C" void kernel_launch(void* const* d_in, const int* in_sizes, int n_in, void* d_out, int out_size, void* d_ws, size_t ws_size, hipStream_t stream) {
  Params p;
  p.x = (const u16*)d_in[0]; p.wqkv = (const u16*)d_in[1]; p.wo = (const u16*)d_in[2];
  p.w1 = (const u16*)d_in[4]; p.b1 = (const u16*)d_in[5]; p.w2 = (const u16*)d_in[6];
  p.ws = (char*)d_ws; p.out = (u16*)d_out;
  (void)hipFuncSetAttribute((const void*)block_mega, hipFuncAttributeMaxDynamicSharedMemorySize, LDS_BYTES);
  block_mega<<<256, 512, LDS_BYTES, stream>>>(p);
}
```
